```python
import torch

def _softmax(x, axis=-1):
    return torch.softmax(x, dim=axis)
def _gelu(x):
    return torch.nn.functional.gelu(x, approximate="tanh")
def _silu(x):
    return torch.nn.functional.silu(x)
def _sigmoid(x):
    return torch.sigmoid(x)
def _layernorm(x, axis=-1, eps=1e-5):
    m = x.mean(dim=axis, keepdim=True)
    v = x.var(dim=axis, keepdim=True, unbiased=False)
    return (x - m) / torch.sqrt(v + eps)
def _scatter_add(tgt, idx, src, axis=0):
    return tgt.clone().index_add_(axis, idx, src)
def _segment_sum(src, seg, n):
    out = torch.zeros((n,) + src.shape[1:], dtype=src.dtype)
    return out.index_add_(0, seg, src)
def _pad(x, pw):
    flat = tuple(v for pair in reversed(pw) for v in pair)
    return torch.nn.functional.pad(x, flat)

kernel_name = 'interleave_layers__spectral_fnet__glu_gated_glu__prompt'

def setup_inputs():

    _w = (lambda a: (lambda t: t.to(torch.bfloat16) if t.is_floating_point() else t)(torch.as_tensor(a)))
    def _A_setup():
        B, S, D = 1, 2048, 512
        ns = torch.arange(S)[:, None] * torch.arange(S)[None, :]
        nd = torch.arange(D)[:, None] * torch.arange(D)[None, :]
        Ws_re = torch.cos(-2.0 * torch.pi * ns / S).to(float)
        Ws_im = torch.sin(-2.0 * torch.pi * ns / S).to(float)
        Wd_re = torch.cos(-2.0 * torch.pi * nd / D).to(float)
        Wd_im = torch.sin(-2.0 * torch.pi * nd / D).to(float)
        return {
            "x": _w((lambda *s: torch.randn(*s, dtype=torch.bfloat16) * 0.05)(B, S, D)),
            "dft_seq_re": _w(Ws_re),
            "dft_seq_im": _w(Ws_im),
            "dft_hid_re": _w(Wd_re),
            "dft_hid_im": _w(Wd_im),
        }
    _A_in = _A_setup()

    def _B_setup():
        B, S, D, F = 1, 2048, 512, 1792
        return {
            "x": _w((lambda *s: torch.randn(*s, dtype=torch.bfloat16) * 0.05)(B, S, D)),
            "w_gate": _w((lambda *s: torch.randn(*s, dtype=torch.bfloat16) * 1.6)(D, F)),
            "w_up": _w((lambda *s: torch.randn(*s, dtype=torch.bfloat16) * 0.8)(D, F)),
            "w_down": _w((lambda *s: torch.randn(*s, dtype=torch.bfloat16) * 0.8)(F, D)),
        }
    _B_in = _B_setup()

    return {
        "A_x": _A_in["x"],
        "A_dft_seq_re": _A_in["dft_seq_re"],
        "A_dft_seq_im": _A_in["dft_seq_im"],
        "A_dft_hid_re": _A_in["dft_hid_re"],
        "A_dft_hid_im": _A_in["dft_hid_im"],
        "B_x": _B_in["x"],
        "B_w_gate": _B_in["w_gate"],
        "B_w_up": _B_in["w_up"],
        "B_w_down": _B_in["w_down"],
    }

def reference(A_x, A_dft_seq_re, A_dft_seq_im, A_dft_hid_re, A_dft_hid_im, B_x, B_w_gate, B_w_up, B_w_down):
    def _A_ref(x, dft_seq_re, dft_seq_im, dft_hid_re, dft_hid_im):
        a = torch.einsum("bsd,ed->bse", x, dft_hid_re)
        b = torch.einsum("bsd,ed->bse", x, dft_hid_im)
        y = torch.einsum("ts,bse->bte", dft_seq_re, a) - torch.einsum("ts,bse->bte", dft_seq_im, b)
        return y

    def _B_ref(x, w_gate, w_up, w_down):
        g = torch.einsum("bsd,df->bsf", x, w_gate)
        u = torch.einsum("bsd,df->bsf", x, w_up)
        h = _sigmoid(g) * u
        return torch.einsum("bsf,fd->bsd", h, w_down)

    x = A_x
    x = x + _A_ref(_layernorm(x, axis=-1), A_dft_seq_re, A_dft_seq_im, A_dft_hid_re, A_dft_hid_im)
    x = x + _B_ref(_layernorm(x, axis=-1), B_w_gate, B_w_up, B_w_down)
    x = x + _A_ref(_layernorm(x, axis=-1), A_dft_seq_re, A_dft_seq_im, A_dft_hid_re, A_dft_hid_im)
    x = x + _B_ref(_layernorm(x, axis=-1), B_w_gate, B_w_up, B_w_down)
    return x
```

```cpp
#include <hip/hip_runtime.h>
#include <hip/hip_bf16.h>
#include <stdint.h>
#define REASSOC 0
typedef unsigned short u16;
__device__ __forceinline__ float bf2f(u16 v){ return __uint_as_float(((unsigned)v)<<16); }
__device__ __forceinline__ u16 f2bf(float f){ unsigned u=__float_as_uint(f); u = (u + 0x7FFF + ((u>>16)&1))>>16; return (u16)u; }

__global__ void gemm(const u16* A, int lda, const u16* B, int sbk, int sbn, const u16* A2, const u16* B2, float sign2,
                     u16* C, int ldc, const u16* R, int M, int N, int K, float sgn){
  __shared__ float As[16][65], Bs[16][65];
  int tx=threadIdx.x&15, ty=threadIdx.x>>4; int bm=blockIdx.y*64, bn=blockIdx.x*64;
  float acc[4][4]={};
  for(int pass=0; pass<(A2?2:1); ++pass){
    const u16* a_ = pass?A2:A; const u16* b_ = pass?B2:B; float s = pass?sign2:1.f;
    for(int k0=0;k0<K;k0+=16){
      for(int i=threadIdx.x;i<1024;i+=256){ int r=i>>4,k=i&15; As[k][r]=s*bf2f(a_[(size_t)(bm+r)*lda+k0+k]); }
      for(int i=threadIdx.x;i<1024;i+=256){ int r,k; if(sbk==1){r=i>>4;k=i&15;} else {r=i&63;k=i>>6;} Bs[k][r]=bf2f(b_[(size_t)(k0+k)*sbk+(size_t)(bn+r)*sbn]); }
      __syncthreads();
      for(int k=0;k<16;++k){ float a[4],b[4];
        for(int i=0;i<4;++i){a[i]=As[k][ty*4+i]; b[i]=Bs[k][tx*4+i];}
        for(int i=0;i<4;++i)for(int j=0;j<4;++j)acc[i][j]+=a[i]*b[j]; }
      __syncthreads();
    }
  }
  for(int i=0;i<4;++i)for(int j=0;j<4;++j){ size_t o=(size_t)(bm+ty*4+i)*ldc+bn+tx*4+j; float v=acc[i][j]*sgn; if(R) v+=bf2f(R[o]); C[o]=f2bf(v);}
}
__global__ void ln(const u16* x, u16* y){
  int r=blockIdx.x; __shared__ float s1[256], s2[256];
  float a=bf2f(x[r*512+threadIdx.x]), b=bf2f(x[r*512+256+threadIdx.x]);
  s1[threadIdx.x]=a+b; __syncthreads();
  for(int o=128;o;o>>=1){ if(threadIdx.x<o) s1[threadIdx.x]+=s1[threadIdx.x+o]; __syncthreads(); }
  float m=s1[0]/512; s2[threadIdx.x]=(a-m)*(a-m)+(b-m)*(b-m); __syncthreads();
  for(int o=128;o;o>>=1){ if(threadIdx.x<o) s2[threadIdx.x]+=s2[threadIdx.x+o]; __syncthreads(); }
  float rs=1.f/sqrtf(s2[0]/512+1e-5f);
  y[r*512+threadIdx.x]=f2bf((a-m)*rs); y[r*512+256+threadIdx.x]=f2bf((b-m)*rs);
}
__global__ void glu(const u16* g,const u16* u,u16* h,int n){ int i=blockIdx.x*256+threadIdx.x; if(i<n){ float gv=bf2f(g[i]); h[i]=f2bf(bf2f(u[i])/(1.f+__expf(-gv))); } }

extern "C" void kernel_launch(void* const* d_in, const int* in_sizes, int n_in, void* d_out, int out_size, void* d_ws, size_t ws_size, hipStream_t stream) {
  const u16* x0=(const u16*)d_in[0]; const u16* Sre=(const u16*)d_in[1]; const u16* Sim=(const u16*)d_in[2];
  const u16* Dre=(const u16*)d_in[3]; const u16* Dim=(const u16*)d_in[4];
  const u16* Wg=(const u16*)d_in[6]; const u16* Wu=(const u16*)d_in[7]; const u16* Wd=(const u16*)d_in[8];
  u16* ws=(u16*)d_ws; const size_t T=2048*512; u16* xn=ws; u16* P=ws+T; u16* Q=ws+2*T; u16* xa=ws+3*T; u16* xb=ws+4*T; u16* g=ws+5*T; u16* u=g+2048*1792; u16* h=u+2048*1792;
  const u16* x=x0;
  for(int l=0;l<2;++l){
    ln<<<2048,256,0,stream>>>(x,xn);
    gemm<<<dim3(8,32),256,0,stream>>>(xn,512,Dre,1,512,nullptr,nullptr,0.f,P,512,nullptr,2048,512,512,1.f);
    gemm<<<dim3(8,32),256,0,stream>>>(xn,512,Dim,1,512,nullptr,nullptr,0.f,Q,512,nullptr,2048,512,512,1.f);
    gemm<<<dim3(8,32),256,0,stream>>>(Sre,2048,P,512,1,Sim,Q,-1.f,xa,512,x,2048,512,2048,1.f);
    ln<<<2048,256,0,stream>>>(xa,xn);
    gemm<<<dim3(28,32),256,0,stream>>>(xn,512,Wg,1792,1,nullptr,nullptr,0.f,g,1792,nullptr,2048,1792,512,1.f);
    gemm<<<dim3(28,32),256,0,stream>>>(xn,512,Wu,1792,1,nullptr,nullptr,0.f,u,1792,nullptr,2048,1792,512,1.f);
    glu<<<2048*1792/256,256,0,stream>>>(g,u,h,2048*1792);
    u16* o = l==1 ? (u16*)d_out : xb;
    gemm<<<dim3(8,32),256,0,stream>>>(h,1792,Wd,512,1,nullptr,nullptr,0.f,o,512,xa,2048,512,1792,1.f);
    x=xb;
  }
}
```

```cpp
#include <hip/hip_runtime.h>
#include <stdint.h>

namespace {
typedef unsigned short u16;
typedef short s16x4 __attribute__((ext_vector_type(4)));
typedef short s16x8 __attribute__((ext_vector_type(8)));
typedef __bf16 bf16x8 __attribute__((ext_vector_type(8)));
typedef __bf16 bf16x2 __attribute__((ext_vector_type(2)));
typedef float f32x2 __attribute__((ext_vector_type(2)));
typedef float f32x4 __attribute__((ext_vector_type(4)));
typedef float f32x16 __attribute__((ext_vector_type(16)));
typedef unsigned u32x4 __attribute__((ext_vector_type(4)));
typedef unsigned u32x2 __attribute__((ext_vector_type(2)));
#define LAS __attribute__((address_space(3)))
typedef __amdgpu_buffer_rsrc_t rsrc_t;
typedef LAS char* lds_t;

constexpr int SEQ = 2048, DM = 512, FF = 1792;
constexpr int LDS_BYTES = 163840;

__device__ __forceinline__ rsrc_t mk_rsrc(const void* p, unsigned bytes) { return __builtin_amdgcn_make_buffer_rsrc((void*)p, 0, bytes, 0x00020000); }
template <int AUX> __device__ __forceinline__ void dma16(rsrc_t rs, lds_t dst, unsigned voff, unsigned soff) {
  __builtin_amdgcn_raw_ptr_buffer_load_lds(rs, (LAS void*)dst, 16, voff, soff, 0, AUX);
}
#define WAIT_V(n) asm volatile("s_waitcnt vmcnt(%0)" ::"n"(n) : "memory")
#define SCHED() __builtin_amdgcn_sched_barrier(0)
#define WAIT_L0() __builtin_amdgcn_s_waitcnt(0xC07F)
__device__ __forceinline__ void bar() { asm volatile("" ::: "memory"); __builtin_amdgcn_s_barrier(); asm volatile("" ::: "memory"); }
__device__ __forceinline__ void cbar() { WAIT_L0(); bar(); }

template <int I> struct IC { static constexpr int v = I; };
template <int A, int B, class F> __device__ __forceinline__ void static_for(F&& f) {
  if constexpr (A < B) { f(IC<A>{}); static_for<A + 1, B>(f); }
}

template <int NST_, int NX_, int NW_, int nX, int nW> struct Sched {
  static constexpr int NST = NST_, NX = NX_, NW = NW_;
  static constexpr int waitN(int s) {
    int total = 0, last = 0;
    for (int j = 0; j < NW && j < NST; ++j) { total += nW; if (j == s) last = total; }
    for (int j = 0; j < NX && j < NST; ++j) { total += nX; if (j == s) last = total; }
    for (int j = 1; j < s; ++j) {
      if (j - 1 + NX < NST) { total += nX; if (j - 1 + NX == s) last = total; }
      if (j - 1 + NW < NST) { total += nW; if (j - 1 + NW == s && total > last) last = total; }
    }
    int n = total - last;
    return n > 63 ? 63 : n;
  }
};
#define THR 63
template <class SC, class FW> __device__ __forceinline__ void l_prefetch_w(FW issueW) {
  static_for<0, (SC::NW < SC::NST ? SC::NW : SC::NST)>([&](auto j) { issueW(j, IC<j.v % SC::NW>{}, IC<THR>{}); });
}
template <class SC, class FX, class FW> __device__ __forceinline__ void l_run(FX issueX, FW issueW) {
  static_for<0, (SC::NX < SC::NST ? SC::NX : SC::NST)>([&](auto j) { issueX(j, IC<j.v % SC::NX>{}); });
  static_for<0, SC::NST>([&](auto s) {
    constexpr int S = s.v;
    WAIT_V(SC::waitN(S));
    bar();
    if constexpr (S >= 1 && S - 1 + SC::NX < SC::NST) issueX(IC<S - 1 + SC::NX>{}, IC<(S - 1) % SC::NX>{});
    if constexpr (S >= 1 && S - 1 + SC::NW < SC::NST) issueW(IC<S - 1 + SC::NW>{}, IC<(S - 1) % SC::NW>{}, IC<63>{});
  });
  bar();
}

__device__ __forceinline__ bf16x8 ld_b128(lds_t p) { return *(LAS bf16x8*)p; }
__device__ __forceinline__ bf16x8 ld_tr(lds_t p0, lds_t p1) {
  s16x4 a = __builtin_amdgcn_ds_read_tr16_b64_v4i16((LAS s16x4*)p0);
  s16x4 b = __builtin_amdgcn_ds_read_tr16_b64_v4i16((LAS s16x4*)p1);
  s16x8 f = __builtin_shufflevector(a, b, 0, 1, 2, 3, 4, 5, 6, 7);
  return __builtin_bit_cast(bf16x8, f);
}
__device__ __forceinline__ f32x16 mfma(bf16x8 a, bf16x8 b, f32x16 c) { return __builtin_amdgcn_mfma_f32_32x32x16_bf16(a, b, c, 0, 0, 0); }
__device__ __forceinline__ unsigned pk2(float a, float b) { f32x2 v = {a, b}; return __builtin_bit_cast(unsigned, __builtin_convertvector(v, bf16x2)); }
__device__ __forceinline__ float bflo(unsigned u) { return __uint_as_float(u << 16); }
__device__ __forceinline__ float bfhi(unsigned u) { return __uint_as_float(u & 0xffff0000u); }
__device__ __forceinline__ float xhalf_sum(float v) {
  auto r = __builtin_amdgcn_permlane32_swap(__float_as_uint(v), __float_as_uint(v), false, false);
  return __uint_as_float(r[0]) + __uint_as_float(r[1]);
}
__device__ __forceinline__ unsigned km_read_base(int row, int h) { return row * 128 + ((h ^ ((row >> 1) & 7)) << 4); }
__device__ __forceinline__ int perm23(int i) { return (i & ~12) | ((i & 4) << 1) | ((i & 8) >> 1); }
__device__ __forceinline__ unsigned km_src(int lw, int lane, int pitch) {
  return (8 * lw + (lane >> 3)) * pitch + (((lane & 7) ^ (((lw & 1) << 2) | (lane >> 4))) << 4);
}
__device__ __forceinline__ unsigned kn_src(int lw, int lane, int pitch) {
  return (8 * lw + (lane >> 3)) * pitch + (((lane & 7) ^ (((lane >> 4) & 1) << 2)) << 4);
}
__device__ __forceinline__ unsigned kn_read_base(int lane, int ft) {
  int h = lane >> 5, blk = (lane >> 4) & 1, q = (lane & 15) >> 2, p = lane & 3, pp = ((p & 1) << 1) | (p >> 1);
  int c = 4 * ft + 2 * blk + (pp >> 1);
  return (8 * h + q) * 128 + ((c ^ (((q >> 1) & 1) << 2)) << 4) + 8 * (pp & 1);
}

#define STAMP(i)
#define LSTAMP(ph, i)
#define STAMP_ARG
#define STAMP_PASS(ph)

typedef __attribute__((address_space(1))) unsigned gu32;
#define RLX_AGENT __ATOMIC_RELAXED, __HIP_MEMORY_SCOPE_AGENT
constexpr int SC1 = 16;
constexpr unsigned SPIN_CAP = 1u << 18;

struct Ptrs {
  unsigned long long* stamps;
  const u16 *x0, *sre, *sim, *dre, *dim, *wg, *wu, *wd;
  u16 *out, *xn, *pq, *xa, *hh;
  float* stats;
  unsigned *csf, *gran, *mbox, *tmo;
};
constexpr size_t XN_L = (size_t)SEQ * DM, PQ_L = (size_t)SEQ * 1024, HH_L = (size_t)SEQ * FF, ST_L = (size_t)16 * SEQ * 2;

#define SLP 1
__device__ __forceinline__ void signal(unsigned* mbox, int seam, int consumer, int slot, bool active) {
  if (active) __hip_atomic_store((gu32*)mbox + ((size_t)seam * 256 + consumer) * 32 + slot, 1u, RLX_AGENT);
}
__device__ __forceinline__ void await(unsigned* mbox, unsigned* tmo, int seam, int me, int nprod, int lane) {
  gu32* q = (gu32*)mbox + ((size_t)seam * 256 + me) * 32 + (lane < nprod ? lane : 0);
  for (unsigned spins = 0;; ++spins) {
    unsigned x = __hip_atomic_load(q, RLX_AGENT);
    if (__all(x == 1u)) break;
    if (spins > SPIN_CAP) { if (lane == 0) __hip_atomic_store((gu32*)tmo, 1u, RLX_AGENT); break; }
    if (SLP) __builtin_amdgcn_s_sleep(SLP);
  }
}
#define MODE 1
template <class LD> __device__ __forceinline__ void l_seam(LD& L) {
  if (MODE == 0) { L.prefetch(); bar(); bar(); }
  if (MODE == 1) { bar(); L.prefetch(); bar(); }
  if (MODE == 2) { bar(); bar(); L.prefetch(); }
}
__device__ __forceinline__ void drain_and_join() { WAIT_V(0); bar(); }
__device__ __forceinline__ int fresh_v(int x) { asm volatile("" : "+v"(x)); return x; }
__device__ __forceinline__ int fresh_s(int x) { asm volatile("" : "+s"(x)); return x; }

using SchedP = Sched<32, 6, 7, 2, 4>;
constexpr int P_WB = 0, P_XB = 7 * 16384;
struct PLoader {
  rsrc_t rre, rim; unsigned vw, vx; lds_t lds; int lw;
  __device__ __forceinline__ PLoader(lds_t lds_, const Ptrs& p, int tb, int lw_, int lane) : lds(lds_), lw(lw_) {
    rre = mk_rsrc(p.sre + (size_t)tb * 64 * SEQ, 64 * SEQ * 2); rim = mk_rsrc(p.sim + (size_t)tb * 64 * SEQ, 64 * SEQ * 2);
    vw = km_src(lw, lane, SEQ * 2); vx = kn_src(lw, lane, DM * 2);
  }
  template <class S, class T, class H> __device__ __forceinline__ void W(S s, T slot, H thr) {
    static_for<0, 4>([&](auto n) { dma16<0>(n.v < 2 ? rre : rim, lds + P_WB + slot.v * 16384 + (lw + 4 * n.v) * 1024, vw, s.v * 128 + (n.v & 1) * 32 * SEQ * 2); if constexpr (thr.v < 63) WAIT_V(thr.v); });
  }
  __device__ __forceinline__ void prefetch() { l_prefetch_w<SchedP>([&](auto s, auto t, auto h) { W(s, t, h); }); }
  __device__ __forceinline__ void run(const u16* xn, int et) {
    const rsrc_t rx = mk_rsrc(xn + et * 64, SEQ * DM * 2 - et * 128);
    l_run<SchedP>([&](auto s, auto slot) {
      static_for<0, 2>([&](auto n) { dma16<SC1>(rx, lds + P_XB + slot.v * 8192 + (lw + 4 * n.v) * 1024, vx, (s.v * 64 + 32 * n.v) * DM * 2); });
    }, [&](auto s, auto t, auto h) { W(s, t, h); });
  }
};
__device__ __forceinline__ void p_compute(lds_t lds, u16* pq, int tb, int et, int w, int lane STAMP_ARG) {
  const int r = lane & 31, h = lane >> 5;
  const unsigned a0 = km_read_base(32 * w + r, h), f0 = kn_read_base(lane, 0), f1 = kn_read_base(lane, 1);
  f32x16 acc0 = {}, acc1 = {};
  unsigned wo = P_WB, xo = P_XB;
  struct Fr { bf16x8 tok[4], fa[4], fb[4]; };
  Fr A, B;
  auto rd = [&](Fr& f, int k) {
    lds_t wb = lds + wo, xb = lds + xo;
    f.tok[k] = ld_b128(wb + (a0 ^ (k << 5)));
    f.fa[k] = ld_tr(xb + f0 + k * 2048, xb + f0 + k * 2048 + 512);
    f.fb[k] = ld_tr(xb + f1 + k * 2048, xb + f1 + k * 2048 + 512);
  };
  auto adv = [&]() {
    wo = wo + 16384 == P_WB + 7 * 16384 ? P_WB : wo + 16384;
    xo = xo + 8192 == P_XB + 6 * 8192 ? P_XB : xo + 8192;
  };
  auto mm = [&](const Fr& f, int k) { acc0 = mfma(f.fa[k], f.tok[k], acc0); acc1 = mfma(f.fb[k], f.tok[k], acc1); };
  auto step = [&](Fr& nx, const Fr& cu) {
    cbar();
#pragma unroll
    for (int k = 0; k < 4; ++k) { rd(nx, k); SCHED(); mm(cu, k); SCHED(); }
    adv();
  };
  cbar();
  STAMP(1);
#pragma unroll
  for (int k = 0; k < 4; ++k) rd(A, k);
  adv();
#pragma unroll 1
  for (int s = 1; s < 31; s += 2) { step(B, A); step(A, B); }
  step(B, A);
  cbar();
  STAMP(2);
#pragma unroll
  for (int k = 0; k < 4; ++k) mm(B, k);
  const float sg = w >= 2 ? -1.f : 1.f;
  const rsrc_t ro = mk_rsrc(pq + (size_t)(64 * tb + 32 * (w & 1)) * 1024 + (w >> 1) * 512 + 64 * et, 32 * 2048);
  const unsigned vo = r * 2048 + h * 16;
#pragma unroll
  for (int ft = 0; ft < 2; ++ft)
#pragma unroll
    for (int q = 0; q < 2; ++q) {
      const f32x16& a = ft ? acc1 : acc0;
      u32x4 v = {pk2(sg * a[8 * q], sg * a[8 * q + 1]), pk2(sg * a[8 * q + 2], sg * a[8 * q + 3]), pk2(sg * a[8 * q + 4], sg * a[8 * q + 5]), pk2(sg * a[8 * q + 6], sg * a[8 * q + 7])};
      __builtin_amdgcn_raw_buffer_store_b128(v, ro, vo + ft * 64 + q * 32, 0, SC1);
    }
}

__device__ __forceinline__ void add_res(const f32x16& acc, u32x4& r0, u32x4& r1) {
#pragma unroll
  for (int i = 0; i < 4; ++i) {
    r0[i] = pk2(acc[2 * i] + bflo(r0[i]), acc[2 * i + 1] + bfhi(r0[i]));
    r1[i] = pk2(acc[8 + 2 * i] + bflo(r1[i]), acc[8 + 2 * i + 1] + bfhi(r1[i]));
  }
}
template <int AUX> __device__ __forceinline__ void store_tile(u16* xo, const u32x4& o0, const u32x4& o1, int row0, int col0, int lane) {
  const rsrc_t ro = mk_rsrc(xo + (size_t)row0 * DM + col0, 32 * DM * 2);
  const unsigned vo = (lane & 31) * DM * 2 + (lane >> 5) * 16;
  __builtin_amdgcn_raw_buffer_store_b128(o0, ro, vo, 0, AUX);
  __builtin_amdgcn_raw_buffer_store_b128(o1, ro, vo + 32, 0, AUX);
}
__device__ __forceinline__ void part_stats(const u32x4& o0, const u32x4& o1, float (&v)[16], float& m, float& m2) {
  float s = 0.f;
#pragma unroll
  for (int i = 0; i < 4; ++i) { v[2 * i] = bflo(o0[i]); v[2 * i + 1] = bfhi(o0[i]); v[8 + 2 * i] = bflo(o1[i]); v[8 + 2 * i + 1] = bfhi(o1[i]); }
#pragma unroll
  for (int i = 0; i < 16; ++i) s += v[i];
  m = xhalf_sum(s) * (1.f / 32.f);
  m2 = 0.f;
#pragma unroll
  for (int i = 0; i < 16; ++i) { float d = v[i] - m; m2 += d * d; }
  m2 = xhalf_sum(m2);
}
__device__ __forceinline__ void store_stats(float* stats, float m, float m2, int row0, int col0, int lane) {
  const rsrc_t rs = mk_rsrc(stats + ((size_t)(col0 >> 5) * SEQ + row0) * 2, 32 * 8);
  if (lane < 32) { u32x2 sv = {__float_as_uint(m), __float_as_uint(m2)}; __builtin_amdgcn_raw_buffer_store_b64(sv, rs, lane * 8, 0, SC1); }
}
__device__ __forceinline__ void load_stats(const float* stats, int row0, int lane, u32x2 (&t)[8]) {
  const rsrc_t rs = mk_rsrc(stats + (size_t)row0 * 2, 16 * SEQ * 8 - row0 * 8);
#pragma unroll
  for (int i = 0; i < 8; ++i) t[i] = __builtin_amdgcn_raw_buffer_load_b64(rs, (lane & 31) * 8 + (8 * (lane >> 5) + i) * SEQ * 8, 0, SC1);
}
__device__ __forceinline__ void combine_stats(const u32x2 (&t)[8], float& mean, float& rstd) {
  float ms = 0.f, m2 = 0.f;
#pragma unroll
  for (int i = 0; i < 8; ++i) { ms += __uint_as_float(t[i][0]); m2 += __uint_as_float(t[i][1]); }
  mean = xhalf_sum(ms) * (1.f / 16.f);
#pragma unroll
  for (int i = 0; i < 8; ++i) { float d = __uint_as_float(t[i][0]) - mean; m2 += 32.f * d * d; }
  rstd = rsqrtf(xhalf_sum(m2) * (1.f / 512.f) + 1e-5f);
}

constexpr int T_WB = 0, T_XB = 6 * 16384;
template <int NST, class FF_> __device__ __forceinline__ f32x16 tile64_loop(lds_t lds, unsigned a0, FF_ feat, int w, int lane STAMP_ARG) {
  f32x16 acc = {};
  unsigned wo = T_WB, xo = T_XB;
  struct Fr { bf16x8 tok[8], f[8]; };
  Fr A, B;
  auto rd = [&](Fr& fr, int k) {
    fr.tok[k] = ld_b128(lds + xo + (k >> 2) * 8192 + (a0 ^ ((k & 3) << 5)));
    fr.f[k] = feat(lds + wo, k);
  };
  auto adv = [&]() {
    wo = wo + 16384 == T_WB + 6 * 16384 ? T_WB : wo + 16384;
    xo = xo + 16384 == T_XB + 4 * 16384 ? T_XB : xo + 16384;
  };
  auto step = [&](Fr& nx, const Fr& cu) {
    cbar();
#pragma unroll
    for (int k = 0; k < 8; ++k) { rd(nx, k); SCHED(); acc = mfma(cu.f[k], cu.tok[k], acc); SCHED(); }
    adv();
  };
  cbar();
  STAMP(1);
#pragma unroll
  for (int k = 0; k < 8; ++k) rd(A, k);
  adv();
#pragma unroll 1
  for (int s = 1; s < NST - 1; s += 2) { step(B, A); step(A, B); }
  step(B, A);
  cbar();
  STAMP(2);
#pragma unroll
  for (int k = 0; k < 8; ++k) acc = mfma(B.f[k], B.tok[k], acc);
  return acc;
}

using SchedY = Sched<8, 4, 6, 4, 4>;
struct YLoader {
  rsrc_t rre, rim; unsigned vw, vx; lds_t lds; int lw;
  __device__ __forceinline__ YLoader(lds_t lds_, const Ptrs& p, int et, int lw_, int lane) : lds(lds_), lw(lw_) {
    rre = mk_rsrc(p.dre + (size_t)et * 64 * DM, 64 * DM * 2); rim = mk_rsrc(p.dim + (size_t)et * 64 * DM, 64 * DM * 2);
    vw = km_src(lw, lane, DM * 2); vx = km_src(lw, lane, 2048);
  }
  template <class S, class T, class H> __device__ __forceinline__ void W(S s, T slot, H thr) {
    static_for<0, 4>([&](auto i) {
      constexpr int kk = 2 * s.v + (i.v >> 1), n = i.v & 1;
      dma16<0>(kk < 8 ? rre : rim, lds + T_WB + slot.v * 16384 + (i.v >> 1) * 8192 + (lw + 4 * n) * 1024, vw, (kk & 7) * 128 + n * 32 * DM * 2);
      if constexpr (thr.v < 63) WAIT_V(thr.v);
    });
  }
  __device__ __forceinline__ void prefetch() { l_prefetch_w<SchedY>([&](auto s, auto t, auto h) { W(s, t, h); }); }
  __device__ __forceinline__ void run(const u16* pq, int tb) {
    const rsrc_t rx = mk_rsrc(pq + (size_t)tb * 64 * 1024, 64 * 2048);
    l_run<SchedY>([&](auto s, auto slot) {
      static_for<0, 4>([&](auto i) {
        constexpr int kk = 2 * s.v + (i.v >> 1), n = i.v & 1;
        dma16<SC1>(rx, lds + T_XB + slot.v * 16384 + (i.v >> 1) * 8192 + (lw + 4 * n) * 1024, vx, kk * 128 + n * 32 * 2048);
      });
    }, [&](auto s, auto t, auto h) { W(s, t, h); });
  }
};

using SchedC = Sched<8, 3, 4, 4, 7>;
constexpr int C_WB = 0, C_WS = 28672, C_XB = 4 * C_WS;
struct CLoader {
  const char* wsrc[7]; unsigned vx; lds_t lds; int lw;
  __device__ __forceinline__ CLoader(lds_t lds_, const Ptrs& p, int fc, int lw_, int lane) : lds(lds_), lw(lw_) {
    vx = km_src(lw, lane, DM * 2);
#pragma unroll
    for (int n = 0; n < 7; ++n) {
      int idx = 64 * n + lane, kr = idx / 28, c = idx - kr * 28;
      const u16* base = c < 14 ? p.wg : p.wu;
      int cc = c < 14 ? c : c - 14;
      wsrc[n] = (const char*)(base + (size_t)(16 * lw + kr) * FF + 112 * fc + 8 * cc);
    }
  }
  template <class S, class T, class H> __device__ __forceinline__ void W(S s, T slot, H thr) {
    static_for<0, 7>([&](auto n) {
      __builtin_amdgcn_global_load_lds((const unsigned*)(wsrc[n.v] + (size_t)s.v * 64 * FF * 2), (LAS unsigned*)(lds + C_WB + slot.v * C_WS + lw * 7168 + n.v * 1024), 16, 0, 0);
      if constexpr (thr.v < 63) WAIT_V(thr.v);
    });
  }
  __device__ __forceinline__ void prefetch() { l_prefetch_w<SchedC>([&](auto s, auto t, auto h) { W(s, t, h); }); }
  __device__ __forceinline__ void run(const u16* xa, int rb) {
    const rsrc_t rx = mk_rsrc(xa + (size_t)rb * 128 * DM, 128 * DM * 2);
    l_run<SchedC>([&](auto s, auto slot) {
      static_for<0, 4>([&](auto n) { dma16<SC1>(rx, lds + C_XB + slot.v * 16384 + (lw + 4 * n.v) * 1024, vx, s.v * 128 + n.v * 32 * DM * 2); });
    }, [&](auto s, auto t, auto h) { W(s, t, h); });
  }
};
__device__ __forceinline__ void c_compute(lds_t lds, const float* stats, const unsigned* csf, u16* hh, int rb, int fc, int w, int lane STAMP_ARG) {
  const int r = lane & 31, h = lane >> 5;
  const unsigned a0 = km_read_base(32 * w + r, h);
  const int blk = (lane >> 4) & 1, q = (lane & 15) >> 2, pp = ((lane & 1) << 1) | ((lane >> 1) & 1);
  const unsigned f0 = (8 * h + q) * 448 + blk * 224 + 8 * pp;
  u32x4 cs[4];
#pragma unroll
  for (int i = 0; i < 4; ++i) cs[i] = *(const u32x4*)(csf + ((size_t)fc * 32 + r) * 16 + 4 * i);
  f32x16 acc[7];
#pragma unroll
  for (int j = 0; j < 7; ++j) acc[j] = f32x16{};
  unsigned wo = C_WB, xo = C_XB;
  struct Fr { bf16x8 tok, f[7]; };
  Fr A, B;
  auto rdt = [&](Fr& fr, int k) { fr.tok = ld_b128(lds + xo + (a0 ^ (k << 5))); };
  auto rdf = [&](Fr& fr, int k, int j) { lds_t wb = lds + wo + f0; fr.f[j] = ld_tr(wb + 16 * k * 448 + 32 * j, wb + (16 * k + 4) * 448 + 32 * j); };
  auto grp = [&](Fr& nx, const Fr& cu, int k, bool rdn) {
    if (rdn) rdt(nx, k);
#pragma unroll
    for (int j = 0; j < 7; ++j) {
      if (rdn) rdf(nx, k, j);
      SCHED();
      acc[j] = mfma(cu.f[j], cu.tok, acc[j]);
      SCHED();
    }
  };
  auto adv = [&]() {
    wo = wo + C_WS == C_WB + 4 * C_WS ? C_WB : wo + C_WS;
    xo = xo + 16384 == C_XB + 3 * 16384 ? C_XB : xo + 16384;
  };
  cbar();
  STAMP(1);
  u32x2 st[8];
  load_stats(stats, 128 * rb + 32 * w, lane, st);
  rdt(A, 0);
#pragma unroll
  for (int j = 0; j < 7; ++j) rdf(A, 0, j);
#pragma unroll 1
  for (int s = 0; s < 7; ++s) {
    grp(B, A, 1, true);
    grp(A, B, 2, true);
    grp(B, A, 3, true);
    adv();
    cbar();
    grp(A, B, 0, true);
  }
  grp(B, A, 1, true);
  grp(A, B, 2, true);
  grp(B, A, 3, true);
  cbar();
  STAMP(2);
  grp(A, B, 0, false);
  float mean, rstd;
  combine_stats(st, mean, rstd);
  {
    const unsigned mh = pk2(-mean, 0.f) & 0xffffu;
    const unsigned ml = pk2(-mean - bflo(mh), 0.f) & 0xffffu;
    u32x4 bt = {h ? 0u : (mh | (ml << 16)), h ? 0u : mh, 0u, 0u};
#pragma unroll
    for (int j = 0; j < 7; ++j) {
      u32x4 at = {h ? 0u : cs[j >> 1][2 * (j & 1)], h ? 0u : cs[j >> 1][2 * (j & 1) + 1], 0u, 0u};
      acc[j] = mfma(__builtin_bit_cast(bf16x8, at), __builtin_bit_cast(bf16x8, bt), acc[j]);
    }
  }
  const rsrc_t ro = mk_rsrc(hh + (size_t)(128 * rb + 32 * w) * FF + 112 * fc, 32 * FF * 2);
  const float ng = -1.4426950408889634f * rstd;
#pragma unroll
  for (int j = 0; j < 7; ++j) {
    float o[8];
#pragma unroll
    for (int i = 0; i < 8; i += 2) {
      f32x2 g = {acc[j][i], acc[j][i + 1]}, u = {acc[j][8 + i], acc[j][9 + i]};
      g *= ng; u *= rstd;
      f32x2 e = {__builtin_amdgcn_exp2f(g[0]), __builtin_amdgcn_exp2f(g[1])};
      e += 1.f;
      f32x2 sg = {__builtin_amdgcn_rcpf(e[0]), __builtin_amdgcn_rcpf(e[1])};
      u *= sg;
      o[i] = u[0]; o[i + 1] = u[1];
    }
    u32x4 v = {pk2(o[0], o[1]), pk2(o[2], o[3]), pk2(o[4], o[5]), pk2(o[6], o[7])};
    __builtin_amdgcn_raw_buffer_store_b128(v, ro, r * FF * 2 + h * 16 + 32 * j, 0, SC1);
  }
}

using SchedD = Sched<14, 4, 6, 4, 4>;
struct DLoader {
  rsrc_t rw; unsigned vw, vx; lds_t lds; int lw;
  __device__ __forceinline__ DLoader(lds_t lds_, const Ptrs& p, int et, int lw_, int lane) : lds(lds_), lw(lw_) {
    rw = mk_rsrc(p.wd + et * 64, FF * DM * 2 - et * 128);
    vw = kn_src(lw, lane, DM * 2); vx = km_src(lw, lane, FF * 2);
  }
  template <class S, class T, class H> __device__ __forceinline__ void W(S s, T slot, H thr) {
    static_for<0, 4>([&](auto i) {
      constexpr int kk = 2 * s.v + (i.v >> 1), n = i.v & 1;
      dma16<0>(rw, lds + T_WB + slot.v * 16384 + (i.v >> 1) * 8192 + (lw + 4 * n) * 1024, vw, (kk * 64 + 32 * n) * DM * 2);
      if constexpr (thr.v < 63) WAIT_V(thr.v);
    });
  }
  __device__ __forceinline__ void prefetch() { l_prefetch_w<SchedD>([&](auto s, auto t, auto h) { W(s, t, h); }); }
  __device__ __forceinline__ void run(const u16* hh, int tb) {
    const rsrc_t rx = mk_rsrc(hh + (size_t)tb * 64 * FF, 64 * FF * 2);
    l_run<SchedD>([&](auto s, auto slot) {
      static_for<0, 4>([&](auto i) {
        constexpr int kk = 2 * s.v + (i.v >> 1), n = i.v & 1;
        dma16<SC1>(rx, lds + T_XB + slot.v * 16384 + (i.v >> 1) * 8192 + (lw + 4 * n) * 1024, vx, kk * 128 + n * 32 * FF * 2);
      });
    }, [&](auto s, auto t, auto h) { W(s, t, h); });
  }
};

__global__ __launch_bounds__(512, 2) __attribute__((amdgpu_waves_per_eu(2, 2))) void k_main(Ptrs p) {
  extern __shared__ __attribute__((aligned(16))) char smem[];
  lds_t lds = (lds_t)smem;
  const int lane = threadIdx.x & 63, wid = __builtin_amdgcn_readfirstlane(threadIdx.x >> 6);
  const int xcd = blockIdx.x & 7, j = blockIdx.x >> 3;
  const int tb = xcd * 4 + (j & 3), et = j >> 2;
  const int rb = (xcd & 1) * 8 + (j & 7), fc = (xcd >> 1) * 4 + (j >> 3);
  if (wid >= 4) {
    { PLoader L(lds, p, fresh_s(tb), fresh_s(wid - 4), fresh_v(lane)); LSTAMP(0, 0); L.prefetch(); LSTAMP(0, 1); LSTAMP(0, 2); L.run(p.xn, et); }
#pragma unroll 1
    for (int l = 0; l < 2; ++l) {
      { YLoader L(lds, p, fresh_s(et), fresh_s(wid - 4), fresh_v(lane)); LSTAMP(4 * l + 1, 0); l_seam(L); LSTAMP(4 * l + 1, 1); LSTAMP(4 * l + 1, 2); L.run(p.pq + l * PQ_L, fresh_s(tb)); }
      { CLoader L(lds, p, fresh_s(fc), fresh_s(wid - 4), fresh_v(lane)); LSTAMP(4 * l + 2, 0); l_seam(L); LSTAMP(4 * l + 2, 1); LSTAMP(4 * l + 2, 2); L.run(p.xa + l * XN_L, fresh_s(rb)); }
      { DLoader L(lds, p, fresh_s(et), fresh_s(wid - 4), fresh_v(lane)); LSTAMP(4 * l + 3, 0); l_seam(L); LSTAMP(4 * l + 3, 1); LSTAMP(4 * l + 3, 2); L.run(p.hh + l * HH_L, fresh_s(tb)); }
      if (l == 0) { PLoader L(lds, p, fresh_s(tb), fresh_s(wid - 4), fresh_v(lane)); LSTAMP(4, 0); l_seam(L); LSTAMP(4, 1); LSTAMP(4, 2); L.run(p.xn + XN_L, fresh_s(et)); }
    }
  } else {
    const int w = wid, tw = w & 1, fw = w >> 1;
    const int row0 = 64 * tb + 32 * tw, col0 = 64 * et + 32 * fw;
    u32x4 r0, r1;
    {
      const int r = lane & 31, h = lane >> 5;
      const rsrc_t rr = mk_rsrc(p.x0 + (size_t)row0 * DM + col0, 32 * DM * 2);
      r0 = __builtin_amdgcn_raw_buffer_load_b128(rr, r * DM * 2 + h * 16, 0, 0);
      r1 = __builtin_amdgcn_raw_buffer_load_b128(rr, r * DM * 2 + h * 16 + 32, 0, 0);
    }
#pragma unroll 1
    for (int l = 0; l < 2; ++l) {
      const int sb = 4 * l;
      {
        STAMP(0);
        p_compute(lds, p.pq + l * PQ_L, fresh_s(tb), fresh_s(et), fresh_s(w), fresh_v(lane) STAMP_PASS(4 * l + 0));
        STAMP(3); WAIT_V(0); STAMP(4); bar(); STAMP(5);
        if (w == 0) { signal(p.mbox, sb + 0, tb * 8 + (lane & 7), et, lane < 8); await(p.mbox, p.tmo, sb + 0, tb * 8 + et, 8, lane); }
        bar(); STAMP(6);
      }
      {
        STAMP(0);
        const int ln = fresh_v(lane), r = ln & 31, h = ln >> 5;
        const unsigned a0 = km_read_base(32 * tw + r, h), b0 = km_read_base(32 * fw + perm23(r), h);
        f32x16 acc = tile64_loop<8>(lds, a0, [&](lds_t wb, int k) { return ld_b128(wb + (k >> 2) * 8192 + (b0 ^ ((k & 3) << 5))); }, w, lane STAMP_PASS(4 * l + 1));
        add_res(acc, r0, r1);
        store_tile<SC1>(p.xa + l * XN_L, r0, r1, row0, col0, fresh_v(lane));
        float v[16], m, m2;
        part_stats(r0, r1, v, m, m2);
        store_stats(p.stats + l * ST_L, m, m2, row0, col0, fresh_v(lane));
        STAMP(3); WAIT_V(0); STAMP(4); bar(); STAMP(5);
        if (w == 0) { signal(p.mbox, sb + 1, (tb >> 1) * 16 + (lane & 15), (tb & 1) * 8 + et, lane < 16); await(p.mbox, p.tmo, sb + 1, rb * 16 + fc, 16, lane); }
        bar(); STAMP(6);
      }
      {
        STAMP(0);
        c_compute(lds, p.stats + l * ST_L, p.csf, p.hh + l * HH_L, fresh_s(rb), fresh_s(fc), fresh_s(w), fresh_v(lane) STAMP_PASS(4 * l + 2));
        STAMP(3); WAIT_V(0); STAMP(4); bar(); STAMP(5);
        if (w == 0) { signal(p.mbox, sb + 2, (2 * rb + ((lane >> 3) & 1)) * 8 + (lane & 7), fc, lane < 16); await(p.mbox, p.tmo, sb + 2, tb * 8 + et, 16, lane); }
        bar(); STAMP(6);
      }
      {
        STAMP(0);
        const int ln = fresh_v(lane), r = ln & 31, h = ln >> 5;
        const unsigned a0 = km_read_base(32 * tw + r, h), f0 = kn_read_base(ln, fw);
        f32x16 acc = tile64_loop<14>(lds, a0, [&](lds_t wb, int k) { return ld_tr(wb + f0 + k * 2048, wb + f0 + k * 2048 + 512); }, w, lane STAMP_PASS(4 * l + 3));
        add_res(acc, r0, r1);
        if (l == 1) {
          store_tile<0>(p.out, r0, r1, row0, col0, fresh_v(lane));
        } else {
          float v[16], m, m2;
          part_stats(r0, r1, v, m, m2);
          const int le = fresh_v(lane);
          store_stats((float*)p.gran, m, m2, row0, col0, le);
          u32x2 st[8];
          for (unsigned spins = 0;; ++spins) {
            asm volatile("" ::: "memory");
            load_stats((const float*)p.gran, row0, le, st);
            bool ok = true;
#pragma unroll
            for (int i = 0; i < 8; ++i) ok = ok && st[i][1] != 0xffffffffu;
            if (__all(ok)) break;
            if (spins > SPIN_CAP) { if (lane == 0) __hip_atomic_store((gu32*)p.tmo, 2u, RLX_AGENT); break; }
            if (SLP) __builtin_amdgcn_s_sleep(SLP);
          }
          float mean, rstd;
          combine_stats(st, mean, rstd);
          u32x4 n0, n1;
#pragma unroll
          for (int i = 0; i < 4; ++i) {
            n0[i] = pk2((v[2 * i] - mean) * rstd, (v[2 * i + 1] - mean) * rstd);
            n1[i] = pk2((v[8 + 2 * i] - mean) * rstd, (v[8 + 2 * i + 1] - mean) * rstd);
          }
          store_tile<SC1>(p.xn + XN_L, n0, n1, row0, col0, le);
          STAMP(3); WAIT_V(0); STAMP(4); bar(); STAMP(5);
          if (w == 0) { signal(p.mbox, 3, (lane & 31) * 8 + et, tb, lane < 32); await(p.mbox, p.tmo, 3, tb * 8 + et, 32, lane); }
          bar(); STAMP(6);
        }
      }
    }
  }
}

__device__ __forceinline__ float wave_sum(float v) {
#pragma unroll
  for (int o = 1; o < 64; o <<= 1) v += __shfl_xor(v, o);
  return v;
}
__global__ __launch_bounds__(512) void k_pre(Ptrs p) {
  const int lane = threadIdx.x & 63, wid = threadIdx.x >> 6;
  if (blockIdx.x < 256) {
    const int row = blockIdx.x * 8 + wid;
    u32x4 t = *(const u32x4*)(p.x0 + (size_t)row * DM + lane * 8);
    const int gid = blockIdx.x * 512 + threadIdx.x;
    if (gid < 7 * 256 * 8) ((u32x4*)p.mbox)[gid] = u32x4{0u, 0u, 0u, 0u};
    if (gid < 16 * SEQ / 2) ((u32x4*)p.gran)[gid] = u32x4{~0u, ~0u, ~0u, ~0u};
    if (gid == 0) *p.tmo = 0u;
    float v[8], s = 0.f;
#pragma unroll
    for (int i = 0; i < 4; ++i) { v[2 * i] = bflo(t[i]); v[2 * i + 1] = bfhi(t[i]); s += v[2 * i] + v[2 * i + 1]; }
    const float m = wave_sum(s) * (1.f / 512.f);
    float m2 = 0.f;
#pragma unroll
    for (int i = 0; i < 8; ++i) { v[i] -= m; m2 += v[i] * v[i]; }
    const float rs = rsqrtf(wave_sum(m2) * (1.f / 512.f) + 1e-5f);
    u32x4 o = {pk2(v[0] * rs, v[1] * rs), pk2(v[2] * rs, v[3] * rs), pk2(v[4] * rs, v[5] * rs), pk2(v[6] * rs, v[7] * rs)};
    *(u32x4*)(p.xn + (size_t)row * DM + lane * 8) = o;
  } else {
    __shared__ float red[8][64];
    const int b = blockIdx.x - 256;
    const u16* w = (b < 28 ? p.wg : p.wu) + (b % 28) * 64 + (lane & 7) * 8;
    float a[8] = {};
#pragma unroll
    for (int i = 0; i < 8; ++i) {
      u32x4 t = *(const u32x4*)(w + (size_t)(i * 64 + wid * 8 + (lane >> 3)) * FF);
#pragma unroll
      for (int e = 0; e < 4; ++e) { a[2 * e] += bflo(t[e]); a[2 * e + 1] += bfhi(t[e]); }
    }
#pragma unroll
    for (int e = 0; e < 8; ++e) { a[e] += __shfl_xor(a[e], 8); a[e] += __shfl_xor(a[e], 16); a[e] += __shfl_xor(a[e], 32); }
    if (lane < 8) {
#pragma unroll
      for (int e = 0; e < 8; ++e) red[wid][lane * 8 + e] = a[e];
    }
    __syncthreads();
    if (threadIdx.x < 64) {
      float s = 0.f;
#pragma unroll
      for (int i = 0; i < 8; ++i) s += red[i][threadIdx.x];
      const int c = (b % 28) * 64 + threadIdx.x, fc = c / 112, rem = c - fc * 112, jj = rem >> 4, i = perm23(rem & 15) + (b < 28 ? 0 : 16);
      const unsigned hi = pk2(s, 0.f) & 0xffffu, lo = pk2(s - bflo(hi), 0.f) & 0xffffu;
      u32x2 o = {hi | (hi << 16), lo};
      *(u32x2*)(p.csf + ((size_t)fc * 32 + i) * 16 + 2 * jj) = o;
    }
  }
}
}

extern "C" void kernel_launch(void* const* d_in, const int* in_sizes, int n_in, void* d_out, int out_size, void* d_ws, size_t ws_size, hipStream_t stream) {
  Ptrs p;
  p.x0 = (const u16*)d_in[0]; p.sre = (const u16*)d_in[1]; p.sim = (const u16*)d_in[2]; p.dre = (const u16*)d_in[3]; p.dim = (const u16*)d_in[4];
  p.wg = (const u16*)d_in[6]; p.wu = (const u16*)d_in[7]; p.wd = (const u16*)d_in[8];
  char* ws = (char*)d_ws;
  const size_t MB = 1 << 20;
  p.out = (u16*)d_out;
  p.xn = (u16*)ws;
  p.pq = (u16*)(ws + 4 * MB);
  p.xa = (u16*)(ws + 12 * MB);
  p.hh = (u16*)(ws + 16 * MB);
  p.stats = (float*)(ws + 31 * MB);
  p.stamps = (unsigned long long*)(ws + 32 * MB);
  p.csf = (unsigned*)(ws + 33 * MB);
  p.gran = (unsigned*)(ws + 34 * MB);
  p.mbox = (unsigned*)(ws + 35 * MB);
  p.tmo = (unsigned*)(ws + 36 * MB);
  (void)hipFuncSetAttribute((const void*)k_main, hipFuncAttributeMaxDynamicSharedMemorySize, LDS_BYTES);
  k_pre<<<256 + 56, 512, 0, stream>>>(p);
  k_main<<<256, 512, LDS_BYTES, stream>>>(p);
}
```

```cpp
#include <hip/hip_runtime.h>
#include <stdint.h>

namespace {
typedef unsigned short u16;
typedef short s16x4 __attribute__((ext_vector_type(4)));
typedef short s16x8 __attribute__((ext_vector_type(8)));
typedef __bf16 bf16x8 __attribute__((ext_vector_type(8)));
typedef __bf16 bf16x2 __attribute__((ext_vector_type(2)));
typedef float f32x2 __attribute__((ext_vector_type(2)));
typedef float f32x4 __attribute__((ext_vector_type(4)));
typedef float f32x16 __attribute__((ext_vector_type(16)));
typedef unsigned u32x4 __attribute__((ext_vector_type(4)));
typedef unsigned u32x2 __attribute__((ext_vector_type(2)));
#define LAS __attribute__((address_space(3)))
typedef __amdgpu_buffer_rsrc_t rsrc_t;
typedef LAS char* lds_t;
typedef __attribute__((address_space(1))) unsigned gu32;
typedef __attribute__((address_space(1))) unsigned long long gu64;
#define RLX_AGENT __ATOMIC_RELAXED, __HIP_MEMORY_SCOPE_AGENT

constexpr int SEQ = 2048, DM = 512, FF = 1792;
constexpr int LDS_BYTES = 163840, XB = 98304;
constexpr int SC1 = 16;
constexpr unsigned SPIN_CAP = 1u << 18;

__device__ __forceinline__ rsrc_t mk_rsrc(const void* p, unsigned bytes) { return __builtin_amdgcn_make_buffer_rsrc((void*)p, 0, bytes, 0x00020000); }
template <int AUX> __device__ __forceinline__ void dma16(rsrc_t rs, lds_t dst, unsigned voff, unsigned soff) {
  __builtin_amdgcn_raw_ptr_buffer_load_lds(rs, (LAS void*)dst, 16, voff, soff, 0, AUX);
}
#define WAIT_V(n) asm volatile("s_waitcnt vmcnt(%0)" ::"n"(n) : "memory")
#define SCHED() __builtin_amdgcn_sched_barrier(0)
#define WAIT_L0() __builtin_amdgcn_s_waitcnt(0xC07F)
__device__ __forceinline__ void bar() { asm volatile("" ::: "memory"); __builtin_amdgcn_s_barrier(); asm volatile("" ::: "memory"); }
__device__ __forceinline__ void cbar() { WAIT_L0(); bar(); }
__device__ __forceinline__ int fresh_v(int x) { asm volatile("" : "+v"(x)); return x; }
__device__ __forceinline__ int fresh_s(int x) { asm volatile("" : "+s"(x)); return x; }

template <int I> struct IC { static constexpr int v = I; };
template <int A, int B, class F> __device__ __forceinline__ void static_for(F&& f) {
  if constexpr (A < B) { f(IC<A>{}); static_for<A + 1, B>(f); }
}

#define PXI 3
#define ROT 8
#define ROTD 1
struct GeoP { static constexpr int NST = 32, NX = 8, nX = 2, NW = 6, nW = 4, OW = 0; };
struct GeoY { static constexpr int NST = 8,  NX = 4, nX = 4, NW = 6, nW = 4, OW = 4; };
struct GeoC { static constexpr int NST = 8,  NX = 4, nX = 4, NW = 3, nW = 7, OW = 0; };
struct GeoD { static constexpr int NST = 14, NX = 4, nX = 4, NW = 6, nW = 4, OW = 4; };
struct EarlyNone { static constexpr int at(int) { return -1; } };
struct EarlyY { static constexpr int at(int j) { constexpr int t[6] = {29, 30, 31, -1, 27, 28}; return j < 6 ? t[j] : -1; } };
struct EarlyC { static constexpr int at(int j) { constexpr int t[3] = {4, 6, -1}; return j < 3 ? t[j] : -1; } };
struct EarlyD { static constexpr int at(int j) { constexpr int t[6] = {6, 7, 7, -1, -1, -1}; return j < 6 ? t[j] : -1; } };
struct EarlyP { static constexpr int at(int j) { constexpr int t[6] = {9, 10, 11, 12, 13, -1}; return j < 6 ? t[j] : -1; } };

constexpr int PLAN_MAX = 40;
template <class G, class MINE, class GN, class NEXT> struct Plan {
  int xstep[PLAN_MAX], wstep[PLAN_MAX], waitn[PLAN_MAX];
  constexpr Plan() : xstep{}, wstep{}, waitn{} {
    int nx = 0;
    for (int j = 0; j < G::NST; ++j) { xstep[j] = -2; wstep[j] = MINE::at(j) >= 0 ? -3 : -2; }
    for (int s = -1; s < G::NST; ++s) {
      const int lx = s - 1 + G::NX < G::NX - 1 ? G::NX - 1 : s - 1 + G::NX, lw = s - 1 + G::NW < G::NW - 1 ? G::NW - 1 : s - 1 + G::NW;
      for (int c = 0; c < (s < 0 ? PXI : 2); ++c) if (nx < G::NST && nx <= lx) xstep[nx++] = s;
      int c = 0;
      for (int j = 0; j < G::NST; ++j)
        if (wstep[j] == -2 && j <= lw && (s < 0 ? (j < PXI) : c < 2)) { wstep[j] = s; ++c; }
    }
    for (int s = 0; s < G::NST; ++s) {
      int total = 0, last = 0;
      for (int b = 0; b < PLAN_MAX; ++b)
        for (int j = 0; j < G::NST; ++j) if (MINE::at(j) == b) { total += G::nW; if (j == s) last = total; }
      for (int t = -1; t < s; ++t) {
        for (int j = 0; j < G::NST; ++j) {
          if (xstep[j] == t) { total += G::nX; if (j == s) last = total; }
          if (t < 0 && wstep[j] == t) { total += G::nW; if (j == s) last = total; }
        }
        if (t >= 0) {
          for (int j = 0; j < G::NST; ++j) if (wstep[j] == t) { total += G::nW; if (j == s) last = total; }
          for (int j = 0; j < GN::NST; ++j) if (NEXT::at(j) == t) total += GN::nW;
        }
      }
      waitn[s] = total - last > 63 ? 63 : total - last;
    }
  }
  constexpr bool ok() const {
    for (int j = 0; j < G::NST; ++j) if (xstep[j] >= j || xstep[j] < -1 || wstep[j] >= j || wstep[j] == -2) return false;
    return true;
  }
};
template <class G, class MINE, class GN, class NEXT, class FX, class FW, class FN>
__device__ __forceinline__ void l_run(FX issueX, FW issueW, FN issueNext) {
  constexpr Plan<G, MINE, GN, NEXT> plan{};
  static_assert(plan.ok(), "a stage would be waited for before it is issued");
  static_for<0, G::NST>([&](auto j) {
    constexpr Plan<G, MINE, GN, NEXT> pl{};
    if constexpr (pl.xstep[j.v] == -1) issueX(j);
    if constexpr (pl.wstep[j.v] == -1) issueW(j);
  });
  static_for<0, G::NST>([&](auto s) {
    constexpr Plan<G, MINE, GN, NEXT> pl{};
    WAIT_V(pl.waitn[s.v]);
    bar();
    static_for<0, G::NST>([&](auto j) { if constexpr (pl.xstep[j.v] == s.v) issueX(j); });
    static_for<0, G::NST>([&](auto j) { if constexpr (pl.wstep[j.v] == s.v) issueW(j); });
    static_for<0, GN::NST>([&](auto j) { if constexpr (NEXT::at(j.v) == s.v) issueNext(j); });
  });
  bar();
}
template <class G> constexpr int wslot(int j) { return (G::OW + j) % G::NW; }

__device__ __forceinline__ bf16x8 ld_b128(lds_t p) { return *(LAS bf16x8*)p; }
__device__ __forceinline__ bf16x8 ld_tr(lds_t p0, lds_t p1) {
  s16x4 a = __builtin_amdgcn_ds_read_tr16_b64_v4i16((LAS s16x4*)p0);
  s16x4 b = __builtin_amdgcn_ds_read_tr16_b64_v4i16((LAS s16x4*)p1);
  s16x8 f = __builtin_shufflevector(a, b, 0, 1, 2, 3, 4, 5, 6, 7);
  return __builtin_bit_cast(bf16x8, f);
}
__device__ __forceinline__ f32x16 mfma(bf16x8 a, bf16x8 b, f32x16 c) { return __builtin_amdgcn_mfma_f32_32x32x16_bf16(a, b, c, 0, 0, 0); }
__device__ __forceinline__ unsigned pk2(float a, float b) { f32x2 v = {a, b}; return __builtin_bit_cast(unsigned, __builtin_convertvector(v, bf16x2)); }
__device__ __forceinline__ float bflo(unsigned u) { return __uint_as_float(u << 16); }
__device__ __forceinline__ float bfhi(unsigned u) { return __uint_as_float(u & 0xffff0000u); }
__device__ __forceinline__ float xhalf_sum(float v) {
  auto r = __builtin_amdgcn_permlane32_swap(__float_as_uint(v), __float_as_uint(v), false, false);
  return __uint_as_float(r[0]) + __uint_as_float(r[1]);
}
__device__ __forceinline__ unsigned km_read_base(int row, int h) { return row * 128 + ((h ^ ((row >> 1) & 7)) << 4); }
__device__ __forceinline__ int perm23(int i) { return (i & ~12) | ((i & 4) << 1) | ((i & 8) >> 1); }
__device__ __forceinline__ unsigned km_src(int lw, int lane, int pitch) {
  return (8 * lw + (lane >> 3)) * pitch + (((lane & 7) ^ (((lw & 1) << 2) | (lane >> 4))) << 4);
}
__device__ __forceinline__ unsigned kn_src(int lw, int lane, int pitch) {
  return (8 * lw + (lane >> 3)) * pitch + (((lane & 7) ^ (((lane >> 4) & 1) << 2)) << 4);
}
__device__ __forceinline__ unsigned kn_read_base(int lane, int ft) {
  int h = lane >> 5, blk = (lane >> 4) & 1, q = (lane & 15) >> 2, p = lane & 3, pp = ((p & 1) << 1) | (p >> 1);
  int c = 4 * ft + 2 * blk + (pp >> 1);
  return (8 * h + q) * 128 + ((c ^ (((q >> 1) & 1) << 2)) << 4) + 8 * (pp & 1);
}

#define SPAN_BEGIN()
#define SPAN_MAIN()
#define SPAN_END()
#define STAMP(i)
#define STAMP_ARG
#define STAMP_PASS(ph)
#define STAMP_PTR(ph)

struct Ptrs {
  unsigned long long* stamps;
  const u16 *x0, *sre, *sim, *dre, *dim, *wg, *wu, *wd;
  u16 *out, *xn, *pq, *xa, *hh;
  float* stats;
  unsigned *csf, *gran, *mbox, *tmo;
};
constexpr size_t XN_L = (size_t)SEQ * DM, PQ_L = (size_t)SEQ * 1024, HH_L = (size_t)SEQ * FF, ST_L = (size_t)16 * SEQ * 2;

__device__ __forceinline__ void signal(unsigned* mbox, int seam, int consumer, int slot, bool active) {
  if (active) __hip_atomic_store((gu32*)mbox + ((size_t)seam * 256 + consumer) * 32 + slot, 1u, RLX_AGENT);
}
__device__ __forceinline__ void await(unsigned* mbox, unsigned* tmo, int seam, int me, int nprod, int lane) {
  gu32* q = (gu32*)mbox + ((size_t)seam * 256 + me) * 32 + (lane < nprod ? lane : 0);
  for (unsigned spins = 0;; ++spins) {
    unsigned x = __hip_atomic_load(q, RLX_AGENT);
    if (__all(x == 1u)) break;
    if (spins > SPIN_CAP) { if (lane == 0) __hip_atomic_store((gu32*)tmo, 1u, RLX_AGENT); break; }
    __builtin_amdgcn_s_sleep(1);
  }
}

struct PLoader {
  rsrc_t rre, rim; unsigned vw, vx; lds_t lds; int lw, k0;
  __device__ __forceinline__ PLoader(lds_t lds_, const Ptrs& p, int tb, int et, int lw_, int lane) : lds(lds_), lw(lw_), k0((ROT * et) & 31) {
    rre = mk_rsrc(p.sre + (size_t)tb * 64 * SEQ, 64 * SEQ * 2); rim = mk_rsrc(p.sim + (size_t)tb * 64 * SEQ, 64 * SEQ * 2);
    vw = km_src(lw, lane, SEQ * 2);
    vx = (lane >> 5) * 512 + (((lane & 31) ^ (4 * ((2 * lw + (lane >> 5)) & 3))) << 4);
  }
  template <class S> __device__ __forceinline__ void W(S s) {
    const int ks = (s.v + k0) & 31;
    static_for<0, 4>([&](auto n) { dma16<0>(n.v < 2 ? rre : rim, lds + wslot<GeoP>(s.v) * 16384 + (lw + 4 * n.v) * 1024, vw, ks * 128 + (n.v & 1) * 32 * SEQ * 2); });
  }
  template <class S> __device__ __forceinline__ void X(S s, rsrc_t rx) {
    const int ks = (s.v + k0) & 31;
    static_for<0, 2>([&](auto n) { dma16<SC1>(rx, lds + XB + (s.v % 8) * 8192 + n.v * 4096 + lw * 1024, vx, (2 * ks + n.v) * 64 * 512); });
  }
  __device__ __forceinline__ rsrc_t xr(const u16* xn, int et) { return mk_rsrc((const char*)xn + (8 * et + 2 * lw) * 512, SEQ * DM * 2 - (8 * et + 2 * lw) * 512); }
};
struct YLoader {
  rsrc_t rre, rim; unsigned vw, vx; lds_t lds; int lw;
  __device__ __forceinline__ YLoader(lds_t lds_, const Ptrs& p, int et, int lw_, int lane) : lds(lds_), lw(lw_) {
    rre = mk_rsrc(p.dre + (size_t)et * 64 * DM, 64 * DM * 2); rim = mk_rsrc(p.dim + (size_t)et * 64 * DM, 64 * DM * 2);
    vw = km_src(lw, lane, DM * 2); vx = lane * 16;
  }
  template <class S> __device__ __forceinline__ void W(S s) {
    static_for<0, 4>([&](auto i) {
      constexpr int kb = i.v >> 1, n = i.v & 1;
      dma16<0>(s.v < 4 ? rre : rim, lds + wslot<GeoY>(s.v) * 16384 + kb * 8192 + (lw + 4 * n) * 1024, vw, (2 * (s.v & 3) + kb) * 128 + n * 32 * DM * 2);
    });
  }
  template <class S> __device__ __forceinline__ void X(S s, rsrc_t rx) {
    static_for<0, 4>([&](auto i) {
      constexpr int blk = i.v >> 1, n = i.v & 1;
      dma16<SC1>(rx, lds + XB + (s.v % 4) * 16384 + blk * 8192 + n * 1024 + lw * 2048, vx, blk * 128 * 512 + (16 * s.v + 2 * n) * 512);
    });
  }
  __device__ __forceinline__ rsrc_t xr(const u16* pq, int tb) { return mk_rsrc((const char*)pq + ((size_t)2 * tb * 128 + 4 * lw) * 512, 2 * 128 * 512 - 4 * lw * 512); }
};
constexpr int C_WS = 28672;
struct CLoader {
  const char* wsrc[7]; unsigned vx; lds_t lds; int lw;
  __device__ __forceinline__ CLoader(lds_t lds_, const Ptrs& p, int fc, int lw_, int lane) : lds(lds_), lw(lw_) {
    vx = lane * 16;
#pragma unroll
    for (int n = 0; n < 7; ++n) {
      int idx = 64 * n + lane, kr = idx / 28, c = idx - kr * 28;
      const u16* base = c < 14 ? p.wg : p.wu;
      int cc = c < 14 ? c : c - 14;
      wsrc[n] = (const char*)(base + (size_t)(16 * lw + kr) * FF + 112 * fc + 8 * cc);
    }
  }
  template <class S> __device__ __forceinline__ void W(S s) {
    static_for<0, 7>([&](auto n) {
      __builtin_amdgcn_global_load_lds((const unsigned*)(wsrc[n.v] + (size_t)s.v * 64 * FF * 2), (LAS unsigned*)(lds + wslot<GeoC>(s.v) * C_WS + lw * 7168 + n.v * 1024), 16, 0, 0);
    });
  }
  template <class S> __device__ __forceinline__ void X(S s, rsrc_t rx) {
    static_for<0, 4>([&](auto n) { dma16<SC1>(rx, lds + XB + (s.v % 4) * 16384 + lw * 4096 + n.v * 1024, vx, (8 * s.v + 2 * n.v) * 512); });
  }
  __device__ __forceinline__ rsrc_t xr(const u16* xa, int rb) { return mk_rsrc((const char*)xa + (size_t)(4 * rb + lw) * 64 * 512, 64 * 512); }
};
struct DLoader {
  rsrc_t rw; unsigned vw, vx; lds_t lds; int lw, k0;
  __device__ __forceinline__ DLoader(lds_t lds_, const Ptrs& p, int tb, int et, int lw_, int lane) : lds(lds_), lw(lw_), k0(ROTD ? (tb * 3) % 14 : 0) {
    rw = mk_rsrc(p.wd + et * 64, FF * DM * 2 - et * 128);
    vw = kn_src(lw, lane, DM * 2); vx = lane * 16;
  }
  template <class S> __device__ __forceinline__ void W(S s) {
    const int ks = s.v + k0 < 14 ? s.v + k0 : s.v + k0 - 14;
    static_for<0, 4>([&](auto i) {
      constexpr int kb = i.v >> 1, n = i.v & 1;
      dma16<0>(rw, lds + wslot<GeoD>(s.v) * 16384 + kb * 8192 + (lw + 4 * n) * 1024, vw, ((2 * ks + kb) * 64 + 32 * n) * DM * 2);
    });
  }
  template <class S> __device__ __forceinline__ void X(S s, rsrc_t rx) {
    const int ks = s.v + k0 < 14 ? s.v + k0 : s.v + k0 - 14;
    static_for<0, 4>([&](auto i) {
      constexpr int blk = i.v >> 1, n = i.v & 1;
      dma16<SC1>(rx, lds + XB + (s.v % 4) * 16384 + blk * 8192 + n * 1024 + lw * 2048, vx, blk * 224 * 512 + (16 * ks + 2 * n) * 512);
    });
  }
  __device__ __forceinline__ rsrc_t xr(const u16* hh, int tb) { return mk_rsrc((const char*)hh + ((size_t)2 * tb * 224 + 4 * lw) * 512, 2 * 224 * 512 - 4 * lw * 512); }
};
template <class G, class MINE, class GN, class NEXT, class LD, class LN>
__device__ __forceinline__ void l_phase(LD& L, rsrc_t rx, LN& N) {
  l_run<G, MINE, GN, NEXT>([&](auto s) { L.X(s, rx); }, [&](auto s) { L.W(s); }, [&](auto s) { N.W(s); });
}

__device__ __forceinline__ void p_compute(lds_t lds, u16* pq, int tb, int et, int w, int lane, const char* t0, const char* t1, bool touch, unsigned& dummy STAMP_ARG) {
  const int r = lane & 31, h = lane >> 5;
  const unsigned a0 = km_read_base(32 * w + r, h);
  const int bl = (lane >> 4) & 1, q4 = (lane & 15) >> 2, pp = ((lane & 1) << 1) | ((lane >> 1) & 1), cc0 = 2 * bl + (pp >> 1);
  const unsigned f0 = cc0 * 512 + (((8 * h + q4) ^ (4 * cc0)) << 4) + 8 * (pp & 1), f1 = f0 ^ 64;
  f32x16 acc0 = {}, acc1 = {};
  unsigned wo = GeoP::OW * 16384, xo = XB;
  struct Fr { bf16x8 tok[4], fa[4], fb[4]; };
  Fr A, B;
  auto rdt = [&](Fr& f, int k) { f.tok[k] = ld_b128(lds + wo + (a0 ^ (k << 5))); };
  auto rda = [&](Fr& f, int k) { lds_t xb = lds + xo + (k >> 1) * 4096 + (k & 1) * 256; f.fa[k] = ld_tr(xb + f0, xb + f1); };
  auto rdb = [&](Fr& f, int k) { lds_t xb = lds + xo + (k >> 1) * 4096 + (k & 1) * 256 + 2048; f.fb[k] = ld_tr(xb + f0, xb + f1); };
  auto adv = [&]() {
    wo = wo + 16384 == 6 * 16384 ? 0 : wo + 16384;
    xo = xo + 8192 == LDS_BYTES ? XB : xo + 8192;
  };
  auto step = [&](Fr& nx, const Fr& cu) {
    cbar();
#pragma unroll
    for (int k = 0; k < 4; ++k) {
      rdt(nx, k); rda(nx, k); SCHED();
      acc0 = mfma(cu.fa[k], cu.tok[k], acc0); SCHED();
      rdb(nx, k); SCHED();
      acc1 = mfma(cu.fb[k], cu.tok[k], acc1); SCHED();
    }
    adv();
  };
  cbar();
  STAMP(1);
#pragma unroll
  for (int k = 0; k < 4; ++k) { rdt(A, k); rda(A, k); rdb(A, k); }
  adv();
#pragma unroll 1
  for (int s = 1; s < 31; s += 2) {
    step(B, A);
    if (touch && s == 7) {
      asm volatile("global_load_dword %0, %1, off" : "+v"(dummy) : "v"(t0) : "memory");
      asm volatile("global_load_dword %0, %1, off" : "+v"(dummy) : "v"(t1) : "memory");
    }
    step(A, B);
  }
  step(B, A);
  cbar();
  STAMP(2);
#pragma unroll
  for (int k = 0; k < 4; ++k) { acc0 = mfma(B.fa[k], B.tok[k], acc0); acc1 = mfma(B.fb[k], B.tok[k], acc1); }
  const float sg = w >= 2 ? -1.f : 1.f;
  const rsrc_t ro = mk_rsrc((char*)pq + ((size_t)(2 * tb + (w & 1)) * 128 + (w >> 1) * 64 + 8 * et) * 512, 4096);
#pragma unroll
  for (int ft = 0; ft < 2; ++ft)
#pragma unroll
    for (int q = 0; q < 2; ++q) {
      const f32x16& a = ft ? acc1 : acc0;
      u32x4 v = {pk2(sg * a[8 * q], sg * a[8 * q + 1]), pk2(sg * a[8 * q + 2], sg * a[8 * q + 3]), pk2(sg * a[8 * q + 4], sg * a[8 * q + 5]), pk2(sg * a[8 * q + 6], sg * a[8 * q + 7])};
      __builtin_amdgcn_raw_buffer_store_b128(v, ro, lane * 16 + ft * 2048 + q * 1024, 0, SC1);
    }
}

__device__ __forceinline__ void add_res(const f32x16& acc, u32x4& r0, u32x4& r1) {
#pragma unroll
  for (int i = 0; i < 4; ++i) {
    r0[i] = pk2(acc[2 * i] + bflo(r0[i]), acc[2 * i + 1] + bfhi(r0[i]));
    r1[i] = pk2(acc[8 + 2 * i] + bflo(r1[i]), acc[8 + 2 * i + 1] + bfhi(r1[i]));
  }
}
__device__ __forceinline__ void store_rowmajor(u16* xo, const u32x4& o0, const u32x4& o1, int row0, int col0, int lane) {
  const rsrc_t ro = mk_rsrc(xo + (size_t)row0 * DM + col0, 32 * DM * 2);
  const unsigned vo = (lane & 31) * DM * 2 + (lane >> 5) * 16;
  __builtin_amdgcn_raw_buffer_store_b128(o0, ro, vo, 0, 0);
  __builtin_amdgcn_raw_buffer_store_b128(o1, ro, vo + 32, 0, 0);
}
__device__ __forceinline__ void store_blocked(u16* xo, const u32x4& o0, const u32x4& o1, int row0, int col0, int lane) {
  const rsrc_t ro = mk_rsrc((char*)xo + ((size_t)(row0 >> 5) * 64 + (col0 >> 3)) * 512, 2048);
  __builtin_amdgcn_raw_buffer_store_b128(o0, ro, lane * 16, 0, SC1);
  __builtin_amdgcn_raw_buffer_store_b128(o1, ro, lane * 16 + 1024, 0, SC1);
}
__device__ __forceinline__ void part_stats(const u32x4& o0, const u32x4& o1, float (&v)[16], float& m, float& m2) {
  float s0 = 0.f, s1 = 0.f;
#pragma unroll
  for (int i = 0; i < 4; ++i) { v[2 * i] = bflo(o0[i]); v[2 * i + 1] = bfhi(o0[i]); v[8 + 2 * i] = bflo(o1[i]); v[8 + 2 * i + 1] = bfhi(o1[i]); }
#pragma unroll
  for (int i = 0; i < 8; ++i) { s0 += v[i]; s1 += v[8 + i]; }
  m = xhalf_sum(s0 + s1) * (1.f / 32.f);
  float q0 = 0.f, q1 = 0.f;
#pragma unroll
  for (int i = 0; i < 8; ++i) { float d = v[i] - m, e = v[8 + i] - m; q0 += d * d; q1 += e * e; }
  m2 = xhalf_sum(q0 + q1);
}
__device__ __forceinline__ void store_stats(float* stats, float m, float m2, int row0, int col0, int lane) {
  const rsrc_t rs = mk_rsrc(stats + ((size_t)(col0 >> 5) * SEQ + row0) * 2, 32 * 8);
  if (lane < 32) { u32x2 sv = {__float_as_uint(m), __float_as_uint(m2)}; __builtin_amdgcn_raw_buffer_store_b64(sv, rs, lane * 8, 0, SC1); }
}
__device__ __forceinline__ void load_stats(const float* stats, int row0, int lane, u32x2 (&t)[8]) {
  gu64* q = (gu64*)stats + row0 + (lane & 31) + (size_t)(8 * (lane >> 5)) * SEQ;
#pragma unroll
  for (int i = 0; i < 8; ++i) { unsigned long long x = __hip_atomic_load(q + i * SEQ, RLX_AGENT); t[i] = u32x2{(unsigned)x, (unsigned)(x >> 32)}; }
}
__device__ __forceinline__ void combine_stats(const u32x2 (&t)[8], float& mean, float& rstd) {
  float ms = 0.f, m2 = 0.f;
#pragma unroll
  for (int i = 0; i < 8; ++i) { ms += __uint_as_float(t[i][0]); m2 += __uint_as_float(t[i][1]); }
  mean = xhalf_sum(ms) * (1.f / 16.f);
#pragma unroll
  for (int i = 0; i < 8; ++i) { float d = __uint_as_float(t[i][0]) - mean; m2 += 32.f * d * d; }
  rstd = rsqrtf(xhalf_sum(m2) * (1.f / 512.f) + 1e-5f);
}

template <class G, class FF_> __device__ __forceinline__ f32x16 tile64_loop(lds_t lds, unsigned a0, FF_ feat, int w, int lane STAMP_ARG) {
  f32x16 acc = {};
  unsigned wo = G::OW * 16384, xo = XB;
  struct Fr { bf16x8 tok[8], f[8]; };
  Fr A, B;
  auto rd = [&](Fr& fr, int k) {
    fr.tok[k] = ld_b128(lds + xo + a0 + k * 1024);
    fr.f[k] = feat(lds + wo, k);
  };
  auto adv = [&]() {
    wo = wo + 16384 == 6 * 16384 ? 0 : wo + 16384;
    xo = xo + 16384 == LDS_BYTES ? XB : xo + 16384;
  };
  auto step = [&](Fr& nx, const Fr& cu) {
    cbar();
#pragma unroll
    for (int k = 0; k < 8; ++k) { rd(nx, k); SCHED(); acc = mfma(cu.f[k], cu.tok[k], acc); SCHED(); }
    adv();
  };
  cbar();
  STAMP(1);
#pragma unroll
  for (int k = 0; k < 8; ++k) rd(A, k);
  adv();
#pragma unroll 1
  for (int s = 1; s < G::NST - 1; s += 2) { step(B, A); step(A, B); }
  step(B, A);
  cbar();
  STAMP(2);
#pragma unroll
  for (int k = 0; k < 8; ++k) acc = mfma(B.f[k], B.tok[k], acc);
  return acc;
}

__device__ __forceinline__ void c_compute(lds_t lds, const float* stats, const unsigned* csf, u16* hh, int rb, int fc, int w, int lane STAMP_ARG) {
  const int r = lane & 31, h = lane >> 5;
  const unsigned a0 = w * 4096 + lane * 16;
  const int blk = (lane >> 4) & 1, q = (lane & 15) >> 2, pp = ((lane & 1) << 1) | ((lane >> 1) & 1);
  const unsigned f0 = (8 * h + q) * 448 + blk * 224 + 8 * pp;
  f32x16 acc[7];
#pragma unroll
  for (int j = 0; j < 7; ++j) acc[j] = f32x16{};
  unsigned wo = GeoC::OW * C_WS, xo = XB;
  struct Fr { bf16x8 tok, f[7]; };
  Fr A, B;
  auto rdt = [&](Fr& fr, int k) { fr.tok = ld_b128(lds + xo + a0 + k * 1024); };
  auto rdf = [&](Fr& fr, int k, int j) { lds_t wb = lds + wo + f0; fr.f[j] = ld_tr(wb + 16 * k * 448 + 32 * j, wb + (16 * k + 4) * 448 + 32 * j); };
  auto grp = [&](Fr& nx, const Fr& cu, int k, bool rdn) {
#pragma unroll
    for (int j = 0; j < 7; ++j) {
      if (rdn) { if (j == 0) rdt(nx, k); rdf(nx, k, j); }
      SCHED();
      acc[j] = mfma(cu.f[j], cu.tok, acc[j]);
      SCHED();
    }
  };
  auto adv = [&]() {
    wo = wo + C_WS == 3 * C_WS ? 0 : wo + C_WS;
    xo = xo + 16384 == LDS_BYTES ? XB : xo + 16384;
  };
  cbar();
  STAMP(1);
  u32x2 st[8];
  load_stats(stats, 128 * rb + 32 * w, lane, st);
  unsigned long long cs[7];
#pragma unroll
  for (int j = 0; j < 7; ++j) cs[j] = __hip_atomic_load((gu64*)csf + ((size_t)fc * 32 + r) * 8 + j, __ATOMIC_RELAXED, __HIP_MEMORY_SCOPE_WORKGROUP);
  rdt(A, 0);
#pragma unroll
  for (int j = 0; j < 7; ++j) rdf(A, 0, j);
  auto stage = [&]() {
    grp(B, A, 1, true);
    grp(A, B, 2, true);
    grp(B, A, 3, true);
    adv();
    cbar();
    grp(A, B, 0, true);
  };
  stage();
  float mean, rstd;
  combine_stats(st, mean, rstd);
  {
    const unsigned mh = pk2(-mean, 0.f) & 0xffffu;
    const unsigned ml = pk2(-mean - bflo(mh), 0.f) & 0xffffu;
    u32x4 bt = {h ? 0u : (mh | (ml << 16)), h ? 0u : mh, 0u, 0u};
#pragma unroll
    for (int j = 0; j < 7; ++j) {
      u32x4 at = {h ? 0u : (unsigned)cs[j], h ? 0u : (unsigned)(cs[j] >> 32), 0u, 0u};
      acc[j] = mfma(__builtin_bit_cast(bf16x8, at), __builtin_bit_cast(bf16x8, bt), acc[j]);
    }
  }
  SCHED();
#pragma unroll 1
  for (int s = 1; s < 7; ++s) stage();
  grp(B, A, 1, true);
  grp(A, B, 2, true);
  grp(B, A, 3, true);
  cbar();
  STAMP(2);
  grp(A, B, 0, false);
  const rsrc_t ro = mk_rsrc((char*)hh + ((size_t)(4 * rb + w) * 224 + 14 * fc) * 512, 7168);
  const float ng = -1.4426950408889634f * rstd;
#pragma unroll
  for (int j = 0; j < 7; ++j) {
    float o[8];
#pragma unroll
    for (int i = 0; i < 8; i += 2) {
      f32x2 g = {acc[j][i], acc[j][i + 1]}, u = {acc[j][8 + i], acc[j][9 + i]};
      g *= ng; u *= rstd;
      f32x2 e = {__builtin_amdgcn_exp2f(g[0]), __builtin_amdgcn_exp2f(g[1])};
      e += 1.f;
      f32x2 sg = {__builtin_amdgcn_rcpf(e[0]), __builtin_amdgcn_rcpf(e[1])};
      u *= sg;
      o[i] = u[0]; o[i + 1] = u[1];
    }
    u32x4 v = {pk2(o[0], o[1]), pk2(o[2], o[3]), pk2(o[4], o[5]), pk2(o[6], o[7])};
    __builtin_amdgcn_raw_buffer_store_b128(v, ro, lane * 16 + 1024 * j, 0, SC1);
    SCHED();
  }
}

__global__ __launch_bounds__(512, 2) __attribute__((amdgpu_waves_per_eu(2, 2))) void k_main(Ptrs p) {
  extern __shared__ __attribute__((aligned(16))) char smem[];
  lds_t lds = (lds_t)smem;
  const int lane = threadIdx.x & 63, wid = __builtin_amdgcn_readfirstlane(threadIdx.x >> 6);
  const int xcd = blockIdx.x & 7, j = blockIdx.x >> 3;
  const int tb = xcd * 4 + (j & 3), et = j >> 2;
  const int rb = (xcd & 1) * 8 + (j & 7), fc = (xcd >> 1) * 4 + (j >> 3);
  SPAN_MAIN();
  if (wid >= 4) {
    {
      PLoader L(lds, p, fresh_s(tb), fresh_s(et), fresh_s(wid - 4), fresh_v(lane));
      YLoader N(lds, p, fresh_s(et), fresh_s(wid - 4), fresh_v(lane));
      l_phase<GeoP, EarlyNone, GeoY, EarlyY>(L, L.xr(p.xn, et), N);
    }
#pragma unroll 1
    for (int l = 0; l < 2; ++l) {
      {
        YLoader L(lds, p, fresh_s(et), fresh_s(wid - 4), fresh_v(lane));
        CLoader N(lds, p, fresh_s(fc), fresh_s(wid - 4), fresh_v(lane));
        const rsrc_t rx = L.xr(p.pq + l * PQ_L, fresh_s(tb));
        bar(); bar();
        l_phase<GeoY, EarlyY, GeoC, EarlyC>(L, rx, N);
      }
      {
        CLoader L(lds, p, fresh_s(fc), fresh_s(wid - 4), fresh_v(lane));
        DLoader N(lds, p, fresh_s(tb), fresh_s(et), fresh_s(wid - 4), fresh_v(lane));
        const rsrc_t rx = L.xr(p.xa + l * XN_L, fresh_s(rb));
        bar(); bar();
        l_phase<GeoC, EarlyC, GeoD, EarlyD>(L, rx, N);
      }
      if (l == 0) {
        {
          DLoader L(lds, p, fresh_s(tb), fresh_s(et), fresh_s(wid - 4), fresh_v(lane));
          PLoader N(lds, p, fresh_s(tb), fresh_s(et), fresh_s(wid - 4), fresh_v(lane));
          const rsrc_t rx = L.xr(p.hh, fresh_s(tb));
          bar(); bar();
          l_phase<GeoD, EarlyD, GeoP, EarlyP>(L, rx, N);
        }
        PLoader L(lds, p, fresh_s(tb), fresh_s(et), fresh_s(wid - 4), fresh_v(lane));
        YLoader N(lds, p, fresh_s(et), fresh_s(wid - 4), fresh_v(lane));
        const rsrc_t rx = L.xr(p.xn + XN_L, fresh_s(et));
        bar(); bar();
        l_phase<GeoP, EarlyP, GeoY, EarlyY>(L, rx, N);
      } else {
        DLoader L(lds, p, fresh_s(tb), fresh_s(et), fresh_s(wid - 4), fresh_v(lane));
        const rsrc_t rx = L.xr(p.hh + HH_L, fresh_s(tb));
        bar(); bar();
        l_phase<GeoD, EarlyD, GeoP, EarlyNone>(L, rx, L);
      }
    }
  } else {
    const int w = wid, tw = w & 1, fw = w >> 1;
    const int row0 = 64 * tb + 32 * tw, col0 = 64 * et + 32 * fw;
    unsigned dummy = 0;
    u32x4 r0, r1;
    {
      const int r = lane & 31, h = lane >> 5;
      const rsrc_t rr = mk_rsrc(p.x0 + (size_t)row0 * DM + col0, 32 * DM * 2);
      r0 = __builtin_amdgcn_raw_buffer_load_b128(rr, r * DM * 2 + h * 16, 0, 0);
      r1 = __builtin_amdgcn_raw_buffer_load_b128(rr, r * DM * 2 + h * 16 + 32, 0, 0);
    }
#pragma unroll 1
    for (int l = 0; l < 2; ++l) {
      const int sb = 4 * l;
      {
        STAMP_PTR(4 * l + 0);
        STAMP(0);
        const int ln = fresh_v(lane), li = blockIdx.x * 64 + ln, lj = blockIdx.x * 32 + (ln & 31);
        const char* t0 = li < 14336 ? (const char*)p.wd + (size_t)li * 128 : (const char*)p.wd;
        const char* t1 = (ln < 32 ? (const char*)p.dre : (const char*)p.dim) + (size_t)(lj & 4095) * 128;
        p_compute(lds, p.pq + l * PQ_L, fresh_s(tb), fresh_s(et), fresh_s(w), fresh_v(lane), t0, t1, l == 0 && w == 0 && blockIdx.x < 224, dummy STAMP_PASS(4 * l + 0));
        STAMP(5); WAIT_V(0); STAMP(6); bar();
        asm volatile("" :: "v"(dummy));
        if (w == 0) { signal(p.mbox, sb + 0, tb * 8 + (lane & 7), et, lane < 8); await(p.mbox, p.tmo, sb + 0, tb * 8 + et, 8, lane); }
        bar(); STAMP(7);
      }
      {
        STAMP_PTR(4 * l + 1);
        STAMP(0);
        const int ln = fresh_v(lane), r = ln & 31, h = ln >> 5;
        const unsigned a0 = tw * 8192 + ln * 16, b0 = km_read_base(32 * fw + perm23(r), h);
        f32x16 acc = tile64_loop<GeoY>(lds, a0, [&](lds_t wb, int k) { return ld_b128(wb + (k >> 2) * 8192 + (b0 ^ ((k & 3) << 5))); }, w, lane STAMP_PASS(4 * l + 1));
        add_res(acc, r0, r1);
        store_blocked(p.xa + l * XN_L, r0, r1, row0, col0, fresh_v(lane));
        float v[16], m, m2;
        part_stats(r0, r1, v, m, m2);
        store_stats(p.stats + l * ST_L, m, m2, row0, col0, fresh_v(lane));
        STAMP(5); WAIT_V(0); STAMP(6); bar();
        if (w == 0) { signal(p.mbox, sb + 1, (tb >> 1) * 16 + (lane & 15), (tb & 1) * 8 + et, lane < 16); await(p.mbox, p.tmo, sb + 1, rb * 16 + fc, 16, lane); }
        bar(); STAMP(7);
      }
      {
        STAMP_PTR(4 * l + 2);
        STAMP(0);
        c_compute(lds, p.stats + l * ST_L, p.csf, p.hh + l * HH_L, fresh_s(rb), fresh_s(fc), fresh_s(w), fresh_v(lane) STAMP_PASS(4 * l + 2));
        STAMP(5); WAIT_V(0); STAMP(6); bar();
        if (w == 0) { signal(p.mbox, sb + 2, (2 * rb + ((lane >> 3) & 1)) * 8 + (lane & 7), fc, lane < 16); await(p.mbox, p.tmo, sb + 2, tb * 8 + et, 16, lane); }
        bar(); STAMP(7);
      }
      {
        STAMP_PTR(4 * l + 3);
        STAMP(0);
        const int ln = fresh_v(lane);
        const unsigned a0 = tw * 8192 + ln * 16, f0 = kn_read_base(ln, fw);
        f32x16 acc = tile64_loop<GeoD>(lds, a0, [&](lds_t wb, int k) { return ld_tr(wb + f0 + k * 2048, wb + f0 + k * 2048 + 512); }, w, lane STAMP_PASS(4 * l + 3));
        add_res(acc, r0, r1);
        if (l == 1) {
          store_rowmajor(p.out, r0, r1, row0, col0, fresh_v(lane));
          if (w == 0) SPAN_END();
        } else {
          float v[16], m, m2;
          part_stats(r0, r1, v, m, m2);
          const int le = fresh_v(lane);
          store_stats((float*)p.gran, m, m2, row0, col0, le);
          STAMP(3);
          u32x2 st[8];
          for (unsigned spins = 0;; ++spins) {
            load_stats((const float*)p.gran, row0, le, st);
            bool ok = true;
#pragma unroll
            for (int i = 0; i < 8; ++i) ok = ok && st[i][1] != 0xffffffffu;
            if (__all(ok)) break;
            if (spins > SPIN_CAP) { if (lane == 0) __hip_atomic_store((gu32*)p.tmo, 2u, RLX_AGENT); break; }
            __builtin_amdgcn_s_sleep(1);
          }
          STAMP(4);
          float mean, rstd;
          combine_stats(st, mean, rstd);
          u32x4 n0, n1;
#pragma unroll
          for (int i = 0; i < 4; ++i) {
            n0[i] = pk2((v[2 * i] - mean) * rstd, (v[2 * i + 1] - mean) * rstd);
            n1[i] = pk2((v[8 + 2 * i] - mean) * rstd, (v[8 + 2 * i + 1] - mean) * rstd);
          }
          store_blocked(p.xn + XN_L, n0, n1, row0, col0, le);
          STAMP(5); WAIT_V(0); STAMP(6); bar();
          if (w == 0) { signal(p.mbox, 3, (lane & 31) * 8 + et, tb, lane < 32); await(p.mbox, p.tmo, 3, tb * 8 + et, 32, lane); }
          bar(); STAMP(7);
        }
      }
    }
  }
}

__device__ __forceinline__ float wave_sum(float v) {
#pragma unroll
  for (int o = 1; o < 64; o <<= 1) v += __shfl_xor(v, o);
  return v;
}
__global__ __launch_bounds__(512) void k_pre(Ptrs p) {
  const int lane = threadIdx.x & 63, wid = threadIdx.x >> 6;
  if (blockIdx.x < 256) {
    SPAN_BEGIN();
    const int row = blockIdx.x * 8 + wid;
    u32x4 t = *(const u32x4*)(p.x0 + (size_t)row * DM + lane * 8);
    const int gid = blockIdx.x * 512 + threadIdx.x;
    if (gid < 7 * 256 * 8) ((u32x4*)p.mbox)[gid] = u32x4{0u, 0u, 0u, 0u};
    if (gid < 16 * SEQ / 2) ((u32x4*)p.gran)[gid] = u32x4{~0u, ~0u, ~0u, ~0u};
    if (gid == 0) *p.tmo = 0u;
    float v[8], s = 0.f;
#pragma unroll
    for (int i = 0; i < 4; ++i) { v[2 * i] = bflo(t[i]); v[2 * i + 1] = bfhi(t[i]); s += v[2 * i] + v[2 * i + 1]; }
    const float m = wave_sum(s) * (1.f / 512.f);
    float m2 = 0.f;
#pragma unroll
    for (int i = 0; i < 8; ++i) { v[i] -= m; m2 += v[i] * v[i]; }
    const float rs = rsqrtf(wave_sum(m2) * (1.f / 512.f) + 1e-5f);
    u32x4 o = {pk2(v[0] * rs, v[1] * rs), pk2(v[2] * rs, v[3] * rs), pk2(v[4] * rs, v[5] * rs), pk2(v[6] * rs, v[7] * rs)};
    *(u32x4*)((char*)p.xn + ((size_t)(row >> 5) * 64 + lane) * 512 + (row & 31) * 16) = o;
  } else {
    __shared__ float red[8][64];
    const int b = blockIdx.x - 256;
    const u16* w = (b < 28 ? p.wg : p.wu) + (b % 28) * 64 + (lane & 7) * 8;
    float a[8] = {};
#pragma unroll
    for (int i = 0; i < 8; ++i) {
      u32x4 t = *(const u32x4*)(w + (size_t)(i * 64 + wid * 8 + (lane >> 3)) * FF);
#pragma unroll
      for (int e = 0; e < 4; ++e) { a[2 * e] += bflo(t[e]); a[2 * e + 1] += bfhi(t[e]); }
    }
#pragma unroll
    for (int e = 0; e < 8; ++e) { a[e] += __shfl_xor(a[e], 8); a[e] += __shfl_xor(a[e], 16); a[e] += __shfl_xor(a[e], 32); }
    if (lane < 8) {
#pragma unroll
      for (int e = 0; e < 8; ++e) red[wid][lane * 8 + e] = a[e];
    }
    __syncthreads();
    if (threadIdx.x < 64) {
      float s = 0.f;
#pragma unroll
      for (int i = 0; i < 8; ++i) s += red[i][threadIdx.x];
      const int c = (b % 28) * 64 + threadIdx.x, fc = c / 112, rem = c - fc * 112, jj = rem >> 4, i = perm23(rem & 15) + (b < 28 ? 0 : 16);
      const unsigned hi = pk2(s, 0.f) & 0xffffu, lo = pk2(s - bflo(hi), 0.f) & 0xffffu;
      u32x2 o = {hi | (hi << 16), lo};
      *(u32x2*)(p.csf + ((size_t)fc * 32 + i) * 16 + 2 * jj) = o;
    }
  }
}
}

extern "C" void kernel_launch(void* const* d_in, const int* in_sizes, int n_in, void* d_out, int out_size, void* d_ws, size_t ws_size, hipStream_t stream) {
  Ptrs p;
  p.x0 = (const u16*)d_in[0]; p.sre = (const u16*)d_in[1]; p.sim = (const u16*)d_in[2]; p.dre = (const u16*)d_in[3]; p.dim = (const u16*)d_in[4];
  p.wg = (const u16*)d_in[6]; p.wu = (const u16*)d_in[7]; p.wd = (const u16*)d_in[8];
  char* ws = (char*)d_ws;
  const size_t MB = 1 << 20;
  p.out = (u16*)d_out;
  p.xn = (u16*)ws;
  p.pq = (u16*)(ws + 4 * MB);
  p.xa = (u16*)(ws + 12 * MB);
  p.hh = (u16*)(ws + 16 * MB);
  p.stats = (float*)(ws + 31 * MB);
  p.stamps = (unsigned long long*)(ws + 32 * MB);
  p.csf = (unsigned*)(ws + 33 * MB);
  p.gran = (unsigned*)(ws + 34 * MB);
  p.mbox = (unsigned*)(ws + 35 * MB);
  p.tmo = (unsigned*)(ws + 36 * MB);
  (void)hipFuncSetAttribute((const void*)k_main, hipFuncAttributeMaxDynamicSharedMemorySize, LDS_BYTES);
  k_pre<<<256 + 56, 512, 0, stream>>>(p);
  k_main<<<256, 512, LDS_BYTES, stream>>>(p);
}
```

```cpp
#include <hip/hip_runtime.h>
#include <stdint.h>

namespace {
typedef unsigned short u16;
typedef short s16x4 __attribute__((ext_vector_type(4)));
typedef short s16x8 __attribute__((ext_vector_type(8)));
typedef __bf16 bf16x8 __attribute__((ext_vector_type(8)));
typedef __bf16 bf16x2 __attribute__((ext_vector_type(2)));
typedef float f32x2 __attribute__((ext_vector_type(2)));
typedef float f32x4 __attribute__((ext_vector_type(4)));
typedef float f32x16 __attribute__((ext_vector_type(16)));
typedef unsigned u32x4 __attribute__((ext_vector_type(4)));
typedef unsigned u32x2 __attribute__((ext_vector_type(2)));
#define LAS __attribute__((address_space(3)))
typedef __amdgpu_buffer_rsrc_t rsrc_t;
typedef LAS char* lds_t;
typedef __attribute__((address_space(1))) unsigned gu32;
typedef __attribute__((address_space(1))) unsigned long long gu64;
#define RLX_AGENT __ATOMIC_RELAXED, __HIP_MEMORY_SCOPE_AGENT

constexpr int SEQ = 2048, DM = 512, FF = 1792;
constexpr int LDS_BYTES = 163840, XB = 98304;
constexpr int SC1 = 16;
constexpr unsigned SPIN_CAP = 1u << 18;

__device__ __forceinline__ rsrc_t mk_rsrc(const void* p, unsigned bytes) { return __builtin_amdgcn_make_buffer_rsrc((void*)p, 0, bytes, 0x00020000); }
template <int AUX> __device__ __forceinline__ void dma16(rsrc_t rs, lds_t dst, unsigned voff, unsigned soff) {
  __builtin_amdgcn_raw_ptr_buffer_load_lds(rs, (LAS void*)dst, 16, voff, soff, 0, AUX);
}
#define WAIT_V(n) asm volatile("s_waitcnt vmcnt(%0)" ::"n"(n) : "memory")
#define SCHED() __builtin_amdgcn_sched_barrier(0)
#define WAIT_L0() __builtin_amdgcn_s_waitcnt(0xC07F)
__device__ __forceinline__ void bar() { asm volatile("" ::: "memory"); __builtin_amdgcn_s_barrier(); asm volatile("" ::: "memory"); }
__device__ __forceinline__ void cbar() { WAIT_L0(); bar(); }
__device__ __forceinline__ int fresh_v(int x) { asm volatile("" : "+v"(x)); return x; }
__device__ __forceinline__ int fresh_s(int x) { asm volatile("" : "+s"(x)); return x; }

template <int I> struct IC { static constexpr int v = I; };
template <int A, int B, class F> __device__ __forceinline__ void static_for(F&& f) {
  if constexpr (A < B) { f(IC<A>{}); static_for<A + 1, B>(f); }
}

#define PXI 3
#define ROT 8
#define ROT2 0
#define ROTD 1
struct GeoP { static constexpr int NST = 32, NX = 8, nX = 2, NW = 6, nW = 4, OW = 0; };
struct GeoY { static constexpr int NST = 8,  NX = 4, nX = 4, NW = 6, nW = 4, OW = 4; };
struct GeoC { static constexpr int NST = 8,  NX = 4, nX = 4, NW = 3, nW = 7, OW = 0; };
struct GeoD { static constexpr int NST = 14, NX = 4, nX = 4, NW = 6, nW = 4, OW = 4; };
#define ENDY 32
#define ENDC 8
#define ENDD 8
#define ENDP 14
struct EarlyNone { static constexpr int at(int) { return -1; } };
struct EarlyY { static constexpr int at(int j) { constexpr int t[6] = {29, 30, 31, ENDY, 27, 28}; return j < 6 ? t[j] : -1; } };
struct EarlyC { static constexpr int at(int j) { constexpr int t[3] = {4, 6, ENDC}; return j < 3 ? t[j] : -1; } };
struct EarlyD { static constexpr int at(int j) { constexpr int t[6] = {6, 7, 7, ENDD, ENDD, ENDD}; return j < 6 ? t[j] : -1; } };
struct EarlyP { static constexpr int at(int j) { constexpr int t[6] = {9, 10, 11, 12, 13, ENDP}; return j < 6 ? t[j] : -1; } };

constexpr int PLAN_MAX = 40;
template <class G, class MINE, class GN, class NEXT> struct Plan {
  int xstep[PLAN_MAX], wstep[PLAN_MAX], waitn[PLAN_MAX];
  constexpr Plan() : xstep{}, wstep{}, waitn{} {
    int nx = 0;
    for (int j = 0; j < G::NST; ++j) { xstep[j] = -2; wstep[j] = MINE::at(j) >= 0 ? -3 : -2; }
    for (int s = -1; s < G::NST; ++s) {
      const int lx = s - 1 + G::NX < G::NX - 1 ? G::NX - 1 : s - 1 + G::NX, lw = s - 1 + G::NW < G::NW - 1 ? G::NW - 1 : s - 1 + G::NW;
      for (int c = 0; c < (s < 0 ? PXI : 2); ++c) if (nx < G::NST && nx <= lx) xstep[nx++] = s;
      int c = 0;
      for (int j = 0; j < G::NST; ++j)
        if (wstep[j] == -2 && j <= lw && (s < 0 ? (j < PXI) : c < 2)) { wstep[j] = s; ++c; }
    }
    for (int s = 0; s < G::NST; ++s) {
      int total = 0, last = 0;
      for (int b = 0; b < PLAN_MAX; ++b)
        for (int j = 0; j < G::NST; ++j) if (MINE::at(j) == b) { total += G::nW; if (j == s) last = total; }
      for (int t = -1; t < s; ++t) {
        for (int j = 0; j < G::NST; ++j) {
          if (xstep[j] == t) { total += G::nX; if (j == s) last = total; }
          if (t < 0 && wstep[j] == t) { total += G::nW; if (j == s) last = total; }
        }
        if (t >= 0) {
          for (int j = 0; j < G::NST; ++j) if (wstep[j] == t) { total += G::nW; if (j == s) last = total; }
          for (int j = 0; j < GN::NST; ++j) if (NEXT::at(j) == t) total += GN::nW;
        }
      }
      waitn[s] = total - last > 63 ? 63 : total - last;
    }
  }
  constexpr bool ok() const {
    for (int j = 0; j < G::NST; ++j) if (xstep[j] >= j || xstep[j] < -1 || wstep[j] >= j || wstep[j] == -2) return false;
    return true;
  }
};
template <class G, class MINE, class GN, class NEXT> struct PlanOf { static constexpr Plan<G, MINE, GN, NEXT> v{}; };
template <class G, class MINE, class GN, class NEXT, class FX, class FW, class FN>
__device__ __forceinline__ void l_run(FX issueX, FW issueW, FN issueNext) {
  using PL = PlanOf<G, MINE, GN, NEXT>;
  static_assert(PL::v.ok(), "a stage would be waited for before it is issued");
  static_for<0, G::NST>([&](auto j) {
    if constexpr (PL::v.xstep[j.v] == -1) issueX(j);
    if constexpr (PL::v.wstep[j.v] == -1) issueW(j);
  });
  static_for<0, G::NST>([&](auto s) {
    WAIT_V(PL::v.waitn[s.v]);
    bar();
    static_for<0, G::NST>([&](auto j) { if constexpr (PL::v.xstep[j.v] == s.v) issueX(j); });
    static_for<0, G::NST>([&](auto j) { if constexpr (PL::v.wstep[j.v] == s.v) issueW(j); });
    static_for<0, GN::NST>([&](auto j) { if constexpr (NEXT::at(j.v) == s.v) issueNext(j); });
  });
  bar();
  static_for<0, GN::NST>([&](auto j) { if constexpr (NEXT::at(j.v) == G::NST) issueNext(j); });
}
template <class G> constexpr int wslot(int j) { return (G::OW + j) % G::NW; }

__device__ __forceinline__ bf16x8 ld_b128(lds_t p) { return *(LAS bf16x8*)p; }
__device__ __forceinline__ bf16x8 ld_tr(lds_t p0, lds_t p1) {
  s16x4 a = __builtin_amdgcn_ds_read_tr16_b64_v4i16((LAS s16x4*)p0);
  s16x4 b = __builtin_amdgcn_ds_read_tr16_b64_v4i16((LAS s16x4*)p1);
  s16x8 f = __builtin_shufflevector(a, b, 0, 1, 2, 3, 4, 5, 6, 7);
  return __builtin_bit_cast(bf16x8, f);
}
__device__ __forceinline__ f32x16 mfma(bf16x8 a, bf16x8 b, f32x16 c) { return __builtin_amdgcn_mfma_f32_32x32x16_bf16(a, b, c, 0, 0, 0); }
__device__ __forceinline__ unsigned pk2(float a, float b) { f32x2 v = {a, b}; return __builtin_bit_cast(unsigned, __builtin_convertvector(v, bf16x2)); }
__device__ __forceinline__ float bflo(unsigned u) { return __uint_as_float(u << 16); }
__device__ __forceinline__ float bfhi(unsigned u) { return __uint_as_float(u & 0xffff0000u); }
__device__ __forceinline__ float xhalf_sum(float v) {
  auto r = __builtin_amdgcn_permlane32_swap(__float_as_uint(v), __float_as_uint(v), false, false);
  return __uint_as_float(r[0]) + __uint_as_float(r[1]);
}
__device__ __forceinline__ unsigned km_read_base(int row, int h) { return row * 128 + ((h ^ ((row >> 1) & 7)) << 4); }
__device__ __forceinline__ int perm23(int i) { return (i & ~12) | ((i & 4) << 1) | ((i & 8) >> 1); }
__device__ __forceinline__ unsigned km_src(int lw, int lane, int pitch) {
  return (8 * lw + (lane >> 3)) * pitch + (((lane & 7) ^ (((lw & 1) << 2) | (lane >> 4))) << 4);
}
__device__ __forceinline__ unsigned kn_src(int lw, int lane, int pitch) {
  return (8 * lw + (lane >> 3)) * pitch + (((lane & 7) ^ (((lane >> 4) & 1) << 2)) << 4);
}
__device__ __forceinline__ unsigned kn_read_base(int lane, int ft) {
  int h = lane >> 5, blk = (lane >> 4) & 1, q = (lane & 15) >> 2, p = lane & 3, pp = ((p & 1) << 1) | (p >> 1);
  int c = 4 * ft + 2 * blk + (pp >> 1);
  return (8 * h + q) * 128 + ((c ^ (((q >> 1) & 1) << 2)) << 4) + 8 * (pp & 1);
}

#define SPAN_BEGIN()
#define SPAN_MAIN()
#define SPAN_END()
#define STAMP(i)
#define STAMP_ARG
#define STAMP_PASS(ph)
#define STAMP_PTR(ph)

#define PROBE 9
#define NOW() __builtin_amdgcn_s_memrealtime()
struct Ptrs {
  unsigned long long* stamps;
  const u16 *x0, *sre, *sim, *dre, *dim, *wg, *wu, *wd;
  u16 *out, *xn, *pq, *xa, *hh;
  float* stats;
  unsigned *csf, *gran, *mbox, *tmo;
};
constexpr size_t XN_L = (size_t)SEQ * DM, PQ_L = (size_t)SEQ * 1024, HH_L = (size_t)SEQ * FF, ST_L = (size_t)16 * SEQ * 2;

__device__ __forceinline__ void signal(unsigned* mbox, int seam, int consumer, int slot, bool active) {
  if (active) __hip_atomic_store((gu32*)mbox + ((size_t)seam * 256 + consumer) * 32 + slot, 1u, RLX_AGENT);
}
__device__ __forceinline__ void await(unsigned* mbox, unsigned* tmo, int seam, int me, int nprod, int lane) {
  gu32* q = (gu32*)mbox + ((size_t)seam * 256 + me) * 32 + (lane < nprod ? lane : 0);
  for (unsigned spins = 0;; ++spins) {
    unsigned x = __hip_atomic_load(q, RLX_AGENT);
    if (__all(x == 1u)) break;
    if (spins > SPIN_CAP) { if (lane == 0) __hip_atomic_store((gu32*)tmo, 1u, RLX_AGENT); break; }
    __builtin_amdgcn_s_sleep(1);
  }
}

struct PLoader {
  rsrc_t rre, rim; unsigned vw, vx; lds_t lds; int lw, k0;
  __device__ __forceinline__ PLoader(lds_t lds_, const Ptrs& p, int tb, int et, int lw_, int lane, int rot) : lds(lds_), lw(lw_), k0((rot * et) & 31) {
    rre = mk_rsrc(p.sre + (size_t)tb * 64 * SEQ, 64 * SEQ * 2); rim = mk_rsrc(p.sim + (size_t)tb * 64 * SEQ, 64 * SEQ * 2);
    vw = km_src(lw, lane, SEQ * 2);
    vx = (lane >> 5) * 512 + (((lane & 31) ^ (4 * ((2 * lw + (lane >> 5)) & 3))) << 4);
  }
  template <class S> __device__ __forceinline__ void W(S s) {
    const int ks = (s.v + k0) & 31;
    static_for<0, 4>([&](auto n) { dma16<0>(n.v < 2 ? rre : rim, lds + wslot<GeoP>(s.v) * 16384 + (lw + 4 * n.v) * 1024, vw, ks * 128 + (n.v & 1) * 32 * SEQ * 2); });
  }
  template <class S> __device__ __forceinline__ void X(S s, rsrc_t rx) {
    const int ks = (s.v + k0) & 31;
    static_for<0, 2>([&](auto n) { dma16<SC1>(rx, lds + XB + (s.v % 8) * 8192 + n.v * 4096 + lw * 1024, vx, (2 * ks + n.v) * 64 * 512); });
  }
  __device__ __forceinline__ rsrc_t xr(const u16* xn, int et) { return mk_rsrc((const char*)xn + (8 * et + 2 * lw) * 512, SEQ * DM * 2 - (8 * et + 2 * lw) * 512); }
};
struct YLoader {
  rsrc_t rre, rim; unsigned vw, vx; lds_t lds; int lw;
  __device__ __forceinline__ YLoader(lds_t lds_, const Ptrs& p, int et, int lw_, int lane) : lds(lds_), lw(lw_) {
    rre = mk_rsrc(p.dre + (size_t)et * 64 * DM, 64 * DM * 2); rim = mk_rsrc(p.dim + (size_t)et * 64 * DM, 64 * DM * 2);
    vw = km_src(lw, lane, DM * 2); vx = lane * 16;
  }
  template <class S> __device__ __forceinline__ void W(S s) {
    static_for<0, 4>([&](auto i) {
      constexpr int kb = i.v >> 1, n = i.v & 1;
      dma16<0>(s.v < 4 ? rre : rim, lds + wslot<GeoY>(s.v) * 16384 + kb * 8192 + (lw + 4 * n) * 1024, vw, (2 * (s.v & 3) + kb) * 128 + n * 32 * DM * 2);
    });
  }
  template <class S> __device__ __forceinline__ void X(S s, rsrc_t rx) {
    static_for<0, 4>([&](auto i) {
      constexpr int blk = i.v >> 1, n = i.v & 1;
      dma16<SC1>(rx, lds + XB + (s.v % 4) * 16384 + blk * 8192 + n * 1024 + lw * 2048, vx, blk * 128 * 512 + (16 * s.v + 2 * n) * 512);
    });
  }
  __device__ __forceinline__ rsrc_t xr(const u16* pq, int tb) { return mk_rsrc((const char*)pq + ((size_t)2 * tb * 128 + 4 * lw) * 512, 2 * 128 * 512 - 4 * lw * 512); }
};
constexpr int C_WS = 28672;
struct CLoader {
  const char* wsrc[7]; unsigned vx; lds_t lds; int lw;
  __device__ __forceinline__ CLoader(lds_t lds_, const Ptrs& p, int fc, int lw_, int lane) : lds(lds_), lw(lw_) {
    vx = lane * 16;
#pragma unroll
    for (int n = 0; n < 7; ++n) {
      int idx = 64 * n + lane, kr = idx / 28, c = idx - kr * 28;
      const u16* base = c < 14 ? p.wg : p.wu;
      int cc = c < 14 ? c : c - 14;
      wsrc[n] = (const char*)(base + (size_t)(16 * lw + kr) * FF + 112 * fc + 8 * cc);
    }
  }
  template <class S> __device__ __forceinline__ void W(S s) {
    static_for<0, 7>([&](auto n) {
      __builtin_amdgcn_global_load_lds((const unsigned*)(wsrc[n.v] + (size_t)s.v * 64 * FF * 2), (LAS unsigned*)(lds + wslot<GeoC>(s.v) * C_WS + lw * 7168 + n.v * 1024), 16, 0, 0);
    });
  }
  template <class S> __device__ __forceinline__ void X(S s, rsrc_t rx) {
    static_for<0, 4>([&](auto n) { dma16<SC1>(rx, lds + XB + (s.v % 4) * 16384 + lw * 4096 + n.v * 1024, vx, (8 * s.v + 2 * n.v) * 512); });
  }
  __device__ __forceinline__ rsrc_t xr(const u16* xa, int rb) { return mk_rsrc((const char*)xa + (size_t)(4 * rb + lw) * 64 * 512, 64 * 512); }
};
struct DLoader {
  rsrc_t rw; unsigned vw, vx; lds_t lds; int lw, k0;
  __device__ __forceinline__ DLoader(lds_t lds_, const Ptrs& p, int tb, int et, int lw_, int lane) : lds(lds_), lw(lw_), k0(ROTD ? (tb * 3) % 14 : 0) {
    rw = mk_rsrc(p.wd + et * 64, FF * DM * 2 - et * 128);
    vw = kn_src(lw, lane, DM * 2); vx = lane * 16;
  }
  template <class S> __device__ __forceinline__ void W(S s) {
    const int ks = s.v + k0 < 14 ? s.v + k0 : s.v + k0 - 14;
    static_for<0, 4>([&](auto i) {
      constexpr int kb = i.v >> 1, n = i.v & 1;
      dma16<0>(rw, lds + wslot<GeoD>(s.v) * 16384 + kb * 8192 + (lw + 4 * n) * 1024, vw, ((2 * ks + kb) * 64 + 32 * n) * DM * 2);
    });
  }
  template <class S> __device__ __forceinline__ void X(S s, rsrc_t rx) {
    const int ks = s.v + k0 < 14 ? s.v + k0 : s.v + k0 - 14;
    static_for<0, 4>([&](auto i) {
      constexpr int blk = i.v >> 1, n = i.v & 1;
      dma16<SC1>(rx, lds + XB + (s.v % 4) * 16384 + blk * 8192 + n * 1024 + lw * 2048, vx, blk * 224 * 512 + (16 * ks + 2 * n) * 512);
    });
  }
  __device__ __forceinline__ rsrc_t xr(const u16* hh, int tb) { return mk_rsrc((const char*)hh + ((size_t)2 * tb * 224 + 4 * lw) * 512, 2 * 224 * 512 - 4 * lw * 512); }
};
template <class G, class MINE, class GN, class NEXT, class LD, class LN>
__device__ __forceinline__ void l_phase(LD& L, rsrc_t rx, LN& N) {
  l_run<G, MINE, GN, NEXT>([&](auto s) { L.X(s, rx); }, [&](auto s) { L.W(s); }, [&](auto s) { N.W(s); });
}

__device__ __forceinline__ void p_compute(lds_t lds, u16* pq, int tb, int et, int w, int lane, const char* t0, const char* t1, bool touch, unsigned& dummy, unsigned long long& q1, unsigned long long& q2 STAMP_ARG) {
  const int r = lane & 31, h = lane >> 5;
  const unsigned a0 = km_read_base(32 * w + r, h);
  const int bl = (lane >> 4) & 1, q4 = (lane & 15) >> 2, pp = ((lane & 1) << 1) | ((lane >> 1) & 1), cc0 = 2 * bl + (pp >> 1);
  const unsigned f0 = cc0 * 512 + (((8 * h + q4) ^ (4 * cc0)) << 4) + 8 * (pp & 1), f1 = f0 ^ 64;
  f32x16 acc0 = {}, acc1 = {};
  unsigned wo = GeoP::OW * 16384, xo = XB;
  struct Fr { bf16x8 tok[4], fa[4], fb[4]; };
  Fr A, B;
  auto rdt = [&](Fr& f, int k) { f.tok[k] = ld_b128(lds + wo + (a0 ^ (k << 5))); };
  auto rda = [&](Fr& f, int k) { lds_t xb = lds + xo + (k >> 1) * 4096 + (k & 1) * 256; f.fa[k] = ld_tr(xb + f0, xb + f1); };
  auto rdb = [&](Fr& f, int k) { lds_t xb = lds + xo + (k >> 1) * 4096 + (k & 1) * 256 + 2048; f.fb[k] = ld_tr(xb + f0, xb + f1); };
  auto adv = [&]() {
    wo = wo + 16384 == 6 * 16384 ? 0 : wo + 16384;
    xo = xo + 8192 == LDS_BYTES ? XB : xo + 8192;
  };
  auto step = [&](Fr& nx, const Fr& cu) {
    cbar();
#pragma unroll
    for (int k = 0; k < 4; ++k) {
      rdt(nx, k); rda(nx, k); SCHED();
      acc0 = mfma(cu.fa[k], cu.tok[k], acc0); SCHED();
      rdb(nx, k); SCHED();
      acc1 = mfma(cu.fb[k], cu.tok[k], acc1); SCHED();
    }
    adv();
  };
  cbar();
  STAMP(1);
  q1 = NOW();
#pragma unroll
  for (int k = 0; k < 4; ++k) { rdt(A, k); rda(A, k); rdb(A, k); }
  adv();
#pragma unroll 1
  for (int s = 1; s < 31; s += 2) {
    step(B, A);
    if (touch && s == 7) {
      asm volatile("global_load_dword %0, %1, off" : "+v"(dummy) : "v"(t0) : "memory");
      asm volatile("global_load_dword %0, %1, off" : "+v"(dummy) : "v"(t1) : "memory");
    }
    step(A, B);
  }
  step(B, A);
  cbar();
  STAMP(2);
  q2 = NOW();
#pragma unroll
  for (int k = 0; k < 4; ++k) { acc0 = mfma(B.fa[k], B.tok[k], acc0); acc1 = mfma(B.fb[k], B.tok[k], acc1); }
  const float sg = w >= 2 ? -1.f : 1.f;
  const rsrc_t ro = mk_rsrc((char*)pq + ((size_t)(2 * tb + (w & 1)) * 128 + (w >> 1) * 64 + 8 * et) * 512, 4096);
#pragma unroll
  for (int ft = 0; ft < 2; ++ft)
#pragma unroll
    for (int q = 0; q < 2; ++q) {
      const f32x16& a = ft ? acc1 : acc0;
      u32x4 v = {pk2(sg * a[8 * q], sg * a[8 * q + 1]), pk2(sg * a[8 * q + 2], sg * a[8 * q + 3]), pk2(sg * a[8 * q + 4], sg * a[8 * q + 5]), pk2(sg * a[8 * q + 6], sg * a[8 * q + 7])};
      __builtin_amdgcn_raw_buffer_store_b128(v, ro, lane * 16 + ft * 2048 + q * 1024, 0, SC1);
    }
}

__device__ __forceinline__ void add_res(const f32x16& acc, u32x4& r0, u32x4& r1) {
#pragma unroll
  for (int i = 0; i < 4; ++i) {
    r0[i] = pk2(acc[2 * i] + bflo(r0[i]), acc[2 * i + 1] + bfhi(r0[i]));
    r1[i] = pk2(acc[8 + 2 * i] + bflo(r1[i]), acc[8 + 2 * i + 1] + bfhi(r1[i]));
  }
}
__device__ __forceinline__ void store_rowmajor(u16* xo, const u32x4& o0, const u32x4& o1, int row0, int col0, int lane) {
  const rsrc_t ro = mk_rsrc(xo + (size_t)row0 * DM + col0, 32 * DM * 2);
  const unsigned vo = (lane & 31) * DM * 2 + (lane >> 5) * 16;
  __builtin_amdgcn_raw_buffer_store_b128(o0, ro, vo, 0, 0);
  __builtin_amdgcn_raw_buffer_store_b128(o1, ro, vo + 32, 0, 0);
}
__device__ __forceinline__ void store_blocked(u16* xo, const u32x4& o0, const u32x4& o1, int row0, int col0, int lane) {
  const rsrc_t ro = mk_rsrc((char*)xo + ((size_t)(row0 >> 5) * 64 + (col0 >> 3)) * 512, 2048);
  __builtin_amdgcn_raw_buffer_store_b128(o0, ro, lane * 16, 0, SC1);
  __builtin_amdgcn_raw_buffer_store_b128(o1, ro, lane * 16 + 1024, 0, SC1);
}
__device__ __forceinline__ void part_stats(const u32x4& o0, const u32x4& o1, float (&v)[16], float& m, float& m2) {
  float s0 = 0.f, s1 = 0.f;
#pragma unroll
  for (int i = 0; i < 4; ++i) { v[2 * i] = bflo(o0[i]); v[2 * i + 1] = bfhi(o0[i]); v[8 + 2 * i] = bflo(o1[i]); v[8 + 2 * i + 1] = bfhi(o1[i]); }
#pragma unroll
  for (int i = 0; i < 8; ++i) { s0 += v[i]; s1 += v[8 + i]; }
  m = xhalf_sum(s0 + s1) * (1.f / 32.f);
  float q0 = 0.f, q1 = 0.f;
#pragma unroll
  for (int i = 0; i < 8; ++i) { float d = v[i] - m, e = v[8 + i] - m; q0 += d * d; q1 += e * e; }
  m2 = xhalf_sum(q0 + q1);
}
__device__ __forceinline__ void store_stats(float* stats, float m, float m2, int row0, int col0, int lane) {
  const rsrc_t rs = mk_rsrc(stats + ((size_t)(col0 >> 5) * SEQ + row0) * 2, 32 * 8);
  if (lane < 32) { u32x2 sv = {__float_as_uint(m), __float_as_uint(m2)}; __builtin_amdgcn_raw_buffer_store_b64(sv, rs, lane * 8, 0, SC1); }
}
__device__ __forceinline__ void load_stats(const float* stats, int row0, int lane, u32x2 (&t)[8]) {
  gu64* q = (gu64*)stats + row0 + (lane & 31) + (size_t)(8 * (lane >> 5)) * SEQ;
#pragma unroll
  for (int i = 0; i < 8; ++i) { unsigned long long x = __hip_atomic_load(q + i * SEQ, RLX_AGENT); t[i] = u32x2{(unsigned)x, (unsigned)(x >> 32)}; }
}
__device__ __forceinline__ void combine_stats(const u32x2 (&t)[8], float& mean, float& rstd) {
  float ms = 0.f, m2 = 0.f;
#pragma unroll
  for (int i = 0; i < 8; ++i) { ms += __uint_as_float(t[i][0]); m2 += __uint_as_float(t[i][1]); }
  mean = xhalf_sum(ms) * (1.f / 16.f);
#pragma unroll
  for (int i = 0; i < 8; ++i) { float d = __uint_as_float(t[i][0]) - mean; m2 += 32.f * d * d; }
  rstd = rsqrtf(xhalf_sum(m2) * (1.f / 512.f) + 1e-5f);
}

template <class G, class FF_> __device__ __forceinline__ f32x16 tile64_loop(lds_t lds, unsigned a0, FF_ feat, int w, int lane STAMP_ARG) {
  f32x16 acc = {};
  unsigned wo = G::OW * 16384, xo = XB;
  struct Fr { bf16x8 tok[8], f[8]; };
  Fr A, B;
  auto rd = [&](Fr& fr, int k) {
    fr.tok[k] = ld_b128(lds + xo + a0 + k * 1024);
    fr.f[k] = feat(lds + wo, k);
  };
  auto adv = [&]() {
    wo = wo + 16384 == 6 * 16384 ? 0 : wo + 16384;
    xo = xo + 16384 == LDS_BYTES ? XB : xo + 16384;
  };
  auto step = [&](Fr& nx, const Fr& cu) {
    cbar();
#pragma unroll
    for (int k = 0; k < 8; ++k) { rd(nx, k); SCHED(); acc = mfma(cu.f[k], cu.tok[k], acc); SCHED(); }
    adv();
  };
  cbar();
  STAMP(1);
#pragma unroll
  for (int k = 0; k < 8; ++k) rd(A, k);
  adv();
#pragma unroll 1
  for (int s = 1; s < G::NST - 1; s += 2) { step(B, A); step(A, B); }
  step(B, A);
  cbar();
  STAMP(2);
#pragma unroll
  for (int k = 0; k < 8; ++k) acc = mfma(B.f[k], B.tok[k], acc);
  return acc;
}

__device__ __forceinline__ void c_compute(lds_t lds, const float* stats, const unsigned* csf, u16* hh, int rb, int fc, int w, int lane STAMP_ARG) {
  const int r = lane & 31, h = lane >> 5;
  const unsigned a0 = w * 4096 + lane * 16;
  const int blk = (lane >> 4) & 1, q = (lane & 15) >> 2, pp = ((lane & 1) << 1) | ((lane >> 1) & 1);
  const unsigned f0 = (8 * h + q) * 448 + blk * 224 + 8 * pp;
  f32x16 acc[7];
#pragma unroll
  for (int j = 0; j < 7; ++j) acc[j] = f32x16{};
  unsigned wo = GeoC::OW * C_WS, xo = XB;
  struct Fr { bf16x8 tok, f[7]; };
  Fr A, B;
  auto rdt = [&](Fr& fr, int k) { fr.tok = ld_b128(lds + xo + a0 + k * 1024); };
  auto rdf = [&](Fr& fr, int k, int j) { lds_t wb = lds + wo + f0; fr.f[j] = ld_tr(wb + 16 * k * 448 + 32 * j, wb + (16 * k + 4) * 448 + 32 * j); };
  auto grp = [&](Fr& nx, const Fr& cu, int k, bool rdn) {
#pragma unroll
    for (int j = 0; j < 7; ++j) {
      if (rdn) { if (j == 0) rdt(nx, k); rdf(nx, k, j); }
      SCHED();
      acc[j] = mfma(cu.f[j], cu.tok, acc[j]);
      SCHED();
    }
  };
  auto adv = [&]() {
    wo = wo + C_WS == 3 * C_WS ? 0 : wo + C_WS;
    xo = xo + 16384 == LDS_BYTES ? XB : xo + 16384;
  };
  cbar();
  STAMP(1);
  u32x2 st[8];
  load_stats(stats, 128 * rb + 32 * w, lane, st);
  unsigned long long cs[7];
#pragma unroll
  for (int j = 0; j < 7; ++j) cs[j] = __hip_atomic_load((gu64*)csf + ((size_t)fc * 32 + r) * 8 + j, __ATOMIC_RELAXED, __HIP_MEMORY_SCOPE_WORKGROUP);
  rdt(A, 0);
#pragma unroll
  for (int j = 0; j < 7; ++j) rdf(A, 0, j);
  auto stage = [&]() {
    grp(B, A, 1, true);
    grp(A, B, 2, true);
    grp(B, A, 3, true);
    adv();
    cbar();
    grp(A, B, 0, true);
  };
  stage();
  float mean, rstd;
  combine_stats(st, mean, rstd);
  {
    const unsigned mh = pk2(-mean, 0.f) & 0xffffu;
    const unsigned ml = pk2(-mean - bflo(mh), 0.f) & 0xffffu;
    u32x4 bt = {h ? 0u : (mh | (ml << 16)), h ? 0u : mh, 0u, 0u};
#pragma unroll
    for (int j = 0; j < 7; ++j) {
      u32x4 at = {h ? 0u : (unsigned)cs[j], h ? 0u : (unsigned)(cs[j] >> 32), 0u, 0u};
      acc[j] = mfma(__builtin_bit_cast(bf16x8, at), __builtin_bit_cast(bf16x8, bt), acc[j]);
    }
  }
  SCHED();
#pragma unroll 1
  for (int s = 1; s < 7; ++s) stage();
  grp(B, A, 1, true);
  grp(A, B, 2, true);
  grp(B, A, 3, true);
  cbar();
  STAMP(2);
  grp(A, B, 0, false);
  const rsrc_t ro = mk_rsrc((char*)hh + ((size_t)(4 * rb + w) * 224 + 14 * fc) * 512, 7168);
  const float ng = -1.4426950408889634f * rstd;
#pragma unroll
  for (int j = 0; j < 7; ++j) {
    float o[8];
#pragma unroll
    for (int i = 0; i < 8; i += 2) {
      f32x2 g = {acc[j][i], acc[j][i + 1]}, u = {acc[j][8 + i], acc[j][9 + i]};
      g *= ng; u *= rstd;
      f32x2 e = {__builtin_amdgcn_exp2f(g[0]), __builtin_amdgcn_exp2f(g[1])};
      e += 1.f;
      f32x2 sg = {__builtin_amdgcn_rcpf(e[0]), __builtin_amdgcn_rcpf(e[1])};
      u *= sg;
      o[i] = u[0]; o[i + 1] = u[1];
    }
    u32x4 v = {pk2(o[0], o[1]), pk2(o[2], o[3]), pk2(o[4], o[5]), pk2(o[6], o[7])};
    __builtin_amdgcn_raw_buffer_store_b128(v, ro, lane * 16 + 1024 * j, 0, SC1);
    SCHED();
  }
}

__global__ __launch_bounds__(512, 2) __attribute__((amdgpu_waves_per_eu(2, 2))) void k_main(Ptrs p) {
  extern __shared__ __attribute__((aligned(16))) char smem[];
  lds_t lds = (lds_t)smem;
  const int lane = threadIdx.x & 63, wid = __builtin_amdgcn_readfirstlane(threadIdx.x >> 6);
  const int xcd = blockIdx.x & 7, j = blockIdx.x >> 3;
  const int tb = xcd * 4 + (j & 3), et = j >> 2;
  const int rb = (xcd & 1) * 8 + (j & 7), fc = (xcd >> 1) * 4 + (j >> 3);
  SPAN_MAIN();
  unsigned long long pt0 = NOW(), pt1 = 0, pt2 = 0;
  if (wid >= 4) {
    {
      PLoader L(lds, p, fresh_s(tb), fresh_s(et), fresh_s(wid - 4), fresh_v(lane), ROT);
      YLoader N(lds, p, fresh_s(et), fresh_s(wid - 4), fresh_v(lane));
      l_phase<GeoP, EarlyNone, GeoY, EarlyY>(L, L.xr(p.xn, et), N);
    }
#pragma unroll 1
    for (int l = 0; l < 2; ++l) {
      {
        YLoader L(lds, p, fresh_s(et), fresh_s(wid - 4), fresh_v(lane));
        CLoader N(lds, p, fresh_s(fc), fresh_s(wid - 4), fresh_v(lane));
        const rsrc_t rx = L.xr(p.pq + l * PQ_L, fresh_s(tb));
        bar(); bar();
        l_phase<GeoY, EarlyY, GeoC, EarlyC>(L, rx, N);
      }
      {
        CLoader L(lds, p, fresh_s(fc), fresh_s(wid - 4), fresh_v(lane));
        DLoader N(lds, p, fresh_s(tb), fresh_s(et), fresh_s(wid - 4), fresh_v(lane));
        const rsrc_t rx = L.xr(p.xa + l * XN_L, fresh_s(rb));
        bar(); bar();
        l_phase<GeoC, EarlyC, GeoD, EarlyD>(L, rx, N);
      }
      if (l == 0) {
        {
          DLoader L(lds, p, fresh_s(tb), fresh_s(et), fresh_s(wid - 4), fresh_v(lane));
          PLoader N(lds, p, fresh_s(tb), fresh_s(et), fresh_s(wid - 4), fresh_v(lane), ROT2);
          const rsrc_t rx = L.xr(p.hh, fresh_s(tb));
          bar(); bar();
          l_phase<GeoD, EarlyD, GeoP, EarlyP>(L, rx, N);
        }
        PLoader L(lds, p, fresh_s(tb), fresh_s(et), fresh_s(wid - 4), fresh_v(lane), ROT2);
        YLoader N(lds, p, fresh_s(et), fresh_s(wid - 4), fresh_v(lane));
        const rsrc_t rx = L.xr(p.xn + XN_L, fresh_s(et));
        bar(); bar();
        l_phase<GeoP, EarlyP, GeoY, EarlyY>(L, rx, N);
      } else {
        DLoader L(lds, p, fresh_s(tb), fresh_s(et), fresh_s(wid - 4), fresh_v(lane));
        const rsrc_t rx = L.xr(p.hh + HH_L, fresh_s(tb));
        bar(); bar();
        l_phase<GeoD, EarlyD, GeoP, EarlyNone>(L, rx, L);
      }
    }
  } else {
    const int w = wid, tw = w & 1, fw = w >> 1;
    const int row0 = 64 * tb + 32 * tw, col0 = 64 * et + 32 * fw;
    unsigned dummy = 0;
    u32x4 r0, r1;
    {
      const int r = lane & 31, h = lane >> 5;
      const rsrc_t rr = mk_rsrc(p.x0 + (size_t)row0 * DM + col0, 32 * DM * 2);
      r0 = __builtin_amdgcn_raw_buffer_load_b128(rr, r * DM * 2 + h * 16, 0, 0);
      r1 = __builtin_amdgcn_raw_buffer_load_b128(rr, r * DM * 2 + h * 16 + 32, 0, 0);
    }
#pragma unroll 1
    for (int l = 0; l < 2; ++l) {
      const int sb = 4 * l;
      {
        STAMP_PTR(4 * l + 0);
        STAMP(0);
        const int ln = fresh_v(lane), li = blockIdx.x * 64 + ln, lj = blockIdx.x * 32 + (ln & 31);
        const char* t0 = li < 14336 ? (const char*)p.wd + (size_t)li * 128 : (const char*)p.wd;
        const char* t1 = (ln < 32 ? (const char*)p.dre : (const char*)p.dim) + (size_t)(lj & 4095) * 128;
        unsigned long long q1 = 0, q2 = 0;
        p_compute(lds, p.pq + l * PQ_L, fresh_s(tb), fresh_s(et), fresh_s(w), fresh_v(lane), t0, t1, l == 0 && w == 0 && blockIdx.x < 224, dummy, q1, q2 STAMP_PASS(4 * l + 0));
        if (l == 0) { pt1 = q1; pt2 = q2; }
        STAMP(5); WAIT_V(0); STAMP(6); bar();
        asm volatile("" :: "v"(dummy));
        if (w == 0) { signal(p.mbox, sb + 0, tb * 8 + (lane & 7), et, lane < 8); await(p.mbox, p.tmo, sb + 0, tb * 8 + et, 8, lane); }
        bar(); STAMP(7);
      }
      {
        STAMP_PTR(4 * l + 1);
        STAMP(0);
        const int ln = fresh_v(lane), r = ln & 31, h = ln >> 5;
        const unsigned a0 = tw * 8192 + ln * 16, b0 = km_read_base(32 * fw + perm23(r), h);
        f32x16 acc = tile64_loop<GeoY>(lds, a0, [&](lds_t wb, int k) { return ld_b128(wb + (k >> 2) * 8192 + (b0 ^ ((k & 3) << 5))); }, w, lane STAMP_PASS(4 * l + 1));
        add_res(acc, r0, r1);
        store_blocked(p.xa + l * XN_L, r0, r1, row0, col0, fresh_v(lane));
        float v[16], m, m2;
        part_stats(r0, r1, v, m, m2);
        store_stats(p.stats + l * ST_L, m, m2, row0, col0, fresh_v(lane));
        STAMP(5); WAIT_V(0); STAMP(6); bar();
        if (w == 0) { signal(p.mbox, sb + 1, (tb >> 1) * 16 + (lane & 15), (tb & 1) * 8 + et, lane < 16); await(p.mbox, p.tmo, sb + 1, rb * 16 + fc, 16, lane); }
        bar(); STAMP(7);
      }
      {
        STAMP_PTR(4 * l + 2);
        STAMP(0);
        c_compute(lds, p.stats + l * ST_L, p.csf, p.hh + l * HH_L, fresh_s(rb), fresh_s(fc), fresh_s(w), fresh_v(lane) STAMP_PASS(4 * l + 2));
        STAMP(5); WAIT_V(0); STAMP(6); bar();
        if (w == 0) { signal(p.mbox, sb + 2, (2 * rb + ((lane >> 3) & 1)) * 8 + (lane & 7), fc, lane < 16); await(p.mbox, p.tmo, sb + 2, tb * 8 + et, 16, lane); }
        bar(); STAMP(7);
      }
      {
        STAMP_PTR(4 * l + 3);
        STAMP(0);
        const int ln = fresh_v(lane);
        const unsigned a0 = tw * 8192 + ln * 16, f0 = kn_read_base(ln, fw);
        f32x16 acc = tile64_loop<GeoD>(lds, a0, [&](lds_t wb, int k) { return ld_tr(wb + f0 + k * 2048, wb + f0 + k * 2048 + 512); }, w, lane STAMP_PASS(4 * l + 3));
        add_res(acc, r0, r1);
        if (l == 1) {
          store_rowmajor(p.out, r0, r1, row0, col0, fresh_v(lane));
          if (w == 0) SPAN_END();
          if (blockIdx.x == 0 && w == 0) {
            const unsigned long long now = NOW();
            const unsigned long long k0s = __hip_atomic_load((gu64*)p.stamps, RLX_AGENT), k0e = __hip_atomic_load((gu64*)p.stamps + 1, RLX_AGENT);
            unsigned long long T = PROBE == 1 ? pt0 - k0s : PROBE == 2 ? pt1 - pt0 : PROBE == 3 ? pt2 - pt1 : PROBE == 4 ? now - pt2 : PROBE == 5 ? k0e - k0s : PROBE == 6 ? 1000ull : PROBE == 7 ? (now - k0s) / 2 : 0;
            if (T > 20000) T = 20000;
            while (NOW() < now + 2 * T) __builtin_amdgcn_s_sleep(1);
          }
        } else {
          float v[16], m, m2;
          part_stats(r0, r1, v, m, m2);
          const int le = fresh_v(lane);
          store_stats((float*)p.gran, m, m2, row0, col0, le);
          STAMP(3);
          u32x2 st[8];
          for (unsigned spins = 0;; ++spins) {
            load_stats((const float*)p.gran, row0, le, st);
            bool ok = true;
#pragma unroll
            for (int i = 0; i < 8; ++i) ok = ok && st[i][1] != 0xffffffffu;
            if (__all(ok)) break;
            if (spins > SPIN_CAP) { if (lane == 0) __hip_atomic_store((gu32*)p.tmo, 2u, RLX_AGENT); break; }
            __builtin_amdgcn_s_sleep(1);
          }
          STAMP(4);
          float mean, rstd;
          combine_stats(st, mean, rstd);
          u32x4 n0, n1;
#pragma unroll
          for (int i = 0; i < 4; ++i) {
            n0[i] = pk2((v[2 * i] - mean) * rstd, (v[2 * i + 1] - mean) * rstd);
            n1[i] = pk2((v[8 + 2 * i] - mean) * rstd, (v[8 + 2 * i + 1] - mean) * rstd);
          }
          store_blocked(p.xn + XN_L, n0, n1, row0, col0, le);
          STAMP(5); WAIT_V(0); STAMP(6); bar();
          if (w == 0) { signal(p.mbox, 3, (lane & 31) * 8 + et, tb, lane < 32); await(p.mbox, p.tmo, 3, tb * 8 + et, 32, lane); }
          bar(); STAMP(7);
        }
      }
    }
  }
}

__device__ __forceinline__ float wave_sum(float v) {
#pragma unroll
  for (int o = 1; o < 64; o <<= 1) v += __shfl_xor(v, o);
  return v;
}
__global__ __launch_bounds__(512) void k_pre(Ptrs p) {
  const int lane = threadIdx.x & 63, wid = threadIdx.x >> 6;
  if (blockIdx.x < 256) {
    SPAN_BEGIN();
    if (blockIdx.x == 0 && threadIdx.x == 0) p.stamps[0] = NOW();
    const int row = blockIdx.x * 8 + wid;
    u32x4 t = *(const u32x4*)(p.x0 + (size_t)row * DM + lane * 8);
    const int gid = blockIdx.x * 512 + threadIdx.x;
    if (gid < 7 * 256 * 8) ((u32x4*)p.mbox)[gid] = u32x4{0u, 0u, 0u, 0u};
    if (gid < 16 * SEQ / 2) ((u32x4*)p.gran)[gid] = u32x4{~0u, ~0u, ~0u, ~0u};
    if (gid == 0) *p.tmo = 0u;
    float v[8], s = 0.f;
#pragma unroll
    for (int i = 0; i < 4; ++i) { v[2 * i] = bflo(t[i]); v[2 * i + 1] = bfhi(t[i]); s += v[2 * i] + v[2 * i + 1]; }
    const float m = wave_sum(s) * (1.f / 512.f);
    float m2 = 0.f;
#pragma unroll
    for (int i = 0; i < 8; ++i) { v[i] -= m; m2 += v[i] * v[i]; }
    const float rs = rsqrtf(wave_sum(m2) * (1.f / 512.f) + 1e-5f);
    u32x4 o = {pk2(v[0] * rs, v[1] * rs), pk2(v[2] * rs, v[3] * rs), pk2(v[4] * rs, v[5] * rs), pk2(v[6] * rs, v[7] * rs)};
    *(u32x4*)((char*)p.xn + ((size_t)(row >> 5) * 64 + lane) * 512 + (row & 31) * 16) = o;
    if (blockIdx.x == 0 && threadIdx.x == 0) p.stamps[1] = NOW();
  } else {
    __shared__ float red[8][64];
    const int b = blockIdx.x - 256;
    const u16* w = (b < 28 ? p.wg : p.wu) + (b % 28) * 64 + (lane & 7) * 8;
    float a[8] = {};
#pragma unroll
    for (int i = 0; i < 8; ++i) {
      u32x4 t = *(const u32x4*)(w + (size_t)(i * 64 + wid * 8 + (lane >> 3)) * FF);
#pragma unroll
      for (int e = 0; e < 4; ++e) { a[2 * e] += bflo(t[e]); a[2 * e + 1] += bfhi(t[e]); }
    }
#pragma unroll
    for (int e = 0; e < 8; ++e) { a[e] += __shfl_xor(a[e], 8); a[e] += __shfl_xor(a[e], 16); a[e] += __shfl_xor(a[e], 32); }
    if (lane < 8) {
#pragma unroll
      for (int e = 0; e < 8; ++e) red[wid][lane * 8 + e] = a[e];
    }
    __syncthreads();
    if (threadIdx.x < 64) {
      float s = 0.f;
#pragma unroll
      for (int i = 0; i < 8; ++i) s += red[i][threadIdx.x];
      const int c = (b % 28) * 64 + threadIdx.x, fc = c / 112, rem = c - fc * 112, jj = rem >> 4, i = perm23(rem & 15) + (b < 28 ? 0 : 16);
      const unsigned hi = pk2(s, 0.f) & 0xffffu, lo = pk2(s - bflo(hi), 0.f) & 0xffffu;
      u32x2 o = {hi | (hi << 16), lo};
      *(u32x2*)(p.csf + ((size_t)fc * 32 + i) * 16 + 2 * jj) = o;
    }
  }
}
}

extern "C" void kernel_launch(void* const* d_in, const int* in_sizes, int n_in, void* d_out, int out_size, void* d_ws, size_t ws_size, hipStream_t stream) {
  Ptrs p;
  p.x0 = (const u16*)d_in[0]; p.sre = (const u16*)d_in[1]; p.sim = (const u16*)d_in[2]; p.dre = (const u16*)d_in[3]; p.dim = (const u16*)d_in[4];
  p.wg = (const u16*)d_in[6]; p.wu = (const u16*)d_in[7]; p.wd = (const u16*)d_in[8];
  char* ws = (char*)d_ws;
  const size_t MB = 1 << 20;
  p.out = (u16*)d_out;
  p.xn = (u16*)ws;
  p.pq = (u16*)(ws + 4 * MB);
  p.xa = (u16*)(ws + 12 * MB);
  p.hh = (u16*)(ws + 16 * MB);
  p.stats = (float*)(ws + 31 * MB);
  p.stamps = (unsigned long long*)(ws + 32 * MB);
  p.csf = (unsigned*)(ws + 33 * MB);
  p.gran = (unsigned*)(ws + 34 * MB);
  p.mbox = (unsigned*)(ws + 35 * MB);
  p.tmo = (unsigned*)(ws + 36 * MB);
  (void)hipFuncSetAttribute((const void*)k_main, hipFuncAttributeMaxDynamicSharedMemorySize, LDS_BYTES);
  k_pre<<<256 + 56, 512, 0, stream>>>(p);
  k_main<<<256, 512, LDS_BYTES, stream>>>(p);
}
```

```cpp
#include <hip/hip_runtime.h>
#include <stdint.h>

namespace {
typedef unsigned short u16;
typedef short s16x4 __attribute__((ext_vector_type(4)));
typedef short s16x8 __attribute__((ext_vector_type(8)));
typedef __bf16 bf16x8 __attribute__((ext_vector_type(8)));
typedef __bf16 bf16x2 __attribute__((ext_vector_type(2)));
typedef float f32x2 __attribute__((ext_vector_type(2)));
typedef float f32x4 __attribute__((ext_vector_type(4)));
typedef float f32x16 __attribute__((ext_vector_type(16)));
typedef unsigned u32x4 __attribute__((ext_vector_type(4)));
typedef unsigned u32x2 __attribute__((ext_vector_type(2)));
#define LAS __attribute__((address_space(3)))
typedef __amdgpu_buffer_rsrc_t rsrc_t;
typedef LAS char* lds_t;
typedef __attribute__((address_space(1))) unsigned gu32;
typedef __attribute__((address_space(1))) unsigned long long gu64;
#define RLX_AGENT __ATOMIC_RELAXED, __HIP_MEMORY_SCOPE_AGENT

constexpr int SEQ = 2048, DM = 512, FF = 1792;
constexpr int LDS_BYTES = 163840, XB = 98304;
constexpr int SC1 = 16;
constexpr unsigned SPIN_CAP = 1u << 18;

__device__ __forceinline__ rsrc_t mk_rsrc(const void* p, unsigned bytes) { return __builtin_amdgcn_make_buffer_rsrc((void*)p, 0, bytes, 0x00020000); }
template <int AUX> __device__ __forceinline__ void dma16(rsrc_t rs, lds_t dst, unsigned voff, unsigned soff) {
  __builtin_amdgcn_raw_ptr_buffer_load_lds(rs, (LAS void*)dst, 16, voff, soff, 0, AUX);
}
#define WAIT_V(n) asm volatile("s_waitcnt vmcnt(%0)" ::"n"(n) : "memory")
#define SCHED() __builtin_amdgcn_sched_barrier(0)
#define WAIT_L0() __builtin_amdgcn_s_waitcnt(0xC07F)
__device__ __forceinline__ void bar() { asm volatile("" ::: "memory"); __builtin_amdgcn_s_barrier(); asm volatile("" ::: "memory"); }
__device__ __forceinline__ void cbar() { WAIT_L0(); bar(); }
__device__ __forceinline__ int fresh_v(int x) { asm volatile("" : "+v"(x)); return x; }
__device__ __forceinline__ int fresh_s(int x) { asm volatile("" : "+s"(x)); return x; }

template <int I> struct IC { static constexpr int v = I; };
template <int A, int B, class F> __device__ __forceinline__ void static_for(F&& f) {
  if constexpr (A < B) { f(IC<A>{}); static_for<A + 1, B>(f); }
}

#define PXI 3
#define ROT 8
#define TS 17
#define ROT2 0
#define ROTD 1
struct GeoP { static constexpr int NST = 32, NX = 8, nX = 2, NW = 6, nW = 4, OW = 0; };
struct GeoY { static constexpr int NST = 8,  NX = 4, nX = 4, NW = 6, nW = 4, OW = 4; };
struct GeoC { static constexpr int NST = 8,  NX = 4, nX = 4, NW = 3, nW = 7, OW = 0; };
struct GeoD { static constexpr int NST = 14, NX = 4, nX = 4, NW = 6, nW = 4, OW = 4; };
constexpr int GAP = 39;
#define L6(a, b, c, d, e, f) {a, b, c, d, e, f}
#define L3(a, b, c) {a, b, c}
#define EY L6(29, 30, 31, GAP, 27, 28)
#define EC L3(4, 6, GAP)
#define ED L6(6, 7, 7, GAP, GAP, GAP)
#define EP L6(9, 10, 11, 12, 13, GAP)
struct EarlyNone { static constexpr int at(int) { return -1; } };
struct EarlyY { static constexpr int at(int j) { constexpr int t[6] = EY; return j < 6 ? t[j] : -1; } };
struct EarlyC { static constexpr int at(int j) { constexpr int t[3] = EC; return j < 3 ? t[j] : -1; } };
struct EarlyD { static constexpr int at(int j) { constexpr int t[6] = ED; return j < 6 ? t[j] : -1; } };
struct EarlyP { static constexpr int at(int j) { constexpr int t[6] = EP; return j < 6 ? t[j] : -1; } };

constexpr int PLAN_MAX = 40;
template <class G, class MINE, class GN, class NEXT> struct Plan {
  int xstep[PLAN_MAX], wstep[PLAN_MAX], waitn[PLAN_MAX];
  constexpr Plan() : xstep{}, wstep{}, waitn{} {
    int nx = 0;
    for (int j = 0; j < G::NST; ++j) { xstep[j] = -2; wstep[j] = MINE::at(j) >= 0 ? -3 : -2; }
    for (int s = -1; s < G::NST; ++s) {
      const int lx = s - 1 + G::NX < G::NX - 1 ? G::NX - 1 : s - 1 + G::NX, lw = s - 1 + G::NW < G::NW - 1 ? G::NW - 1 : s - 1 + G::NW;
      for (int c = 0; c < (s < 0 ? PXI : 2); ++c) if (nx < G::NST && nx <= lx) xstep[nx++] = s;
      int c = 0;
      for (int j = 0; j < G::NST; ++j)
        if (wstep[j] == -2 && j <= lw && (s < 0 ? (j < PXI) : c < 2)) { wstep[j] = s; ++c; }
    }
    for (int s = 0; s < G::NST; ++s) {
      int total = 0, last = 0;
      for (int b = 0; b < PLAN_MAX; ++b)
        for (int j = 0; j < G::NST; ++j) if (MINE::at(j) == b) { total += G::nW; if (j == s) last = total; }
      for (int t = -1; t < s; ++t) {
        for (int j = 0; j < G::NST; ++j) {
          if (xstep[j] == t) { total += G::nX; if (j == s) last = total; }
          if (t < 0 && wstep[j] == t) { total += G::nW; if (j == s) last = total; }
        }
        if (t >= 0) {
          for (int j = 0; j < G::NST; ++j) if (wstep[j] == t) { total += G::nW; if (j == s) last = total; }
          for (int j = 0; j < GN::NST; ++j) if (NEXT::at(j) == t) total += GN::nW;
        }
      }
      waitn[s] = total - last > 63 ? 63 : total - last;
    }
  }
  constexpr bool ok() const {
    for (int j = 0; j < G::NST; ++j) if (xstep[j] >= j || xstep[j] < -1 || wstep[j] >= j || wstep[j] == -2) return false;
    return true;
  }
};
template <class G, class MINE, class GN, class NEXT> struct PlanOf { static constexpr Plan<G, MINE, GN, NEXT> v{}; };
template <class G, class MINE, class GN, class NEXT, class FX, class FW, class FN>
__device__ __forceinline__ void l_run(FX issueX, FW issueW, FN issueNext) {
  using PL = PlanOf<G, MINE, GN, NEXT>;
  static_assert(PL::v.ok(), "a stage would be waited for before it is issued");
  static_for<0, G::NST>([&](auto j) {
    if constexpr (PL::v.xstep[j.v] == -1) issueX(j);
    if constexpr (PL::v.wstep[j.v] == -1) issueW(j);
  });
  static_for<0, G::NST>([&](auto s) {
    WAIT_V(PL::v.waitn[s.v]);
    bar();
    static_for<0, G::NST>([&](auto j) { if constexpr (PL::v.xstep[j.v] == s.v) issueX(j); });
    static_for<0, G::NST>([&](auto j) { if constexpr (PL::v.wstep[j.v] == s.v) issueW(j); });
    static_for<0, GN::NST>([&](auto j) { if constexpr (NEXT::at(j.v) == s.v) issueNext(j); });
  });
  bar();
  static_for<0, GN::NST>([&](auto j) { if constexpr (NEXT::at(j.v) == G::NST) issueNext(j); });
}
template <class G> constexpr int wslot(int j) { return (G::OW + j) % G::NW; }

__device__ __forceinline__ bf16x8 ld_b128(lds_t p) { return *(LAS bf16x8*)p; }
__device__ __forceinline__ bf16x8 ld_tr(lds_t p0, lds_t p1) {
  s16x4 a = __builtin_amdgcn_ds_read_tr16_b64_v4i16((LAS s16x4*)p0);
  s16x4 b = __builtin_amdgcn_ds_read_tr16_b64_v4i16((LAS s16x4*)p1);
  s16x8 f = __builtin_shufflevector(a, b, 0, 1, 2, 3, 4, 5, 6, 7);
  return __builtin_bit_cast(bf16x8, f);
}
__device__ __forceinline__ f32x16 mfma(bf16x8 a, bf16x8 b, f32x16 c) { return __builtin_amdgcn_mfma_f32_32x32x16_bf16(a, b, c, 0, 0, 0); }
__device__ __forceinline__ unsigned pk2(float a, float b) { f32x2 v = {a, b}; return __builtin_bit_cast(unsigned, __builtin_convertvector(v, bf16x2)); }
__device__ __forceinline__ float bflo(unsigned u) { return __uint_as_float(u << 16); }
__device__ __forceinline__ float bfhi(unsigned u) { return __uint_as_float(u & 0xffff0000u); }
__device__ __forceinline__ float xhalf_sum(float v) {
  auto r = __builtin_amdgcn_permlane32_swap(__float_as_uint(v), __float_as_uint(v), false, false);
  return __uint_as_float(r[0]) + __uint_as_float(r[1]);
}
__device__ __forceinline__ unsigned km_read_base(int row, int h) { return row * 128 + ((h ^ ((row >> 1) & 7)) << 4); }
__device__ __forceinline__ int perm23(int i) { return (i & ~12) | ((i & 4) << 1) | ((i & 8) >> 1); }
__device__ __forceinline__ unsigned km_src(int lw, int lane, int pitch) {
  return (8 * lw + (lane >> 3)) * pitch + (((lane & 7) ^ (((lw & 1) << 2) | (lane >> 4))) << 4);
}
__device__ __forceinline__ unsigned kn_src(int lw, int lane, int pitch) {
  return (8 * lw + (lane >> 3)) * pitch + (((lane & 7) ^ (((lane >> 4) & 1) << 2)) << 4);
}
__device__ __forceinline__ unsigned kn_read_base(int lane, int ft) {
  int h = lane >> 5, blk = (lane >> 4) & 1, q = (lane & 15) >> 2, p = lane & 3, pp = ((p & 1) << 1) | (p >> 1);
  int c = 4 * ft + 2 * blk + (pp >> 1);
  return (8 * h + q) * 128 + ((c ^ (((q >> 1) & 1) << 2)) << 4) + 8 * (pp & 1);
}

#define SPAN_BEGIN()
#define SPAN_MAIN()
#define SPAN_END()
#define STAMP(i)
#define STAMP_ARG
#define STAMP_PASS(ph)
#define STAMP_PTR(ph)

#define PROBE 0
#define NOW() __builtin_amdgcn_s_memrealtime()
struct Ptrs {
  unsigned long long* stamps;
  const u16 *x0, *sre, *sim, *dre, *dim, *wg, *wu, *wd;
  u16 *out, *xn, *pq, *xa, *hh;
  float* stats;
  unsigned *csf, *gran, *mbox, *tmo;
};
constexpr size_t XN_L = (size_t)SEQ * DM, PQ_L = (size_t)SEQ * 1024, HH_L = (size_t)SEQ * FF, ST_L = (size_t)16 * SEQ * 2;

__device__ __forceinline__ void signal(unsigned* mbox, int seam, int consumer, int slot, bool active) {
  if (active) __hip_atomic_store((gu32*)mbox + ((size_t)seam * 256 + consumer) * 32 + slot, 1u, RLX_AGENT);
}
__device__ __forceinline__ void await(unsigned* mbox, unsigned* tmo, int seam, int me, int nprod, int lane) {
  gu32* q = (gu32*)mbox + ((size_t)seam * 256 + me) * 32 + (lane < nprod ? lane : 0);
  for (unsigned spins = 0;; ++spins) {
    unsigned x = __hip_atomic_load(q, RLX_AGENT);
    if (__all(x == 1u)) break;
    if (spins > SPIN_CAP) { if (lane == 0) __hip_atomic_store((gu32*)tmo, 1u, RLX_AGENT); break; }
    __builtin_amdgcn_s_sleep(1);
  }
}

struct PLoader {
  rsrc_t rre, rim; unsigned vw, vx; lds_t lds; int lw, k0;
  __device__ __forceinline__ PLoader(lds_t lds_, const Ptrs& p, int tb, int et, int lw_, int lane, int rot) : lds(lds_), lw(lw_), k0((rot * et) & 31) {
    rre = mk_rsrc(p.sre + (size_t)tb * 64 * SEQ, 64 * SEQ * 2); rim = mk_rsrc(p.sim + (size_t)tb * 64 * SEQ, 64 * SEQ * 2);
    vw = km_src(lw, lane, SEQ * 2);
    vx = (lane >> 5) * 512 + (((lane & 31) ^ (4 * ((2 * lw + (lane >> 5)) & 3))) << 4);
  }
  template <class S> __device__ __forceinline__ void W(S s) {
    const int ks = (s.v + k0) & 31;
    static_for<0, 4>([&](auto n) { dma16<0>(n.v < 2 ? rre : rim, lds + wslot<GeoP>(s.v) * 16384 + (lw + 4 * n.v) * 1024, vw, ks * 128 + (n.v & 1) * 32 * SEQ * 2); });
  }
  template <class S> __device__ __forceinline__ void X(S s, rsrc_t rx) {
    const int ks = (s.v + k0) & 31;
    static_for<0, 2>([&](auto n) { dma16<SC1>(rx, lds + XB + (s.v % 8) * 8192 + n.v * 4096 + lw * 1024, vx, (2 * ks + n.v) * 64 * 512); });
  }
  __device__ __forceinline__ rsrc_t xr(const u16* xn, int et) { return mk_rsrc((const char*)xn + (8 * et + 2 * lw) * 512, SEQ * DM * 2 - (8 * et + 2 * lw) * 512); }
};
struct YLoader {
  rsrc_t rre, rim; unsigned vw, vx; lds_t lds; int lw;
  __device__ __forceinline__ YLoader(lds_t lds_, const Ptrs& p, int et, int lw_, int lane) : lds(lds_), lw(lw_) {
    rre = mk_rsrc(p.dre + (size_t)et * 64 * DM, 64 * DM * 2); rim = mk_rsrc(p.dim + (size_t)et * 64 * DM, 64 * DM * 2);
    vw = km_src(lw, lane, DM * 2); vx = lane * 16;
  }
  template <class S> __device__ __forceinline__ void W(S s) {
    static_for<0, 4>([&](auto i) {
      constexpr int kb = i.v >> 1, n = i.v & 1;
      dma16<0>(s.v < 4 ? rre : rim, lds + wslot<GeoY>(s.v) * 16384 + kb * 8192 + (lw + 4 * n) * 1024, vw, (2 * (s.v & 3) + kb) * 128 + n * 32 * DM * 2);
    });
  }
  template <class S> __device__ __forceinline__ void X(S s, rsrc_t rx) {
    static_for<0, 4>([&](auto i) {
      constexpr int blk = i.v >> 1, n = i.v & 1;
      dma16<SC1>(rx, lds + XB + (s.v % 4) * 16384 + blk * 8192 + n * 1024 + lw * 2048, vx, blk * 128 * 512 + (16 * s.v + 2 * n) * 512);
    });
  }
  __device__ __forceinline__ rsrc_t xr(const u16* pq, int tb) { return mk_rsrc((const char*)pq + ((size_t)2 * tb * 128 + 4 * lw) * 512, 2 * 128 * 512 - 4 * lw * 512); }
};
constexpr int C_WS = 28672;
struct CLoader {
  const char* wsrc[7]; unsigned vx; lds_t lds; int lw;
  __device__ __forceinline__ CLoader(lds_t lds_, const Ptrs& p, int fc, int lw_, int lane) : lds(lds_), lw(lw_) {
    vx = lane * 16;
#pragma unroll
    for (int n = 0; n < 7; ++n) {
      int idx = 64 * n + lane, kr = idx / 28, c = idx - kr * 28;
      const u16* base = c < 14 ? p.wg : p.wu;
      int cc = c < 14 ? c : c - 14;
      wsrc[n] = (const char*)(base + (size_t)(16 * lw + kr) * FF + 112 * fc + 8 * cc);
    }
  }
  template <class S> __device__ __forceinline__ void W(S s) {
    static_for<0, 7>([&](auto n) {
      __builtin_amdgcn_global_load_lds((const unsigned*)(wsrc[n.v] + (size_t)s.v * 64 * FF * 2), (LAS unsigned*)(lds + wslot<GeoC>(s.v) * C_WS + lw * 7168 + n.v * 1024), 16, 0, 0);
    });
  }
  template <class S> __device__ __forceinline__ void X(S s, rsrc_t rx) {
    static_for<0, 4>([&](auto n) { dma16<SC1>(rx, lds + XB + (s.v % 4) * 16384 + lw * 4096 + n.v * 1024, vx, (8 * s.v + 2 * n.v) * 512); });
  }
  __device__ __forceinline__ rsrc_t xr(const u16* xa, int rb) { return mk_rsrc((const char*)xa + (size_t)(4 * rb + lw) * 64 * 512, 64 * 512); }
};
struct DLoader {
  rsrc_t rw; unsigned vw, vx; lds_t lds; int lw, k0;
  __device__ __forceinline__ DLoader(lds_t lds_, const Ptrs& p, int tb, int et, int lw_, int lane) : lds(lds_), lw(lw_), k0(ROTD ? (tb * 3) % 14 : 0) {
    rw = mk_rsrc(p.wd + et * 64, FF * DM * 2 - et * 128);
    vw = kn_src(lw, lane, DM * 2); vx = lane * 16;
  }
  template <class S> __device__ __forceinline__ void W(S s) {
    const int ks = s.v + k0 < 14 ? s.v + k0 : s.v + k0 - 14;
    static_for<0, 4>([&](auto i) {
      constexpr int kb = i.v >> 1, n = i.v & 1;
      dma16<0>(rw, lds + wslot<GeoD>(s.v) * 16384 + kb * 8192 + (lw + 4 * n) * 1024, vw, ((2 * ks + kb) * 64 + 32 * n) * DM * 2);
    });
  }
  template <class S> __device__ __forceinline__ void X(S s, rsrc_t rx) {
    const int ks = s.v + k0 < 14 ? s.v + k0 : s.v + k0 - 14;
    static_for<0, 4>([&](auto i) {
      constexpr int blk = i.v >> 1, n = i.v & 1;
      dma16<SC1>(rx, lds + XB + (s.v % 4) * 16384 + blk * 8192 + n * 1024 + lw * 2048, vx, blk * 224 * 512 + (16 * ks + 2 * n) * 512);
    });
  }
  __device__ __forceinline__ rsrc_t xr(const u16* hh, int tb) { return mk_rsrc((const char*)hh + ((size_t)2 * tb * 224 + 4 * lw) * 512, 2 * 224 * 512 - 4 * lw * 512); }
};
template <class G, class MINE, class LD> __device__ __forceinline__ void l_seam(LD& L) {
  bar(); bar();
  static_for<0, G::NST>([&](auto j) { if constexpr (MINE::at(j.v) == GAP) L.W(j); });
  bar();
}
template <class G, class MINE, class GN, class NEXT, class LD, class LN>
__device__ __forceinline__ void l_phase(LD& L, rsrc_t rx, LN& N) {
  l_run<G, MINE, GN, NEXT>([&](auto s) { L.X(s, rx); }, [&](auto s) { L.W(s); }, [&](auto s) { N.W(s); });
}

__device__ __forceinline__ void p_compute(lds_t lds, u16* pq, int tb, int et, int w, int lane, const char* t0, const char* t1, bool touch, unsigned& dummy, unsigned long long& q1, unsigned long long& q2 STAMP_ARG) {
  const int r = lane & 31, h = lane >> 5;
  const unsigned a0 = km_read_base(32 * w + r, h);
  const int bl = (lane >> 4) & 1, q4 = (lane & 15) >> 2, pp = ((lane & 1) << 1) | ((lane >> 1) & 1), cc0 = 2 * bl + (pp >> 1);
  const unsigned f0 = cc0 * 512 + (((8 * h + q4) ^ (4 * cc0)) << 4) + 8 * (pp & 1), f1 = f0 ^ 64;
  f32x16 acc0 = {}, acc1 = {};
  unsigned wo = GeoP::OW * 16384, xo = XB;
  struct Fr { bf16x8 tok[4], fa[4], fb[4]; };
  Fr A, B;
  auto rdt = [&](Fr& f, int k) { f.tok[k] = ld_b128(lds + wo + (a0 ^ (k << 5))); };
  auto rda = [&](Fr& f, int k) { lds_t xb = lds + xo + (k >> 1) * 4096 + (k & 1) * 256; f.fa[k] = ld_tr(xb + f0, xb + f1); };
  auto rdb = [&](Fr& f, int k) { lds_t xb = lds + xo + (k >> 1) * 4096 + (k & 1) * 256 + 2048; f.fb[k] = ld_tr(xb + f0, xb + f1); };
  auto adv = [&]() {
    wo = wo + 16384 == 6 * 16384 ? 0 : wo + 16384;
    xo = xo + 8192 == LDS_BYTES ? XB : xo + 8192;
  };
  auto step = [&](Fr& nx, const Fr& cu) {
    cbar();
#pragma unroll
    for (int k = 0; k < 4; ++k) {
      rdt(nx, k); rda(nx, k); SCHED();
      acc0 = mfma(cu.fa[k], cu.tok[k], acc0); SCHED();
      rdb(nx, k); SCHED();
      acc1 = mfma(cu.fb[k], cu.tok[k], acc1); SCHED();
    }
    adv();
  };
  cbar();
  STAMP(1);
#pragma unroll
  for (int k = 0; k < 4; ++k) { rdt(A, k); rda(A, k); rdb(A, k); }
  adv();
#pragma unroll 1
  for (int s = 1; s < 31; s += 2) {
    step(B, A);
    if (touch && s == TS) {
      asm volatile("global_load_dword %0, %1, off" : "+v"(dummy) : "v"(t0) : "memory");
      asm volatile("global_load_dword %0, %1, off" : "+v"(dummy) : "v"(t1) : "memory");
    }
    step(A, B);
  }
  step(B, A);
  cbar();
  STAMP(2);
#pragma unroll
  for (int k = 0; k < 4; ++k) { acc0 = mfma(B.fa[k], B.tok[k], acc0); acc1 = mfma(B.fb[k], B.tok[k], acc1); }
  const float sg = w >= 2 ? -1.f : 1.f;
  const rsrc_t ro = mk_rsrc((char*)pq + ((size_t)(2 * tb + (w & 1)) * 128 + (w >> 1) * 64 + 8 * et) * 512, 4096);
#pragma unroll
  for (int ft = 0; ft < 2; ++ft)
#pragma unroll
    for (int q = 0; q < 2; ++q) {
      const f32x16& a = ft ? acc1 : acc0;
      u32x4 v = {pk2(sg * a[8 * q], sg * a[8 * q + 1]), pk2(sg * a[8 * q + 2], sg * a[8 * q + 3]), pk2(sg * a[8 * q + 4], sg * a[8 * q + 5]), pk2(sg * a[8 * q + 6], sg * a[8 * q + 7])};
      __builtin_amdgcn_raw_buffer_store_b128(v, ro, lane * 16 + ft * 2048 + q * 1024, 0, SC1);
    }
}

__device__ __forceinline__ void add_res(const f32x16& acc, u32x4& r0, u32x4& r1) {
#pragma unroll
  for (int i = 0; i < 4; ++i) {
    r0[i] = pk2(acc[2 * i] + bflo(r0[i]), acc[2 * i + 1] + bfhi(r0[i]));
    r1[i] = pk2(acc[8 + 2 * i] + bflo(r1[i]), acc[8 + 2 * i + 1] + bfhi(r1[i]));
  }
}
__device__ __forceinline__ void store_rowmajor(u16* xo, const u32x4& o0, const u32x4& o1, int row0, int col0, int lane) {
  const rsrc_t ro = mk_rsrc(xo + (size_t)row0 * DM + col0, 32 * DM * 2);
  const unsigned vo = (lane & 31) * DM * 2 + (lane >> 5) * 16;
  __builtin_amdgcn_raw_buffer_store_b128(o0, ro, vo, 0, 0);
  __builtin_amdgcn_raw_buffer_store_b128(o1, ro, vo + 32, 0, 0);
}
__device__ __forceinline__ void store_blocked(u16* xo, const u32x4& o0, const u32x4& o1, int row0, int col0, int lane) {
  const rsrc_t ro = mk_rsrc((char*)xo + ((size_t)(row0 >> 5) * 64 + (col0 >> 3)) * 512, 2048);
  __builtin_amdgcn_raw_buffer_store_b128(o0, ro, lane * 16, 0, SC1);
  __builtin_amdgcn_raw_buffer_store_b128(o1, ro, lane * 16 + 1024, 0, SC1);
}
__device__ __forceinline__ void part_stats(const u32x4& o0, const u32x4& o1, float (&v)[16], float& m, float& m2) {
  float s0 = 0.f, s1 = 0.f;
#pragma unroll
  for (int i = 0; i < 4; ++i) { v[2 * i] = bflo(o0[i]); v[2 * i + 1] = bfhi(o0[i]); v[8 + 2 * i] = bflo(o1[i]); v[8 + 2 * i + 1] = bfhi(o1[i]); }
#pragma unroll
  for (int i = 0; i < 8; ++i) { s0 += v[i]; s1 += v[8 + i]; }
  m = xhalf_sum(s0 + s1) * (1.f / 32.f);
  float q0 = 0.f, q1 = 0.f;
#pragma unroll
  for (int i = 0; i < 8; ++i) { float d = v[i] - m, e = v[8 + i] - m; q0 += d * d; q1 += e * e; }
  m2 = xhalf_sum(q0 + q1);
}
__device__ __forceinline__ void store_stats(float* stats, float m, float m2, int row0, int col0, int lane) {
  const rsrc_t rs = mk_rsrc(stats + ((size_t)(col0 >> 5) * SEQ + row0) * 2, 32 * 8);
  if (lane < 32) { u32x2 sv = {__float_as_uint(m), __float_as_uint(m2)}; __builtin_amdgcn_raw_buffer_store_b64(sv, rs, lane * 8, 0, SC1); }
}
__device__ __forceinline__ void load_stats(const float* stats, int row0, int lane, u32x2 (&t)[8]) {
  gu64* q = (gu64*)stats + row0 + (lane & 31) + (size_t)(8 * (lane >> 5)) * SEQ;
#pragma unroll
  for (int i = 0; i < 8; ++i) { unsigned long long x = __hip_atomic_load(q + i * SEQ, RLX_AGENT); t[i] = u32x2{(unsigned)x, (unsigned)(x >> 32)}; }
}
__device__ __forceinline__ void combine_stats(const u32x2 (&t)[8], float& mean, float& rstd) {
  float ms = 0.f, m2 = 0.f;
#pragma unroll
  for (int i = 0; i < 8; ++i) { ms += __uint_as_float(t[i][0]); m2 += __uint_as_float(t[i][1]); }
  mean = xhalf_sum(ms) * (1.f / 16.f);
#pragma unroll
  for (int i = 0; i < 8; ++i) { float d = __uint_as_float(t[i][0]) - mean; m2 += 32.f * d * d; }
  rstd = rsqrtf(xhalf_sum(m2) * (1.f / 512.f) + 1e-5f);
}

template <class G, class FF_> __device__ __forceinline__ f32x16 tile64_loop(lds_t lds, unsigned a0, FF_ feat, int w, int lane STAMP_ARG) {
  f32x16 acc = {};
  unsigned wo = G::OW * 16384, xo = XB;
  struct Fr { bf16x8 tok[8], f[8]; };
  Fr A, B;
  auto rd = [&](Fr& fr, int k) {
    fr.tok[k] = ld_b128(lds + xo + a0 + k * 1024);
    fr.f[k] = feat(lds + wo, k);
  };
  auto adv = [&]() {
    wo = wo + 16384 == 6 * 16384 ? 0 : wo + 16384;
    xo = xo + 16384 == LDS_BYTES ? XB : xo + 16384;
  };
  auto step = [&](Fr& nx, const Fr& cu) {
    cbar();
#pragma unroll
    for (int k = 0; k < 8; ++k) { rd(nx, k); SCHED(); acc = mfma(cu.f[k], cu.tok[k], acc); SCHED(); }
    adv();
  };
  cbar();
  STAMP(1);
#pragma unroll
  for (int k = 0; k < 8; ++k) rd(A, k);
  adv();
#pragma unroll 1
  for (int s = 1; s < G::NST - 1; s += 2) { step(B, A); step(A, B); }
  step(B, A);
  cbar();
  STAMP(2);
#pragma unroll
  for (int k = 0; k < 8; ++k) acc = mfma(B.f[k], B.tok[k], acc);
  return acc;
}

__device__ __forceinline__ void c_compute(lds_t lds, const float* stats, const unsigned* csf, u16* hh, int rb, int fc, int w, int lane STAMP_ARG) {
  const int r = lane & 31, h = lane >> 5;
  const unsigned a0 = w * 4096 + lane * 16;
  const int blk = (lane >> 4) & 1, q = (lane & 15) >> 2, pp = ((lane & 1) << 1) | ((lane >> 1) & 1);
  const unsigned f0 = (8 * h + q) * 448 + blk * 224 + 8 * pp;
  f32x16 acc[7];
#pragma unroll
  for (int j = 0; j < 7; ++j) acc[j] = f32x16{};
  unsigned wo = GeoC::OW * C_WS, xo = XB;
  struct Fr { bf16x8 tok, f[7]; };
  Fr A, B;
  auto rdt = [&](Fr& fr, int k) { fr.tok = ld_b128(lds + xo + a0 + k * 1024); };
  auto rdf = [&](Fr& fr, int k, int j) { lds_t wb = lds + wo + f0; fr.f[j] = ld_tr(wb + 16 * k * 448 + 32 * j, wb + (16 * k + 4) * 448 + 32 * j); };
  auto grp = [&](Fr& nx, const Fr& cu, int k, bool rdn) {
#pragma unroll
    for (int j = 0; j < 7; ++j) {
      if (rdn) { if (j == 0) rdt(nx, k); rdf(nx, k, j); }
      SCHED();
      acc[j] = mfma(cu.f[j], cu.tok, acc[j]);
      SCHED();
    }
  };
  auto adv = [&]() {
    wo = wo + C_WS == 3 * C_WS ? 0 : wo + C_WS;
    xo = xo + 16384 == LDS_BYTES ? XB : xo + 16384;
  };
  cbar();
  STAMP(1);
  u32x2 st[8];
  load_stats(stats, 128 * rb + 32 * w, lane, st);
  unsigned long long cs[7];
#pragma unroll
  for (int j = 0; j < 7; ++j) cs[j] = __hip_atomic_load((gu64*)csf + ((size_t)fc * 32 + r) * 8 + j, __ATOMIC_RELAXED, __HIP_MEMORY_SCOPE_WORKGROUP);
  rdt(A, 0);
#pragma unroll
  for (int j = 0; j < 7; ++j) rdf(A, 0, j);
  auto stage = [&]() {
    grp(B, A, 1, true);
    grp(A, B, 2, true);
    grp(B, A, 3, true);
    adv();
    cbar();
    grp(A, B, 0, true);
  };
  stage();
  float mean, rstd;
  combine_stats(st, mean, rstd);
  {
    const unsigned mh = pk2(-mean, 0.f) & 0xffffu;
    const unsigned ml = pk2(-mean - bflo(mh), 0.f) & 0xffffu;
    u32x4 bt = {h ? 0u : (mh | (ml << 16)), h ? 0u : mh, 0u, 0u};
#pragma unroll
    for (int j = 0; j < 7; ++j) {
      u32x4 at = {h ? 0u : (unsigned)cs[j], h ? 0u : (unsigned)(cs[j] >> 32), 0u, 0u};
      acc[j] = mfma(__builtin_bit_cast(bf16x8, at), __builtin_bit_cast(bf16x8, bt), acc[j]);
    }
  }
  SCHED();
#pragma unroll 1
  for (int s = 1; s < 7; ++s) stage();
  grp(B, A, 1, true);
  grp(A, B, 2, true);
  grp(B, A, 3, true);
  cbar();
  STAMP(2);
  grp(A, B, 0, false);
  const rsrc_t ro = mk_rsrc((char*)hh + ((size_t)(4 * rb + w) * 224 + 14 * fc) * 512, 7168);
  const float ng = -1.4426950408889634f * rstd;
#pragma unroll
  for (int j = 0; j < 7; ++j) {
    float o[8];
#pragma unroll
    for (int i = 0; i < 8; i += 2) {
      f32x2 g = {acc[j][i], acc[j][i + 1]}, u = {acc[j][8 + i], acc[j][9 + i]};
      g *= ng; u *= rstd;
      f32x2 e = {__builtin_amdgcn_exp2f(g[0]), __builtin_amdgcn_exp2f(g[1])};
      e += 1.f;
      f32x2 sg = {__builtin_amdgcn_rcpf(e[0]), __builtin_amdgcn_rcpf(e[1])};
      u *= sg;
      o[i] = u[0]; o[i + 1] = u[1];
    }
    u32x4 v = {pk2(o[0], o[1]), pk2(o[2], o[3]), pk2(o[4], o[5]), pk2(o[6], o[7])};
    __builtin_amdgcn_raw_buffer_store_b128(v, ro, lane * 16 + 1024 * j, 0, SC1);
    SCHED();
  }
}

__global__ __launch_bounds__(512, 2) __attribute__((amdgpu_waves_per_eu(2, 2))) void k_main(Ptrs p) {
  extern __shared__ __attribute__((aligned(16))) char smem[];
  lds_t lds = (lds_t)smem;
  const int lane = threadIdx.x & 63, wid = __builtin_amdgcn_readfirstlane(threadIdx.x >> 6);
  const int xcd = blockIdx.x & 7, j = blockIdx.x >> 3;
  const int tb = xcd * 4 + (j & 3), et = j >> 2;
  const int rb = (xcd & 1) * 8 + (j & 7), fc = (xcd >> 1) * 4 + (j >> 3);
  SPAN_MAIN();
  if (wid >= 4) {
    {
      PLoader L(lds, p, fresh_s(tb), fresh_s(et), fresh_s(wid - 4), fresh_v(lane), ROT);
      YLoader N(lds, p, fresh_s(et), fresh_s(wid - 4), fresh_v(lane));
      l_phase<GeoP, EarlyNone, GeoY, EarlyY>(L, L.xr(p.xn, et), N);
    }
#pragma unroll 1
    for (int l = 0; l < 2; ++l) {
      {
        YLoader L(lds, p, fresh_s(et), fresh_s(wid - 4), fresh_v(lane));
        CLoader N(lds, p, fresh_s(fc), fresh_s(wid - 4), fresh_v(lane));
        const rsrc_t rx = L.xr(p.pq + l * PQ_L, fresh_s(tb));
        l_seam<GeoY, EarlyY>(L);
        l_phase<GeoY, EarlyY, GeoC, EarlyC>(L, rx, N);
      }
      {
        CLoader L(lds, p, fresh_s(fc), fresh_s(wid - 4), fresh_v(lane));
        DLoader N(lds, p, fresh_s(tb), fresh_s(et), fresh_s(wid - 4), fresh_v(lane));
        const rsrc_t rx = L.xr(p.xa + l * XN_L, fresh_s(rb));
        l_seam<GeoC, EarlyC>(L);
        l_phase<GeoC, EarlyC, GeoD, EarlyD>(L, rx, N);
      }
      if (l == 0) {
        {
          DLoader L(lds, p, fresh_s(tb), fresh_s(et), fresh_s(wid - 4), fresh_v(lane));
          PLoader N(lds, p, fresh_s(tb), fresh_s(et), fresh_s(wid - 4), fresh_v(lane), ROT2);
          const rsrc_t rx = L.xr(p.hh, fresh_s(tb));
          l_seam<GeoD, EarlyD>(L);
          l_phase<GeoD, EarlyD, GeoP, EarlyP>(L, rx, N);
        }
        PLoader L(lds, p, fresh_s(tb), fresh_s(et), fresh_s(wid - 4), fresh_v(lane), ROT2);
        YLoader N(lds, p, fresh_s(et), fresh_s(wid - 4), fresh_v(lane));
        const rsrc_t rx = L.xr(p.xn + XN_L, fresh_s(et));
        l_seam<GeoP, EarlyP>(L);
        l_phase<GeoP, EarlyP, GeoY, EarlyY>(L, rx, N);
      } else {
        DLoader L(lds, p, fresh_s(tb), fresh_s(et), fresh_s(wid - 4), fresh_v(lane));
        const rsrc_t rx = L.xr(p.hh + HH_L, fresh_s(tb));
        l_seam<GeoD, EarlyD>(L);
        l_phase<GeoD, EarlyD, GeoP, EarlyNone>(L, rx, L);
      }
    }
  } else {
    const int w = wid, tw = w & 1, fw = w >> 1;
    const int row0 = 64 * tb + 32 * tw, col0 = 64 * et + 32 * fw;
    unsigned dummy = 0;
    u32x4 r0, r1;
    {
      const int r = lane & 31, h = lane >> 5;
      const rsrc_t rr = mk_rsrc(p.x0 + (size_t)row0 * DM + col0, 32 * DM * 2);
      r0 = __builtin_amdgcn_raw_buffer_load_b128(rr, r * DM * 2 + h * 16, 0, 0);
      r1 = __builtin_amdgcn_raw_buffer_load_b128(rr, r * DM * 2 + h * 16 + 32, 0, 0);
    }
#pragma unroll 1
    for (int l = 0; l < 2; ++l) {
      const int sb = 4 * l;
      {
        STAMP_PTR(4 * l + 0);
        STAMP(0);
        const int ln = fresh_v(lane), li = blockIdx.x * 64 + ln, lj = blockIdx.x * 32 + (ln & 31);
        const char* t0 = li < 14336 ? (const char*)p.wd + (size_t)li * 128 : (const char*)p.wd;
        const char* t1 = (ln < 32 ? (const char*)p.dre : (const char*)p.dim) + (size_t)(lj & 4095) * 128;
        unsigned long long q1 = 0, q2 = 0;
        p_compute(lds, p.pq + l * PQ_L, fresh_s(tb), fresh_s(et), fresh_s(w), fresh_v(lane), t0, t1, l == 0 && w == 0 && blockIdx.x < 224, dummy, q1, q2 STAMP_PASS(4 * l + 0));
        STAMP(5); WAIT_V(0); STAMP(6); bar();
        asm volatile("" :: "v"(dummy));
        if (w == 0) signal(p.mbox, sb + 0, tb * 8 + (lane & 7), et, lane < 8);
        bar();
        if (w == 0) await(p.mbox, p.tmo, sb + 0, tb * 8 + et, 8, lane);
        bar(); STAMP(7);
      }
      {
        STAMP_PTR(4 * l + 1);
        STAMP(0);
        const int ln = fresh_v(lane), r = ln & 31, h = ln >> 5;
        const unsigned a0 = tw * 8192 + ln * 16, b0 = km_read_base(32 * fw + perm23(r), h);
        f32x16 acc = tile64_loop<GeoY>(lds, a0, [&](lds_t wb, int k) { return ld_b128(wb + (k >> 2) * 8192 + (b0 ^ ((k & 3) << 5))); }, w, lane STAMP_PASS(4 * l + 1));
        add_res(acc, r0, r1);
        store_blocked(p.xa + l * XN_L, r0, r1, row0, col0, fresh_v(lane));
        float v[16], m, m2;
        part_stats(r0, r1, v, m, m2);
        store_stats(p.stats + l * ST_L, m, m2, row0, col0, fresh_v(lane));
        STAMP(5); WAIT_V(0); STAMP(6); bar();
        if (w == 0) signal(p.mbox, sb + 1, (tb >> 1) * 16 + (lane & 15), (tb & 1) * 8 + et, lane < 16);
        bar();
        if (w == 0) await(p.mbox, p.tmo, sb + 1, rb * 16 + fc, 16, lane);
        bar(); STAMP(7);
      }
      {
        STAMP_PTR(4 * l + 2);
        STAMP(0);
        c_compute(lds, p.stats + l * ST_L, p.csf, p.hh + l * HH_L, fresh_s(rb), fresh_s(fc), fresh_s(w), fresh_v(lane) STAMP_PASS(4 * l + 2));
        STAMP(5); WAIT_V(0); STAMP(6); bar();
        if (w == 0) signal(p.mbox, sb + 2, (2 * rb + ((lane >> 3) & 1)) * 8 + (lane & 7), fc, lane < 16);
        bar();
        if (w == 0) await(p.mbox, p.tmo, sb + 2, tb * 8 + et, 16, lane);
        bar(); STAMP(7);
      }
      {
        STAMP_PTR(4 * l + 3);
        STAMP(0);
        const int ln = fresh_v(lane);
        const unsigned a0 = tw * 8192 + ln * 16, f0 = kn_read_base(ln, fw);
        f32x16 acc = tile64_loop<GeoD>(lds, a0, [&](lds_t wb, int k) { return ld_tr(wb + f0 + k * 2048, wb + f0 + k * 2048 + 512); }, w, lane STAMP_PASS(4 * l + 3));
        add_res(acc, r0, r1);
        if (l == 1) {
          store_rowmajor(p.out, r0, r1, row0, col0, fresh_v(lane));
          if (w == 0) SPAN_END();
        } else {
          float v[16], m, m2;
          part_stats(r0, r1, v, m, m2);
          const int le = fresh_v(lane);
          store_stats((float*)p.gran, m, m2, row0, col0, le);
          STAMP(3);
          u32x2 st[8];
          for (unsigned spins = 0;; ++spins) {
            load_stats((const float*)p.gran, row0, le, st);
            bool ok = true;
#pragma unroll
            for (int i = 0; i < 8; ++i) ok = ok && st[i][1] != 0xffffffffu;
            if (__all(ok)) break;
            if (spins > SPIN_CAP) { if (lane == 0) __hip_atomic_store((gu32*)p.tmo, 2u, RLX_AGENT); break; }
            __builtin_amdgcn_s_sleep(1);
          }
          STAMP(4);
          float mean, rstd;
          combine_stats(st, mean, rstd);
          u32x4 n0, n1;
#pragma unroll
          for (int i = 0; i < 4; ++i) {
            n0[i] = pk2((v[2 * i] - mean) * rstd, (v[2 * i + 1] - mean) * rstd);
            n1[i] = pk2((v[8 + 2 * i] - mean) * rstd, (v[8 + 2 * i + 1] - mean) * rstd);
          }
          store_blocked(p.xn + XN_L, n0, n1, row0, col0, le);
          STAMP(5); WAIT_V(0); STAMP(6); bar();
          if (w == 0) signal(p.mbox, 3, (lane & 31) * 8 + et, tb, lane < 32);
          bar();
          if (w == 0) await(p.mbox, p.tmo, 3, tb * 8 + et, 32, lane);
          bar(); STAMP(7);
        }
      }
    }
  }
}

__device__ __forceinline__ float wave_sum(float v) {
#pragma unroll
  for (int o = 1; o < 64; o <<= 1) v += __shfl_xor(v, o);
  return v;
}
__global__ __launch_bounds__(512) void k_pre(Ptrs p) {
  const int lane = threadIdx.x & 63, wid = threadIdx.x >> 6;
  if (blockIdx.x < 256) {
    SPAN_BEGIN();
    const int row = blockIdx.x * 8 + wid;
    u32x4 t = *(const u32x4*)(p.x0 + (size_t)row * DM + lane * 8);
    const int gid = blockIdx.x * 512 + threadIdx.x;
    if (gid < 7 * 256 * 8) ((u32x4*)p.mbox)[gid] = u32x4{0u, 0u, 0u, 0u};
    if (gid < 16 * SEQ / 2) ((u32x4*)p.gran)[gid] = u32x4{~0u, ~0u, ~0u, ~0u};
    if (gid == 0) *p.tmo = 0u;
    float v[8], s = 0.f;
#pragma unroll
    for (int i = 0; i < 4; ++i) { v[2 * i] = bflo(t[i]); v[2 * i + 1] = bfhi(t[i]); s += v[2 * i] + v[2 * i + 1]; }
    const float m = wave_sum(s) * (1.f / 512.f);
    float m2 = 0.f;
#pragma unroll
    for (int i = 0; i < 8; ++i) { v[i] -= m; m2 += v[i] * v[i]; }
    const float rs = rsqrtf(wave_sum(m2) * (1.f / 512.f) + 1e-5f);
    u32x4 o = {pk2(v[0] * rs, v[1] * rs), pk2(v[2] * rs, v[3] * rs), pk2(v[4] * rs, v[5] * rs), pk2(v[6] * rs, v[7] * rs)};
    *(u32x4*)((char*)p.xn + ((size_t)(row >> 5) * 64 + lane) * 512 + (row & 31) * 16) = o;
  } else {
    __shared__ float red[8][64];
    const int b = blockIdx.x - 256;
    const u16* w = (b < 28 ? p.wg : p.wu) + (b % 28) * 64 + (lane & 7) * 8;
    float a[8] = {};
#pragma unroll
    for (int i = 0; i < 8; ++i) {
      u32x4 t = *(const u32x4*)(w + (size_t)(i * 64 + wid * 8 + (lane >> 3)) * FF);
#pragma unroll
      for (int e = 0; e < 4; ++e) { a[2 * e] += bflo(t[e]); a[2 * e + 1] += bfhi(t[e]); }
    }
#pragma unroll
    for (int e = 0; e < 8; ++e) { a[e] += __shfl_xor(a[e], 8); a[e] += __shfl_xor(a[e], 16); a[e] += __shfl_xor(a[e], 32); }
    if (lane < 8) {
#pragma unroll
      for (int e = 0; e < 8; ++e) red[wid][lane * 8 + e] = a[e];
    }
    __syncthreads();
    if (threadIdx.x < 64) {
      float s = 0.f;
#pragma unroll
      for (int i = 0; i < 8; ++i) s += red[i][threadIdx.x];
      const int c = (b % 28) * 64 + threadIdx.x, fc = c / 112, rem = c - fc * 112, jj = rem >> 4, i = perm23(rem & 15) + (b < 28 ? 0 : 16);
      const unsigned hi = pk2(s, 0.f) & 0xffffu, lo = pk2(s - bflo(hi), 0.f) & 0xffffu;
      u32x2 o = {hi | (hi << 16), lo};
      *(u32x2*)(p.csf + ((size_t)fc * 32 + i) * 16 + 2 * jj) = o;
    }
  }
}
}

extern "C" void kernel_launch(void* const* d_in, const int* in_sizes, int n_in, void* d_out, int out_size, void* d_ws, size_t ws_size, hipStream_t stream) {
  Ptrs p;
  p.x0 = (const u16*)d_in[0]; p.sre = (const u16*)d_in[1]; p.sim = (const u16*)d_in[2]; p.dre = (const u16*)d_in[3]; p.dim = (const u16*)d_in[4];
  p.wg = (const u16*)d_in[6]; p.wu = (const u16*)d_in[7]; p.wd = (const u16*)d_in[8];
  char* ws = (char*)d_ws;
  const size_t MB = 1 << 20;
  p.out = (u16*)d_out;
  p.xn = (u16*)ws;
  p.pq = (u16*)(ws + 4 * MB);
  p.xa = (u16*)(ws + 12 * MB);
  p.hh = (u16*)(ws + 16 * MB);
  p.stats = (float*)(ws + 31 * MB);
  p.stamps = (unsigned long long*)(ws + 32 * MB);
  p.csf = (unsigned*)(ws + 33 * MB);
  p.gran = (unsigned*)(ws + 34 * MB);
  p.mbox = (unsigned*)(ws + 35 * MB);
  p.tmo = (unsigned*)(ws + 36 * MB);
  (void)hipFuncSetAttribute((const void*)k_main, hipFuncAttributeMaxDynamicSharedMemorySize, LDS_BYTES);
  k_pre<<<256 + 56, 512, 0, stream>>>(p);
  k_main<<<256, 512, LDS_BYTES, stream>>>(p);
}
```

```cpp
#include <hip/hip_runtime.h>
#include <stdint.h>

namespace {
typedef unsigned short u16;
typedef short s16x4 __attribute__((ext_vector_type(4)));
typedef short s16x8 __attribute__((ext_vector_type(8)));
typedef __bf16 bf16x8 __attribute__((ext_vector_type(8)));
typedef __bf16 bf16x2 __attribute__((ext_vector_type(2)));
typedef float f32x2 __attribute__((ext_vector_type(2)));
typedef float f32x4 __attribute__((ext_vector_type(4)));
typedef float f32x16 __attribute__((ext_vector_type(16)));
typedef unsigned u32x4 __attribute__((ext_vector_type(4)));
typedef unsigned u32x2 __attribute__((ext_vector_type(2)));
#define LAS __attribute__((address_space(3)))
typedef __amdgpu_buffer_rsrc_t rsrc_t;
typedef LAS char* lds_t;
typedef __attribute__((address_space(1))) unsigned gu32;
typedef __attribute__((address_space(1))) unsigned long long gu64;
#define RLX_AGENT __ATOMIC_RELAXED, __HIP_MEMORY_SCOPE_AGENT

constexpr int SEQ = 2048, DM = 512, FF = 1792;
constexpr int LDS_BYTES = 163840, XB = 98304;
constexpr int SC1 = 16;
constexpr unsigned SPIN_CAP = 1u << 18;

__device__ __forceinline__ rsrc_t mk_rsrc(const void* p, unsigned bytes) { return __builtin_amdgcn_make_buffer_rsrc((void*)p, 0, bytes, 0x00020000); }
template <int AUX> __device__ __forceinline__ void dma16(rsrc_t rs, lds_t dst, unsigned voff, unsigned soff) {
  __builtin_amdgcn_raw_ptr_buffer_load_lds(rs, (LAS void*)dst, 16, voff, soff, 0, AUX);
}
#define WAIT_V(n) asm volatile("s_waitcnt vmcnt(%0)" ::"n"(n) : "memory")
#define SCHED() __builtin_amdgcn_sched_barrier(0)
#define WAIT_L0() __builtin_amdgcn_s_waitcnt(0xC07F)
__device__ __forceinline__ void bar() { asm volatile("" ::: "memory"); __builtin_amdgcn_s_barrier(); asm volatile("" ::: "memory"); }
__device__ __forceinline__ void cbar() { WAIT_L0(); bar(); }
__device__ __forceinline__ int fresh_v(int x) { asm volatile("" : "+v"(x)); return x; }
__device__ __forceinline__ int fresh_s(int x) { asm volatile("" : "+s"(x)); return x; }

template <int I> struct IC { static constexpr int v = I; };
template <int A, int B, class F> __device__ __forceinline__ void static_for(F&& f) {
  if constexpr (A < B) { f(IC<A>{}); static_for<A + 1, B>(f); }
}

#define PXI 3
#define ROT 4
#define TS 17
#define NA 0
#define NG 2
#define TS2 21
#define ROT2 0
#define ROTD 1
struct GeoP { static constexpr int NST = 32, NX = 8, nX = 2, NW = 6, nW = 4, OW = 0; };
struct GeoY { static constexpr int NST = 8,  NX = 4, nX = 4, NW = 6, nW = 4, OW = 4; };
struct GeoC { static constexpr int NST = 8,  NX = 4, nX = 4, NW = 3, nW = 7, OW = 0; };
struct GeoD { static constexpr int NST = 14, NX = 4, nX = 4, NW = 6, nW = 4, OW = 4; };
constexpr int GAP = 39;
#define L6(a, b, c, d, e, f) {a, b, c, d, e, f}
#define L3(a, b, c) {a, b, c}
#define EY L6(29, 30, 31, GAP, 27, 28)
#define EC L3(4, 6, GAP)
#define ED L6(6, 7, 7, GAP, GAP, GAP)
#define EP L6(9, 10, 11, 12, 13, GAP)
struct EarlyNone { static constexpr int at(int) { return -1; } };
struct EarlyY { static constexpr int at(int j) { constexpr int t[6] = EY; return j < 6 ? t[j] : -1; } };
struct EarlyC { static constexpr int at(int j) { constexpr int t[3] = EC; return j < 3 ? t[j] : -1; } };
struct EarlyD { static constexpr int at(int j) { constexpr int t[6] = ED; return j < 6 ? t[j] : -1; } };
struct EarlyP { static constexpr int at(int j) { constexpr int t[6] = EP; return j < 6 ? t[j] : -1; } };

constexpr int PLAN_MAX = 40;
template <class G, class MINE, class GN, class NEXT> struct Plan {
  int xstep[PLAN_MAX], wstep[PLAN_MAX], waitn[PLAN_MAX];
  constexpr Plan() : xstep{}, wstep{}, waitn{} {
    int nx = 0;
    for (int j = 0; j < G::NST; ++j) { xstep[j] = -2; wstep[j] = MINE::at(j) >= 0 ? -3 : -2; }
    for (int s = -1; s < G::NST; ++s) {
      const int lx = s - 1 + G::NX < G::NX - 1 ? G::NX - 1 : s - 1 + G::NX, lw = s - 1 + G::NW < G::NW - 1 ? G::NW - 1 : s - 1 + G::NW;
      for (int c = 0; c < (s < 0 ? PXI : 2); ++c) if (nx < G::NST && nx <= lx) xstep[nx++] = s;
      int c = 0;
      for (int j = 0; j < G::NST; ++j)
        if (wstep[j] == -2 && j <= lw && (s < 0 ? (j < PXI) : c < 2)) { wstep[j] = s; ++c; }
    }
    for (int s = 0; s < G::NST; ++s) {
      int total = 0, last = 0;
      for (int b = 0; b < PLAN_MAX; ++b)
        for (int j = 0; j < G::NST; ++j) if (MINE::at(j) == b) { total += G::nW; if (j == s) last = total; }
      for (int t = -1; t < s; ++t) {
        for (int j = 0; j < G::NST; ++j) {
          if (xstep[j] == t) { total += G::nX; if (j == s) last = total; }
          if (t < 0 && wstep[j] == t) { total += G::nW; if (j == s) last = total; }
        }
        if (t >= 0) {
          for (int j = 0; j < G::NST; ++j) if (wstep[j] == t) { total += G::nW; if (j == s) last = total; }
          for (int j = 0; j < GN::NST; ++j) if (NEXT::at(j) == t) total += GN::nW;
        }
      }
      waitn[s] = total - last > 63 ? 63 : total - last;
    }
  }
  constexpr bool ok() const {
    for (int j = 0; j < G::NST; ++j) if (xstep[j] >= j || xstep[j] < -1 || wstep[j] >= j || wstep[j] == -2) return false;
    return true;
  }
};
template <class G, class MINE, class GN, class NEXT> struct PlanOf { static constexpr Plan<G, MINE, GN, NEXT> v{}; };
struct NoHook { template <class S> __device__ __forceinline__ void operator()(S) const {} };
template <class G, class MINE, class GN, class NEXT, class FX, class FW, class FN, class FH = NoHook>
__device__ __forceinline__ void l_run(FX issueX, FW issueW, FN issueNext, FH hook = NoHook{}) {
  using PL = PlanOf<G, MINE, GN, NEXT>;
  static_assert(PL::v.ok(), "a stage would be waited for before it is issued");
  static_for<0, G::NST>([&](auto j) {
    if constexpr (PL::v.xstep[j.v] == -1) issueX(j);
    if constexpr (PL::v.wstep[j.v] == -1) issueW(j);
  });
  static_for<0, G::NST>([&](auto s) {
    WAIT_V(PL::v.waitn[s.v]);
    bar();
    hook(s);
    static_for<0, G::NST>([&](auto j) { if constexpr (PL::v.xstep[j.v] == s.v) issueX(j); });
    static_for<0, G::NST>([&](auto j) { if constexpr (PL::v.wstep[j.v] == s.v) issueW(j); });
    static_for<0, GN::NST>([&](auto j) { if constexpr (NEXT::at(j.v) == s.v) issueNext(j); });
  });
  bar();
  static_for<0, GN::NST>([&](auto j) { if constexpr (NEXT::at(j.v) == G::NST) issueNext(j); });
}
template <class G> constexpr int wslot(int j) { return (G::OW + j) % G::NW; }

__device__ __forceinline__ bf16x8 ld_b128(lds_t p) { return *(LAS bf16x8*)p; }
__device__ __forceinline__ bf16x8 ld_tr(lds_t p0, lds_t p1) {
  s16x4 a = __builtin_amdgcn_ds_read_tr16_b64_v4i16((LAS s16x4*)p0);
  s16x4 b = __builtin_amdgcn_ds_read_tr16_b64_v4i16((LAS s16x4*)p1);
  s16x8 f = __builtin_shufflevector(a, b, 0, 1, 2, 3, 4, 5, 6, 7);
  return __builtin_bit_cast(bf16x8, f);
}
__device__ __forceinline__ f32x16 mfma(bf16x8 a, bf16x8 b, f32x16 c) { return __builtin_amdgcn_mfma_f32_32x32x16_bf16(a, b, c, 0, 0, 0); }
__device__ __forceinline__ unsigned pk2(float a, float b) { f32x2 v = {a, b}; return __builtin_bit_cast(unsigned, __builtin_convertvector(v, bf16x2)); }
__device__ __forceinline__ float bflo(unsigned u) { return __uint_as_float(u << 16); }
__device__ __forceinline__ float bfhi(unsigned u) { return __uint_as_float(u & 0xffff0000u); }
__device__ __forceinline__ float xhalf_sum(float v) {
  auto r = __builtin_amdgcn_permlane32_swap(__float_as_uint(v), __float_as_uint(v), false, false);
  return __uint_as_float(r[0]) + __uint_as_float(r[1]);
}
template <int N> __device__ __forceinline__ float ror16(float v) {
  return __int_as_float(__builtin_amdgcn_update_dpp(0, __float_as_int(v), 0x120 + N, 0xf, 0xf, false));
}
__device__ __forceinline__ float xrow_sum(float v) {
  auto r = __builtin_amdgcn_permlane16_swap(__float_as_uint(v), __float_as_uint(v), false, false);
  return __uint_as_float(r[0]) + __uint_as_float(r[1]);
}
template <int CTRL> __device__ __forceinline__ float dpp(float v) { return __int_as_float(__builtin_amdgcn_update_dpp(0, __float_as_int(v), CTRL, 0xf, 0xf, false)); }
__device__ __forceinline__ float oct_sum(float v) {
  v += dpp<0xB1>(v);
  v += dpp<0x4E>(v);
  return v + dpp<0x141>(v);
}
__device__ __forceinline__ unsigned km_read_base(int row, int h) { return row * 128 + ((h ^ ((row >> 1) & 7)) << 4); }
__device__ __forceinline__ int perm23(int i) { return (i & ~12) | ((i & 4) << 1) | ((i & 8) >> 1); }
__device__ __forceinline__ unsigned km_src(int lw, int lane, int pitch) {
  return (8 * lw + (lane >> 3)) * pitch + (((lane & 7) ^ (((lw & 1) << 2) | (lane >> 4))) << 4);
}
__device__ __forceinline__ unsigned kn_src(int lw, int lane, int pitch) {
  return (8 * lw + (lane >> 3)) * pitch + (((lane & 7) ^ (((lane >> 4) & 1) << 2)) << 4);
}
__device__ __forceinline__ unsigned kn_read_base(int lane, int ft) {
  int h = lane >> 5, blk = (lane >> 4) & 1, q = (lane & 15) >> 2, p = lane & 3, pp = ((p & 1) << 1) | (p >> 1);
  int c = 4 * ft + 2 * blk + (pp >> 1);
  return (8 * h + q) * 128 + ((c ^ (((q >> 1) & 1) << 2)) << 4) + 8 * (pp & 1);
}

#define SPAN_BEGIN()
#define SPAN_MAIN()
#define SPAN_END()
#define STAMP(i)
#define STAMP_ARG
#define STAMP_PASS(ph)
#define STAMP_PTR(ph)

struct Ptrs {
  unsigned long long* stamps;
  const u16 *x0, *sre, *sim, *dre, *dim, *wg, *wu, *wd;
  u16 *out, *xn, *pq, *xa, *hh;
  float* stats;
  unsigned *gran, *tmo;
  unsigned long long *mbox, *epoch;
};
constexpr size_t XN_L = (size_t)SEQ * DM, PQ_L = (size_t)SEQ * 1024, HH_L = (size_t)SEQ * FF, ST_L = (size_t)16 * SEQ * 2;

typedef unsigned long long u64;
__device__ __forceinline__ void signal(u64* mbox, u64 tag, int seam, int consumer, int slot, bool active) {
  if (active) __hip_atomic_store((gu64*)mbox + ((size_t)seam * 256 + consumer) * 32 + slot, tag, RLX_AGENT);
}
__device__ __forceinline__ void await(u64* mbox, u64 tag, unsigned* tmo, int seam, int me, int nprod, int lane) {
  gu64* q = (gu64*)mbox + ((size_t)seam * 256 + me) * 32 + (lane < nprod ? lane : 0);
  for (unsigned spins = 0;; ++spins) {
    const u64 x = __hip_atomic_load(q, RLX_AGENT);
    if (__all(x == tag)) break;
    if (spins > SPIN_CAP) { if (lane == 0) __hip_atomic_store((gu32*)tmo, 1u, RLX_AGENT); break; }
    __builtin_amdgcn_s_sleep(1);
  }
}

struct PLoader {
  rsrc_t rre, rim; unsigned vw, vx; lds_t lds; int lw, k0;
  __device__ __forceinline__ PLoader(lds_t lds_, const Ptrs& p, int tb, int et, int lw_, int lane, int rot) : lds(lds_), lw(lw_), k0((rot * et) & 31) {
    rre = mk_rsrc(p.sre + (size_t)tb * 64 * SEQ, 64 * SEQ * 2); rim = mk_rsrc(p.sim + (size_t)tb * 64 * SEQ, 64 * SEQ * 2);
    vw = km_src(lw, lane, SEQ * 2);
    vx = (lane >> 5) * 512 + (((lane & 31) ^ (4 * ((2 * lw + (lane >> 5)) & 3))) << 4);
  }
  template <class S> __device__ __forceinline__ void W(S s) {
    const int ks = (s.v + k0) & 31;
    static_for<0, 4>([&](auto n) { dma16<0>(n.v < 2 ? rre : rim, lds + wslot<GeoP>(s.v) * 16384 + (lw + 4 * n.v) * 1024, vw, ks * 128 + (n.v & 1) * 32 * SEQ * 2); });
  }
  template <class S> __device__ __forceinline__ void X(S s, rsrc_t rx) {
    const int ks = (s.v + k0) & 31;
    static_for<0, 2>([&](auto n) { dma16<SC1>(rx, lds + XB + (s.v % 8) * 8192 + n.v * 4096 + lw * 1024, vx, (2 * ks + n.v) * 64 * 512); });
  }
  __device__ __forceinline__ rsrc_t xr(const u16* xn, int et) { return mk_rsrc((const char*)xn + (8 * et + 2 * lw) * 512, SEQ * DM * 2 - (8 * et + 2 * lw) * 512); }
};
struct YLoader {
  rsrc_t rre, rim; unsigned vw, vx; lds_t lds; int lw;
  __device__ __forceinline__ YLoader(lds_t lds_, const Ptrs& p, int et, int lw_, int lane) : lds(lds_), lw(lw_) {
    rre = mk_rsrc(p.dre + (size_t)et * 64 * DM, 64 * DM * 2); rim = mk_rsrc(p.dim + (size_t)et * 64 * DM, 64 * DM * 2);
    vw = km_src(lw, lane, DM * 2); vx = lane * 16;
  }
  template <class S> __device__ __forceinline__ void W(S s) {
    static_for<0, 4>([&](auto i) {
      constexpr int kb = i.v >> 1, n = i.v & 1;
      dma16<0>(s.v < 4 ? rre : rim, lds + wslot<GeoY>(s.v) * 16384 + kb * 8192 + (lw + 4 * n) * 1024, vw, (2 * (s.v & 3) + kb) * 128 + n * 32 * DM * 2);
    });
  }
  template <class S> __device__ __forceinline__ void X(S s, rsrc_t rx) {
    static_for<0, 4>([&](auto i) {
      constexpr int blk = i.v >> 1, n = i.v & 1;
      dma16<SC1>(rx, lds + XB + (s.v % 4) * 16384 + blk * 8192 + n * 1024 + lw * 2048, vx, blk * 128 * 512 + (16 * s.v + 2 * n) * 512);
    });
  }
  __device__ __forceinline__ rsrc_t xr(const u16* pq, int tb) { return mk_rsrc((const char*)pq + ((size_t)2 * tb * 128 + 4 * lw) * 512, 2 * 128 * 512 - 4 * lw * 512); }
};
constexpr int C_WS = 28672;
struct CLoader {
  const char* wsrc[7]; unsigned vx; lds_t lds; int lw;
  __device__ __forceinline__ CLoader(lds_t lds_, const Ptrs& p, int fc, int lw_, int lane) : lds(lds_), lw(lw_) {
    vx = lane * 16;
#pragma unroll
    for (int n = 0; n < 7; ++n) {
      int idx = 64 * n + lane, kr = idx / 28, c = idx - kr * 28;
      const u16* base = c < 14 ? p.wg : p.wu;
      int cc = c < 14 ? c : c - 14;
      wsrc[n] = (const char*)(base + (size_t)(16 * lw + kr) * FF + 112 * fc + 8 * cc);
    }
  }
  template <class S> __device__ __forceinline__ void W(S s) {
    static_for<0, 7>([&](auto n) {
      __builtin_amdgcn_global_load_lds((const unsigned*)(wsrc[n.v] + (size_t)s.v * 64 * FF * 2), (LAS unsigned*)(lds + wslot<GeoC>(s.v) * C_WS + lw * 7168 + n.v * 1024), 16, 0, 0);
    });
  }
  template <class S> __device__ __forceinline__ void X(S s, rsrc_t rx) {
    static_for<0, 4>([&](auto n) { dma16<SC1>(rx, lds + XB + (s.v % 4) * 16384 + lw * 4096 + n.v * 1024, vx, (8 * s.v + 2 * n.v) * 512); });
  }
  __device__ __forceinline__ rsrc_t xr(const u16* xa, int rb) { return mk_rsrc((const char*)xa + (size_t)(4 * rb + lw) * 64 * 512, 64 * 512); }
};
struct DLoader {
  rsrc_t rw; unsigned vw, vx; lds_t lds; int lw, k0;
  __device__ __forceinline__ DLoader(lds_t lds_, const Ptrs& p, int tb, int et, int lw_, int lane) : lds(lds_), lw(lw_), k0(ROTD ? (tb * 3) % 14 : 0) {
    rw = mk_rsrc(p.wd + et * 64, FF * DM * 2 - et * 128);
    vw = kn_src(lw, lane, DM * 2); vx = lane * 16;
  }
  template <class S> __device__ __forceinline__ void W(S s) {
    const int ks = s.v + k0 < 14 ? s.v + k0 : s.v + k0 - 14;
    static_for<0, 4>([&](auto i) {
      constexpr int kb = i.v >> 1, n = i.v & 1;
      dma16<0>(rw, lds + wslot<GeoD>(s.v) * 16384 + kb * 8192 + (lw + 4 * n) * 1024, vw, ((2 * ks + kb) * 64 + 32 * n) * DM * 2);
    });
  }
  template <class S> __device__ __forceinline__ void X(S s, rsrc_t rx) {
    const int ks = s.v + k0 < 14 ? s.v + k0 : s.v + k0 - 14;
    static_for<0, 4>([&](auto i) {
      constexpr int blk = i.v >> 1, n = i.v & 1;
      dma16<SC1>(rx, lds + XB + (s.v % 4) * 16384 + blk * 8192 + n * 1024 + lw * 2048, vx, blk * 224 * 512 + (16 * ks + 2 * n) * 512);
    });
  }
  __device__ __forceinline__ rsrc_t xr(const u16* hh, int tb) { return mk_rsrc((const char*)hh + ((size_t)2 * tb * 224 + 4 * lw) * 512, 2 * 224 * 512 - 4 * lw * 512); }
};
template <class G, class MINE, class LD> __device__ __forceinline__ void l_seam(LD& L) {
  bar(); bar();
  static_for<0, G::NST>([&](auto j) { if constexpr (MINE::at(j.v) == GAP) L.W(j); });
  bar();
}
template <class G, class MINE, class GN, class NEXT, class LD, class LN>
__device__ __forceinline__ void l_phase(LD& L, rsrc_t rx, LN& N) {
  l_run<G, MINE, GN, NEXT>([&](auto s) { L.X(s, rx); }, [&](auto s) { L.W(s); }, [&](auto s) { N.W(s); });
}

__device__ __forceinline__ void p_compute(lds_t lds, u16* pq, int tb, int et, int w, int lane, const char* t0, const char* t1, const char* t2, const char* t3, bool touch, unsigned& dummy STAMP_ARG) {
  const int r = lane & 31, h = lane >> 5;
  const unsigned a0 = km_read_base(32 * w + r, h);
  const int bl = (lane >> 4) & 1, q4 = (lane & 15) >> 2, pp = ((lane & 1) << 1) | ((lane >> 1) & 1), cc0 = 2 * bl + (pp >> 1);
  const unsigned f0 = cc0 * 512 + (((8 * h + q4) ^ (4 * cc0)) << 4) + 8 * (pp & 1), f1 = f0 ^ 64;
  f32x16 acc0 = {}, acc1 = {};
  unsigned wo = GeoP::OW * 16384, xo = XB;
  struct Fr { bf16x8 tok[4], fa[4], fb[4]; };
  Fr A, B;
  auto rdt = [&](Fr& f, int k) { f.tok[k] = ld_b128(lds + wo + (a0 ^ (k << 5))); };
  auto rda = [&](Fr& f, int k) { lds_t xb = lds + xo + (k >> 1) * 4096 + (k & 1) * 256; f.fa[k] = ld_tr(xb + f0, xb + f1); };
  auto rdb = [&](Fr& f, int k) { lds_t xb = lds + xo + (k >> 1) * 4096 + (k & 1) * 256 + 2048; f.fb[k] = ld_tr(xb + f0, xb + f1); };
  auto adv = [&]() {
    wo = wo + 16384 == 6 * 16384 ? 0 : wo + 16384;
    xo = xo + 8192 == LDS_BYTES ? XB : xo + 8192;
  };
  auto step = [&](Fr& nx, const Fr& cu) {
    cbar();
#pragma unroll
    for (int k = 0; k < 4; ++k) {
      rdt(nx, k); rda(nx, k); SCHED();
      acc0 = mfma(cu.fa[k], cu.tok[k], acc0); SCHED();
      rdb(nx, k); SCHED();
      acc1 = mfma(cu.fb[k], cu.tok[k], acc1); SCHED();
    }
    adv();
  };
  cbar();
  STAMP(1);
#pragma unroll
  for (int k = 0; k < 4; ++k) { rdt(A, k); rda(A, k); rdb(A, k); }
  adv();
#pragma unroll 1
  for (int s = 1; s < 31; s += 2) {
    step(B, A);
    if (touch && s == TS) {
      asm volatile("global_load_dword %0, %1, off" : "+v"(dummy) : "v"(t0) : "memory");
      asm volatile("global_load_dword %0, %1, off" : "+v"(dummy) : "v"(t1) : "memory");
    }
    if (touch && s == TS2) {
      asm volatile("global_load_dword %0, %1, off" : "+v"(dummy) : "v"(t2) : "memory");
      asm volatile("global_load_dword %0, %1, off" : "+v"(dummy) : "v"(t3) : "memory");
    }
    step(A, B);
  }
  step(B, A);
  cbar();
  STAMP(2);
#pragma unroll
  for (int k = 0; k < 4; ++k) { acc0 = mfma(B.fa[k], B.tok[k], acc0); acc1 = mfma(B.fb[k], B.tok[k], acc1); }
  const float sg = w >= 2 ? -1.f : 1.f;
  const rsrc_t ro = mk_rsrc((char*)pq + ((size_t)(2 * tb + (w & 1)) * 128 + (w >> 1) * 64 + 8 * et) * 512, 4096);
#pragma unroll
  for (int ft = 0; ft < 2; ++ft)
#pragma unroll
    for (int q = 0; q < 2; ++q) {
      const f32x16& a = ft ? acc1 : acc0;
      u32x4 v = {pk2(sg * a[8 * q], sg * a[8 * q + 1]), pk2(sg * a[8 * q + 2], sg * a[8 * q + 3]), pk2(sg * a[8 * q + 4], sg * a[8 * q + 5]), pk2(sg * a[8 * q + 6], sg * a[8 * q + 7])};
      __builtin_amdgcn_raw_buffer_store_b128(v, ro, lane * 16 + ft * 2048 + q * 1024, 0, SC1);
    }
}

__device__ __forceinline__ void add_res(const f32x16& acc, u32x4& r0, u32x4& r1) {
#pragma unroll
  for (int i = 0; i < 4; ++i) {
    r0[i] = pk2(acc[2 * i] + bflo(r0[i]), acc[2 * i + 1] + bfhi(r0[i]));
    r1[i] = pk2(acc[8 + 2 * i] + bflo(r1[i]), acc[8 + 2 * i + 1] + bfhi(r1[i]));
  }
}
__device__ __forceinline__ void store_rowmajor(u16* xo, const u32x4& o0, const u32x4& o1, int row0, int col0, int lane) {
  const rsrc_t ro = mk_rsrc(xo + (size_t)row0 * DM + col0, 32 * DM * 2);
  const unsigned vo = (lane & 31) * DM * 2 + (lane >> 5) * 16;
  __builtin_amdgcn_raw_buffer_store_b128(o0, ro, vo, 0, 0);
  __builtin_amdgcn_raw_buffer_store_b128(o1, ro, vo + 32, 0, 0);
}
__device__ __forceinline__ void store_blocked(u16* xo, const u32x4& o0, const u32x4& o1, int row0, int col0, int lane) {
  const rsrc_t ro = mk_rsrc((char*)xo + ((size_t)(row0 >> 5) * 64 + (col0 >> 3)) * 512, 2048);
  __builtin_amdgcn_raw_buffer_store_b128(o0, ro, lane * 16, 0, SC1);
  __builtin_amdgcn_raw_buffer_store_b128(o1, ro, lane * 16 + 1024, 0, SC1);
}
__device__ __forceinline__ void part_stats(const u32x4& o0, const u32x4& o1, float (&v)[16], float& m, float& m2) {
  float s0 = 0.f, s1 = 0.f;
#pragma unroll
  for (int i = 0; i < 4; ++i) { v[2 * i] = bflo(o0[i]); v[2 * i + 1] = bfhi(o0[i]); v[8 + 2 * i] = bflo(o1[i]); v[8 + 2 * i + 1] = bfhi(o1[i]); }
#pragma unroll
  for (int i = 0; i < 8; ++i) { s0 += v[i]; s1 += v[8 + i]; }
  m = xhalf_sum(s0 + s1) * (1.f / 32.f);
  float q0 = 0.f, q1 = 0.f;
#pragma unroll
  for (int i = 0; i < 8; ++i) { float d = v[i] - m, e = v[8 + i] - m; q0 += d * d; q1 += e * e; }
  m2 = xhalf_sum(q0 + q1);
}
__device__ __forceinline__ void store_stats(float* stats, float m, float m2, int row0, int col0, int lane) {
  const rsrc_t rs = mk_rsrc(stats + ((size_t)(col0 >> 5) * SEQ + row0) * 2, 32 * 8);
  if (lane < 32) { u32x2 sv = {__float_as_uint(m), __float_as_uint(m2)}; __builtin_amdgcn_raw_buffer_store_b64(sv, rs, lane * 8, 0, SC1); }
}
__device__ __forceinline__ void load_stats(const float* stats, int row0, int lane, u32x2 (&t)[8]) {
  gu64* q = (gu64*)stats + row0 + (lane & 31) + (size_t)(8 * (lane >> 5)) * SEQ;
#pragma unroll
  for (int i = 0; i < 8; ++i) { unsigned long long x = __hip_atomic_load(q + i * SEQ, RLX_AGENT); t[i] = u32x2{(unsigned)x, (unsigned)(x >> 32)}; }
}
__device__ __forceinline__ void store_gran(unsigned* gran, u64 tag, float m, float m2, int row0, int col0, int lane) {
  const rsrc_t rs = mk_rsrc(gran + ((size_t)(col0 >> 5) * SEQ + row0) * 4, 32 * 16);
  const unsigned a = __float_as_uint(m), b = __float_as_uint(m2);
  if (lane < 32) __builtin_amdgcn_raw_buffer_store_b128(u32x4{a, a ^ (unsigned)tag, b, b ^ (unsigned)(tag >> 32)}, rs, lane * 16, 0, SC1);
}
__device__ __forceinline__ bool load_gran(const unsigned* gran, u64 tag, int row0, int lane, u32x2 (&t)[8]) {
  const rsrc_t rs = mk_rsrc(gran + (size_t)row0 * 4, 16 * SEQ * 16 - row0 * 16);
  bool ok = true;
#pragma unroll
  for (int i = 0; i < 8; ++i) {
    const u32x4 g = __builtin_amdgcn_raw_buffer_load_b128(rs, (lane & 31) * 16 + (8 * (lane >> 5) + i) * SEQ * 16, 0, SC1);
    ok = ok && (g[0] ^ g[1]) == (unsigned)tag && (g[2] ^ g[3]) == (unsigned)(tag >> 32);
    t[i] = u32x2{g[0], g[2]};
  }
  return ok;
}
__device__ __forceinline__ void combine_stats(const u32x2 (&t)[8], float& mean, float& rstd) {
  float ms = 0.f, m2 = 0.f;
#pragma unroll
  for (int i = 0; i < 8; ++i) { ms += __uint_as_float(t[i][0]); m2 += __uint_as_float(t[i][1]); }
  mean = xhalf_sum(ms) * (1.f / 16.f);
#pragma unroll
  for (int i = 0; i < 8; ++i) { float d = __uint_as_float(t[i][0]) - mean; m2 += 32.f * d * d; }
  rstd = rsqrtf(xhalf_sum(m2) * (1.f / 512.f) + 1e-5f);
}

template <class G, class FF_> __device__ __forceinline__ f32x16 tile64_loop(lds_t lds, unsigned a0, FF_ feat, int w, int lane STAMP_ARG) {
  f32x16 acc = {};
  unsigned wo = G::OW * 16384, xo = XB;
  struct Fr { bf16x8 tok[8], f[8]; };
  Fr A, B;
  auto rd = [&](Fr& fr, int k) {
    fr.tok[k] = ld_b128(lds + xo + a0 + k * 1024);
    fr.f[k] = feat(lds + wo, k);
  };
  auto adv = [&]() {
    wo = wo + 16384 == 6 * 16384 ? 0 : wo + 16384;
    xo = xo + 16384 == LDS_BYTES ? XB : xo + 16384;
  };
  auto step = [&](Fr& nx, const Fr& cu) {
    cbar();
#pragma unroll
    for (int k = 0; k < 8; ++k) { rd(nx, k); SCHED(); acc = mfma(cu.f[k], cu.tok[k], acc); SCHED(); }
    adv();
  };
  cbar();
  STAMP(1);
#pragma unroll
  for (int k = 0; k < 8; ++k) rd(A, k);
  adv();
#pragma unroll 1
  for (int s = 1; s < G::NST - 1; s += 2) { step(B, A); step(A, B); }
  step(B, A);
  cbar();
  STAMP(2);
#pragma unroll
  for (int k = 0; k < 8; ++k) acc = mfma(B.f[k], B.tok[k], acc);
  return acc;
}

__device__ __forceinline__ void c_compute(lds_t lds, const float* stats, u16* hh, int rb, int fc, int w, int lane STAMP_ARG) {
  const int r = lane & 31, h = lane >> 5;
  const unsigned a0 = w * 4096 + lane * 16;
  const int blk = (lane >> 4) & 1, q = (lane & 15) >> 2, pp = ((lane & 1) << 1) | ((lane >> 1) & 1);
  const unsigned f0 = (8 * h + q) * 448 + blk * 224 + 8 * pp;
  f32x16 acc[7];
#pragma unroll
  for (int j = 0; j < 7; ++j) acc[j] = f32x16{};
  unsigned wo = GeoC::OW * C_WS, xo = XB;
  struct Fr { bf16x8 tok, f[7]; };
  Fr A, B;
  auto rdt = [&](Fr& fr, int k) { fr.tok = ld_b128(lds + xo + a0 + k * 1024); };
  auto rdf = [&](Fr& fr, int k, int j) { lds_t wb = lds + wo + f0; fr.f[j] = ld_tr(wb + 16 * k * 448 + 32 * j, wb + (16 * k + 4) * 448 + 32 * j); };
  float mean, rstd;
  auto center = [&](bf16x8& t, int e) {
    u32x4 v = __builtin_bit_cast(u32x4, t);
    const unsigned u = v[e];
    v[e] = pk2(bflo(u) - mean, bfhi(u) - mean);
    t = __builtin_bit_cast(bf16x8, v);
  };
  auto grp = [&](Fr& nx, const Fr& cu, int k, bool rdn) {
#pragma unroll
    for (int j = 0; j < 7; ++j) {
      if (rdn) { if (j == 0) rdt(nx, k); rdf(nx, k, j); if (j >= 3) center(nx.tok, j - 3); }
      SCHED();
      acc[j] = mfma(cu.f[j], cu.tok, acc[j]);
      SCHED();
    }
  };
  auto adv = [&]() {
    wo = wo + C_WS == 3 * C_WS ? 0 : wo + C_WS;
    xo = xo + 16384 == LDS_BYTES ? XB : xo + 16384;
  };
  {
    u32x2 st[8];
    load_stats(stats, 128 * rb + 32 * w, lane, st);
    combine_stats(st, mean, rstd);
    asm volatile("" : "+v"(mean), "+v"(rstd));
  }
  cbar();
  STAMP(1);
  rdt(A, 0);
#pragma unroll
  for (int j = 0; j < 7; ++j) rdf(A, 0, j);
#pragma unroll
  for (int e = 0; e < 4; ++e) center(A.tok, e);
#pragma unroll 1
  for (int s = 0; s < 7; ++s) {
    grp(B, A, 1, true);
    grp(A, B, 2, true);
    grp(B, A, 3, true);
    adv();
    cbar();
    grp(A, B, 0, true);
  }
  grp(B, A, 1, true);
  grp(A, B, 2, true);
  grp(B, A, 3, true);
  cbar();
  STAMP(2);
  grp(A, B, 0, false);
  const rsrc_t ro = mk_rsrc((char*)hh + ((size_t)(4 * rb + w) * 224 + 14 * fc) * 512, 7168);
  const float ng = -1.4426950408889634f * rstd;
#pragma unroll
  for (int j = 0; j < 7; ++j) {
    float o[8];
#pragma unroll
    for (int i = 0; i < 8; i += 2) {
      f32x2 g = {acc[j][i], acc[j][i + 1]}, u = {acc[j][8 + i], acc[j][9 + i]};
      g *= ng; u *= rstd;
      f32x2 e = {__builtin_amdgcn_exp2f(g[0]), __builtin_amdgcn_exp2f(g[1])};
      e += 1.f;
      f32x2 sg = {__builtin_amdgcn_rcpf(e[0]), __builtin_amdgcn_rcpf(e[1])};
      u *= sg;
      o[i] = u[0]; o[i + 1] = u[1];
    }
    u32x4 v = {pk2(o[0], o[1]), pk2(o[2], o[3]), pk2(o[4], o[5]), pk2(o[6], o[7])};
    __builtin_amdgcn_raw_buffer_store_b128(v, ro, lane * 16 + 1024 * j, 0, SC1);
    SCHED();
  }
}

__device__ __forceinline__ void ln0_tile(const u16* x, u16* xn, int tb, int et, int w, int lane STAMP_ARG) {
  STAMP(0);
#define LN0 1
  const bf16x2 one = __builtin_bit_cast(bf16x2, 0x3f803f80u);
  const rsrc_t rs = mk_rsrc(x + (size_t)(64 * tb + 16 * w) * DM, 16 * DM * 2);
  const rsrc_t ro = mk_rsrc((char*)xn + ((size_t)(2 * tb + (w >> 1)) * 64 + 8 * et) * 512 + (w & 1) * 256, 4096);
  const int r = lane >> 3, c = lane & 7;
  u32x4 t[2][8];
#pragma unroll
  for (int i = 0; i < 8; ++i)
#pragma unroll
    for (int g = 0; g < 2; ++g) t[g][i] = __builtin_amdgcn_raw_buffer_load_b128(rs, (8 * g + r) * DM * 2 + c * 16, ((i + et) & 7) * 128, 0);
  SCHED();
  bar();
  STAMP(1);
  float mean[2], rstd[2];
#pragma unroll
  for (int g = 0; g < 2; ++g) {
    float s0 = 0.f, s1 = 0.f, q0 = 0.f, q1 = 0.f;
#pragma unroll
    for (int i = 0; i < 8; ++i)
#pragma unroll
      for (int e = 0; e < 4; e += 2) {
        const unsigned ua = t[g][i][e], ub = t[g][i][e + 1];
        const bf16x2 a = __builtin_bit_cast(bf16x2, ua), b = __builtin_bit_cast(bf16x2, ub);
        s0 = __builtin_amdgcn_fdot2_f32_bf16(a, one, s0, false); s1 = __builtin_amdgcn_fdot2_f32_bf16(b, one, s1, false);
        q0 = __builtin_amdgcn_fdot2_f32_bf16(a, a, q0, false);   q1 = __builtin_amdgcn_fdot2_f32_bf16(b, b, q1, false);
      }
    const float s = oct_sum(s0 + s1), q = oct_sum(q0 + q1);
    mean[g] = s * (1.f / 512.f);
    rstd[g] = rsqrtf(fmaxf(q * (1.f / 512.f) - mean[g] * mean[g], 0.f) + 1e-5f);
  }
  STAMP(2);
#pragma unroll
  for (int g = 0; g < 2; ++g) {
    u32x4 o;
#pragma unroll
    for (int e = 0; e < 4; ++e) o[e] = pk2((bflo(t[g][0][e]) - mean[g]) * rstd[g], (bfhi(t[g][0][e]) - mean[g]) * rstd[g]);
    __builtin_amdgcn_raw_buffer_store_b128(o, ro, c * 512 + (8 * g + r) * 16, 0, SC1);
  }
}
struct EarlyP0 { static constexpr int at(int j) { return j < NA + NG ? 0 : -1; } };

__global__ __launch_bounds__(512, 2) __attribute__((amdgpu_waves_per_eu(2, 2))) void k_main(Ptrs p) {
  extern __shared__ __attribute__((aligned(16))) char smem[];
  lds_t lds = (lds_t)smem;
  const int lane = threadIdx.x & 63, wid = __builtin_amdgcn_readfirstlane(threadIdx.x >> 6);
  const int xcd = blockIdx.x & 7, j = blockIdx.x >> 3;
  const int tb = xcd * 4 + (j & 3), et = j >> 2;
  const int rb = (xcd & 1) * 8 + (j & 7), fc = (xcd >> 1) * 4 + (j >> 3);
  SPAN_BEGIN();
  SPAN_MAIN();
  if (wid >= 4) {
    {
      PLoader L(lds, p, fresh_s(tb), fresh_s(et), fresh_s(wid - 4), fresh_v(lane), ROT);
      YLoader N(lds, p, fresh_s(et), fresh_s(wid - 4), fresh_v(lane));
      const rsrc_t rx = L.xr(p.xn, et);
      bar();
      static_for<0, NA>([&](auto j) { L.W(j); });
      bar(); bar();
      static_for<NA, NA + NG>([&](auto j) { L.W(j); });
      bar();
      l_phase<GeoP, EarlyP0, GeoY, EarlyY>(L, rx, N);
    }
#pragma unroll 1
    for (int l = 0; l < 2; ++l) {
      {
        YLoader L(lds, p, fresh_s(et), fresh_s(wid - 4), fresh_v(lane));
        CLoader N(lds, p, fresh_s(fc), fresh_s(wid - 4), fresh_v(lane));
        const rsrc_t rx = L.xr(p.pq + l * PQ_L, fresh_s(tb));
        l_seam<GeoY, EarlyY>(L);
        l_phase<GeoY, EarlyY, GeoC, EarlyC>(L, rx, N);
      }
      {
        CLoader L(lds, p, fresh_s(fc), fresh_s(wid - 4), fresh_v(lane));
        DLoader N(lds, p, fresh_s(tb), fresh_s(et), fresh_s(wid - 4), fresh_v(lane));
        const rsrc_t rx = L.xr(p.xa + l * XN_L, fresh_s(rb));
        l_seam<GeoC, EarlyC>(L);
        l_phase<GeoC, EarlyC, GeoD, EarlyD>(L, rx, N);
      }
      if (l == 0) {
        {
          DLoader L(lds, p, fresh_s(tb), fresh_s(et), fresh_s(wid - 4), fresh_v(lane));
          PLoader N(lds, p, fresh_s(tb), fresh_s(et), fresh_s(wid - 4), fresh_v(lane), ROT2);
          const rsrc_t rx = L.xr(p.hh, fresh_s(tb));
          l_seam<GeoD, EarlyD>(L);
          l_phase<GeoD, EarlyD, GeoP, EarlyP>(L, rx, N);
        }
        PLoader L(lds, p, fresh_s(tb), fresh_s(et), fresh_s(wid - 4), fresh_v(lane), ROT2);
        YLoader N(lds, p, fresh_s(et), fresh_s(wid - 4), fresh_v(lane));
        const rsrc_t rx = L.xr(p.xn + XN_L, fresh_s(et));
        l_seam<GeoP, EarlyP>(L);
        l_phase<GeoP, EarlyP, GeoY, EarlyY>(L, rx, N);
      } else {
        DLoader L(lds, p, fresh_s(tb), fresh_s(et), fresh_s(wid - 4), fresh_v(lane));
        const rsrc_t rx = L.xr(p.hh + HH_L, fresh_s(tb));
        l_seam<GeoD, EarlyD>(L);
        l_phase<GeoD, EarlyD, GeoP, EarlyNone>(L, rx, L);
      }
    }
  } else {
    const int w = wid, tw = w & 1, fw = w >> 1;
    const int row0 = 64 * tb + 32 * tw, col0 = 64 * et + 32 * fw;
    unsigned dummy = 0;
    const u64 found = __hip_atomic_load((gu64*)p.epoch, RLX_AGENT);
    u32x4 r0, r1;
    {
      const int r = lane & 31, h = lane >> 5;
      const rsrc_t rr = mk_rsrc(p.x0 + (size_t)row0 * DM + col0, 32 * DM * 2);
      r0 = __builtin_amdgcn_raw_buffer_load_b128(rr, r * DM * 2 + h * 16, 0, 0);
      r1 = __builtin_amdgcn_raw_buffer_load_b128(rr, r * DM * 2 + h * 16 + 32, 0, 0);
    }
    STAMP_PTR(9);
    ln0_tile(p.x0, p.xn, tb, et, w, lane STAMP_PASS(9));
    STAMP(3);
    const u64 e = found + 0x100000001ull;
    const u64 tag = ((u64)(unsigned)__builtin_amdgcn_readfirstlane((unsigned)(e >> 32)) << 32) | (unsigned)__builtin_amdgcn_readfirstlane((unsigned)e);
    WAIT_V(0); STAMP(4); bar(); STAMP(5);
    if (w == 0) signal(p.mbox, tag, 0, (lane & 31) * 8 + et, tb, lane < 32);
    bar();
    if (w == 0) await(p.mbox, tag, p.tmo, 0, tb * 8 + et, 32, lane);
    STAMP(6);
    bar(); STAMP(7);
#pragma unroll 1
    for (int l = 0; l < 2; ++l) {
      const int sb = 4 * l + 1;
      {
        STAMP_PTR(4 * l + 0);
        STAMP(0);
        const int ln = fresh_v(lane), li = blockIdx.x * 64 + ln, lj = blockIdx.x * 32 + (ln & 31);
        const char* t0 = li < 14336 ? (const char*)p.wd + (size_t)li * 128 : (const char*)p.wd;
        const char* t1 = (ln < 32 ? (const char*)p.dre : (const char*)p.dim) + (size_t)(lj & 4095) * 128;
        const char* t2 = (const char*)p.wg + (size_t)(li < 14336 ? li : 0) * 128;
        const char* t3 = (const char*)p.wu + (size_t)(li < 14336 ? li : 0) * 128;
        p_compute(lds, p.pq + l * PQ_L, fresh_s(tb), fresh_s(et), fresh_s(w), fresh_v(lane), t0, t1, t2, t3, l == 0 && w == 0 && blockIdx.x < 224, dummy STAMP_PASS(4 * l + 0));
        STAMP(5); WAIT_V(0); STAMP(6); bar();
        asm volatile("" :: "v"(dummy));
        if (w == 0) signal(p.mbox, tag, sb + 0, tb * 8 + (lane & 7), et, lane < 8);
        bar();
        if (w == 0) await(p.mbox, tag, p.tmo, sb + 0, tb * 8 + et, 8, lane);
        bar(); STAMP(7);
      }
      {
        STAMP_PTR(4 * l + 1);
        STAMP(0);
        const int ln = fresh_v(lane), r = ln & 31, h = ln >> 5;
        const unsigned a0 = tw * 8192 + ln * 16, b0 = km_read_base(32 * fw + perm23(r), h);
        f32x16 acc = tile64_loop<GeoY>(lds, a0, [&](lds_t wb, int k) { return ld_b128(wb + (k >> 2) * 8192 + (b0 ^ ((k & 3) << 5))); }, w, lane STAMP_PASS(4 * l + 1));
        add_res(acc, r0, r1);
        store_blocked(p.xa + l * XN_L, r0, r1, row0, col0, fresh_v(lane));
        float v[16], m, m2;
        part_stats(r0, r1, v, m, m2);
        store_stats(p.stats + l * ST_L, m, m2, row0, col0, fresh_v(lane));
        STAMP(5); WAIT_V(0); STAMP(6); bar();
        if (w == 0) signal(p.mbox, tag, sb + 1, (tb >> 1) * 16 + (lane & 15), (tb & 1) * 8 + et, lane < 16);
        bar();
        if (w == 0) await(p.mbox, tag, p.tmo, sb + 1, rb * 16 + fc, 16, lane);
        bar(); STAMP(7);
      }
      {
        STAMP_PTR(4 * l + 2);
        STAMP(0);
        c_compute(lds, p.stats + l * ST_L, p.hh + l * HH_L, fresh_s(rb), fresh_s(fc), fresh_s(w), fresh_v(lane) STAMP_PASS(4 * l + 2));
        STAMP(5); WAIT_V(0); STAMP(6); bar();
        if (w == 0) signal(p.mbox, tag, sb + 2, (2 * rb + ((lane >> 3) & 1)) * 8 + (lane & 7), fc, lane < 16);
        bar();
        if (w == 0) await(p.mbox, tag, p.tmo, sb + 2, tb * 8 + et, 16, lane);
        bar(); STAMP(7);
      }
      {
        STAMP_PTR(4 * l + 3);
        STAMP(0);
        const int ln = fresh_v(lane);
        const unsigned a0 = tw * 8192 + ln * 16, f0 = kn_read_base(ln, fw);
        f32x16 acc = tile64_loop<GeoD>(lds, a0, [&](lds_t wb, int k) { return ld_tr(wb + f0 + k * 2048, wb + f0 + k * 2048 + 512); }, w, lane STAMP_PASS(4 * l + 3));
        add_res(acc, r0, r1);
        if (l == 1) {
          store_rowmajor(p.out, r0, r1, row0, col0, fresh_v(lane));
          if (w == 0 && blockIdx.x == 0 && lane == 0) __hip_atomic_store((gu64*)p.epoch, tag, RLX_AGENT);
          if (w == 0) SPAN_END();
        } else {
          float v[16], m, m2;
          part_stats(r0, r1, v, m, m2);
          const int le = fresh_v(lane);
          store_gran(p.gran, tag, m, m2, row0, col0, le);
          STAMP(3);
          u32x2 st[8];
          for (unsigned spins = 0;; ++spins) {
            asm volatile("" ::: "memory");
            if (__all(load_gran(p.gran, tag, row0, le, st))) break;
            if (spins > SPIN_CAP) { if (lane == 0) __hip_atomic_store((gu32*)p.tmo, 2u, RLX_AGENT); break; }
            __builtin_amdgcn_s_sleep(1);
          }
          STAMP(4);
          float mean, rstd;
          combine_stats(st, mean, rstd);
          u32x4 n0, n1;
#pragma unroll
          for (int i = 0; i < 4; ++i) {
            n0[i] = pk2((v[2 * i] - mean) * rstd, (v[2 * i + 1] - mean) * rstd);
            n1[i] = pk2((v[8 + 2 * i] - mean) * rstd, (v[8 + 2 * i + 1] - mean) * rstd);
          }
          store_blocked(p.xn + XN_L, n0, n1, row0, col0, le);
          STAMP(5); WAIT_V(0); STAMP(6); bar();
          if (w == 0) signal(p.mbox, tag, 4, (lane & 31) * 8 + et, tb, lane < 32);
          bar();
          if (w == 0) await(p.mbox, tag, p.tmo, 4, tb * 8 + et, 32, lane);
          bar(); STAMP(7);
        }
      }
    }
  }
}

}

extern "C" void kernel_launch(void* const* d_in, const int* in_sizes, int n_in, void* d_out, int out_size, void* d_ws, size_t ws_size, hipStream_t stream) {
  Ptrs p;
  p.x0 = (const u16*)d_in[0]; p.sre = (const u16*)d_in[1]; p.sim = (const u16*)d_in[2]; p.dre = (const u16*)d_in[3]; p.dim = (const u16*)d_in[4];
  p.wg = (const u16*)d_in[6]; p.wu = (const u16*)d_in[7]; p.wd = (const u16*)d_in[8];
  char* ws = (char*)d_ws;
  const size_t MB = 1 << 20;
  p.out = (u16*)d_out;
  p.xn = (u16*)ws;
  p.pq = (u16*)(ws + 4 * MB);
  p.xa = (u16*)(ws + 12 * MB);
  p.hh = (u16*)(ws + 16 * MB);
  p.stats = (float*)(ws + 31 * MB);
  p.stamps = (unsigned long long*)(ws + 32 * MB);
  p.gran = (unsigned*)(ws + 34 * MB);
  p.mbox = (unsigned long long*)(ws + 35 * MB);
  p.tmo = (unsigned*)(ws + 36 * MB);
  p.epoch = (unsigned long long*)(ws + 36 * MB + 256);
  (void)hipFuncSetAttribute((const void*)k_main, hipFuncAttributeMaxDynamicSharedMemorySize, LDS_BYTES);
  k_main<<<256, 512, LDS_BYTES, stream>>>(p);
}
```

```cpp
#include <hip/hip_runtime.h>
#include <stdint.h>

namespace {
typedef unsigned short u16;
typedef short s16x4 __attribute__((ext_vector_type(4)));
typedef short s16x8 __attribute__((ext_vector_type(8)));
typedef __bf16 bf16x8 __attribute__((ext_vector_type(8)));
typedef __bf16 bf16x2 __attribute__((ext_vector_type(2)));
typedef float f32x2 __attribute__((ext_vector_type(2)));
typedef float f32x4 __attribute__((ext_vector_type(4)));
typedef float f32x16 __attribute__((ext_vector_type(16)));
typedef unsigned u32x4 __attribute__((ext_vector_type(4)));
typedef unsigned u32x2 __attribute__((ext_vector_type(2)));
#define LAS __attribute__((address_space(3)))
typedef __amdgpu_buffer_rsrc_t rsrc_t;
typedef LAS char* lds_t;
typedef __attribute__((address_space(1))) unsigned gu32;
typedef __attribute__((address_space(1))) unsigned long long gu64;
#define RLX_AGENT __ATOMIC_RELAXED, __HIP_MEMORY_SCOPE_AGENT

constexpr int SEQ = 2048, DM = 512, FF = 1792;
constexpr int LDS_BYTES = 163840, XB = 98304;
constexpr int SC1 = 16;
constexpr unsigned SPIN_CAP = 1u << 18;

__device__ __forceinline__ rsrc_t mk_rsrc(const void* p, unsigned bytes) { return __builtin_amdgcn_make_buffer_rsrc((void*)p, 0, bytes, 0x00020000); }
template <int AUX> __device__ __forceinline__ void dma16(rsrc_t rs, lds_t dst, unsigned voff, unsigned soff) {
  __builtin_amdgcn_raw_ptr_buffer_load_lds(rs, (LAS void*)dst, 16, voff, soff, 0, AUX);
}
#define WAIT_V(n) asm volatile("s_waitcnt vmcnt(%0)" ::"n"(n) : "memory")
#define SCHED() __builtin_amdgcn_sched_barrier(0)
#define WAIT_L0() __builtin_amdgcn_s_waitcnt(0xC07F)
__device__ __forceinline__ void bar() { asm volatile("" ::: "memory"); __builtin_amdgcn_s_barrier(); asm volatile("" ::: "memory"); }
__device__ __forceinline__ void cbar() { WAIT_L0(); bar(); }
__device__ __forceinline__ int fresh_v(int x) { asm volatile("" : "+v"(x)); return x; }
__device__ __forceinline__ int fresh_s(int x) { asm volatile("" : "+s"(x)); return x; }

template <int I> struct IC { static constexpr int v = I; };
template <int A, int B, class F> __device__ __forceinline__ void static_for(F&& f) {
  if constexpr (A < B) { f(IC<A>{}); static_for<A + 1, B>(f); }
}

#define PXI 3
#define ROT 8
#define TS 17
#define NA 0
#define NG 2
#define TS2 21
#define ROT2 0
#define ROTD 1
struct GeoP { static constexpr int NST = 32, NX = 8, nX = 2, NW = 6, nW = 4, OW = 0; };
struct GeoY { static constexpr int NST = 8,  NX = 4, nX = 4, NW = 6, nW = 4, OW = 4; };
struct GeoC { static constexpr int NST = 8,  NX = 4, nX = 4, NW = 3, nW = 7, OW = 0; };
struct GeoD { static constexpr int NST = 14, NX = 4, nX = 4, NW = 6, nW = 4, OW = 4; };
constexpr int GAP = 39;
#define L6(a, b, c, d, e, f) {a, b, c, d, e, f}
#define L3(a, b, c) {a, b, c}
#define EY L6(29, 30, 31, GAP, 27, 28)
#define EC L3(4, 6, GAP)
#define ED L6(6, 7, 7, GAP, GAP, GAP)
#define EP L6(9, 10, 11, 12, 13, GAP)
struct EarlyNone { static constexpr int at(int) { return -1; } };
struct EarlyY { static constexpr int at(int j) { constexpr int t[6] = EY; return j < 6 ? t[j] : -1; } };
struct EarlyC { static constexpr int at(int j) { constexpr int t[3] = EC; return j < 3 ? t[j] : -1; } };
struct EarlyD { static constexpr int at(int j) { constexpr int t[6] = ED; return j < 6 ? t[j] : -1; } };
struct EarlyP { static constexpr int at(int j) { constexpr int t[6] = EP; return j < 6 ? t[j] : -1; } };

constexpr int PLAN_MAX = 40;
template <class G, class MINE, class GN, class NEXT> struct Plan {
  int xstep[PLAN_MAX], wstep[PLAN_MAX], waitn[PLAN_MAX];
  constexpr Plan() : xstep{}, wstep{}, waitn{} {
    int nx = 0;
    for (int j = 0; j < G::NST; ++j) { xstep[j] = -2; wstep[j] = MINE::at(j) >= 0 ? -3 : -2; }
    for (int s = -1; s < G::NST; ++s) {
      const int lx = s - 1 + G::NX < G::NX - 1 ? G::NX - 1 : s - 1 + G::NX, lw = s - 1 + G::NW < G::NW - 1 ? G::NW - 1 : s - 1 + G::NW;
      for (int c = 0; c < (s < 0 ? PXI : 2); ++c) if (nx < G::NST && nx <= lx) xstep[nx++] = s;
      int c = 0;
      for (int j = 0; j < G::NST; ++j)
        if (wstep[j] == -2 && j <= lw && (s < 0 ? (j < PXI) : c < 2)) { wstep[j] = s; ++c; }
    }
    for (int s = 0; s < G::NST; ++s) {
      int total = 0, last = 0;
      for (int b = 0; b < PLAN_MAX; ++b)
        for (int j = 0; j < G::NST; ++j) if (MINE::at(j) == b) { total += G::nW; if (j == s) last = total; }
      for (int t = -1; t < s; ++t) {
        for (int j = 0; j < G::NST; ++j) {
          if (xstep[j] == t) { total += G::nX; if (j == s) last = total; }
          if (t < 0 && wstep[j] == t) { total += G::nW; if (j == s) last = total; }
        }
        if (t >= 0) {
          for (int j = 0; j < G::NST; ++j) if (wstep[j] == t) { total += G::nW; if (j == s) last = total; }
          for (int j = 0; j < GN::NST; ++j) if (NEXT::at(j) == t) total += GN::nW;
        }
      }
      waitn[s] = total - last > 63 ? 63 : total - last;
    }
  }
  constexpr bool ok() const {
    for (int j = 0; j < G::NST; ++j) if (xstep[j] >= j || xstep[j] < -1 || wstep[j] >= j || wstep[j] == -2) return false;
    return true;
  }
};
template <class G, class MINE, class GN, class NEXT> struct PlanOf { static constexpr Plan<G, MINE, GN, NEXT> v{}; };
struct NoHook { template <class S> __device__ __forceinline__ void operator()(S) const {} };
template <class G, class MINE, class GN, class NEXT, class FX, class FW, class FN, class FH = NoHook>
__device__ __forceinline__ void l_run(FX issueX, FW issueW, FN issueNext, FH hook = NoHook{}) {
  using PL = PlanOf<G, MINE, GN, NEXT>;
  static_assert(PL::v.ok(), "a stage would be waited for before it is issued");
  static_for<0, G::NST>([&](auto j) {
    if constexpr (PL::v.xstep[j.v] == -1) issueX(j);
    if constexpr (PL::v.wstep[j.v] == -1) issueW(j);
  });
  static_for<0, G::NST>([&](auto s) {
    WAIT_V(PL::v.waitn[s.v]);
    bar();
    hook(s);
    static_for<0, G::NST>([&](auto j) { if constexpr (PL::v.xstep[j.v] == s.v) issueX(j); });
    static_for<0, G::NST>([&](auto j) { if constexpr (PL::v.wstep[j.v] == s.v) issueW(j); });
    static_for<0, GN::NST>([&](auto j) { if constexpr (NEXT::at(j.v) == s.v) issueNext(j); });
  });
  bar();
  static_for<0, GN::NST>([&](auto j) { if constexpr (NEXT::at(j.v) == G::NST) issueNext(j); });
}
template <class G> constexpr int wslot(int j) { return (G::OW + j) % G::NW; }

__device__ __forceinline__ bf16x8 ld_b128(lds_t p) { return *(LAS bf16x8*)p; }
__device__ __forceinline__ bf16x8 ld_tr(lds_t p0, lds_t p1) {
  s16x4 a = __builtin_amdgcn_ds_read_tr16_b64_v4i16((LAS s16x4*)p0);
  s16x4 b = __builtin_amdgcn_ds_read_tr16_b64_v4i16((LAS s16x4*)p1);
  s16x8 f = __builtin_shufflevector(a, b, 0, 1, 2, 3, 4, 5, 6, 7);
  return __builtin_bit_cast(bf16x8, f);
}
__device__ __forceinline__ f32x16 mfma(bf16x8 a, bf16x8 b, f32x16 c) { return __builtin_amdgcn_mfma_f32_32x32x16_bf16(a, b, c, 0, 0, 0); }
__device__ __forceinline__ unsigned pk2(float a, float b) { f32x2 v = {a, b}; return __builtin_bit_cast(unsigned, __builtin_convertvector(v, bf16x2)); }
__device__ __forceinline__ float bflo(unsigned u) { return __uint_as_float(u << 16); }
__device__ __forceinline__ float bfhi(unsigned u) { return __uint_as_float(u & 0xffff0000u); }
__device__ __forceinline__ float xhalf_sum(float v) {
  auto r = __builtin_amdgcn_permlane32_swap(__float_as_uint(v), __float_as_uint(v), false, false);
  return __uint_as_float(r[0]) + __uint_as_float(r[1]);
}
template <int N> __device__ __forceinline__ float ror16(float v) {
  return __int_as_float(__builtin_amdgcn_update_dpp(0, __float_as_int(v), 0x120 + N, 0xf, 0xf, false));
}
__device__ __forceinline__ float xrow_sum(float v) {
  auto r = __builtin_amdgcn_permlane16_swap(__float_as_uint(v), __float_as_uint(v), false, false);
  return __uint_as_float(r[0]) + __uint_as_float(r[1]);
}
template <int CTRL> __device__ __forceinline__ float dpp(float v) { return __int_as_float(__builtin_amdgcn_update_dpp(0, __float_as_int(v), CTRL, 0xf, 0xf, false)); }
__device__ __forceinline__ float oct_sum(float v) {
  v += dpp<0xB1>(v);
  v += dpp<0x4E>(v);
  return v + dpp<0x141>(v);
}
__device__ __forceinline__ unsigned km_read_base(int row, int h) { return row * 128 + ((h ^ ((row >> 1) & 7)) << 4); }
__device__ __forceinline__ int perm23(int i) { return (i & ~12) | ((i & 4) << 1) | ((i & 8) >> 1); }
__device__ __forceinline__ unsigned km_src(int lw, int lane, int pitch) {
  return (8 * lw + (lane >> 3)) * pitch + (((lane & 7) ^ (((lw & 1) << 2) | (lane >> 4))) << 4);
}
__device__ __forceinline__ unsigned kn_src(int lw, int lane, int pitch) {
  return (8 * lw + (lane >> 3)) * pitch + (((lane & 7) ^ (((lane >> 4) & 1) << 2)) << 4);
}
__device__ __forceinline__ unsigned kn_read_base(int lane, int ft) {
  int h = lane >> 5, blk = (lane >> 4) & 1, q = (lane & 15) >> 2, p = lane & 3, pp = ((p & 1) << 1) | (p >> 1);
  int c = 4 * ft + 2 * blk + (pp >> 1);
  return (8 * h + q) * 128 + ((c ^ (((q >> 1) & 1) << 2)) << 4) + 8 * (pp & 1);
}

#define SPAN_BEGIN()
#define SPAN_MAIN()
#define SPAN_END()
#define STAMP(i)
#define STAMP_ARG
#define STAMP_PASS(ph)
#define STAMP_PTR(ph)

struct Ptrs {
  unsigned long long* stamps;
  const u16 *x0, *sre, *sim, *dre, *dim, *wg, *wu, *wd;
  u16 *out, *xn, *pq, *xa, *hh;
  float* stats;
  unsigned *gran, *tmo;
  unsigned long long *mbox, *epoch;
};
constexpr size_t XN_L = (size_t)SEQ * DM, PQ_L = (size_t)SEQ * 1024, HH_L = (size_t)SEQ * FF, ST_L = (size_t)16 * SEQ * 2;

typedef unsigned long long u64;
__device__ __forceinline__ void signal(u64* mbox, u64 tag, int seam, int consumer, int slot, bool active) {
  if (active) __hip_atomic_store((gu64*)mbox + ((size_t)seam * 256 + consumer) * 32 + slot, tag, RLX_AGENT);
}
__device__ __forceinline__ void await(u64* mbox, u64 tag, unsigned* tmo, int seam, int me, int nprod, int lane) {
  gu64* q = (gu64*)mbox + ((size_t)seam * 256 + me) * 32 + (lane < nprod ? lane : 0);
  for (unsigned spins = 0;; ++spins) {
    const u64 x = __hip_atomic_load(q, RLX_AGENT);
    if (__all(x == tag)) break;
    if (spins > SPIN_CAP) { if (lane == 0) __hip_atomic_store((gu32*)tmo, 1u, RLX_AGENT); break; }
    __builtin_amdgcn_s_sleep(1);
  }
}

struct PLoader {
  rsrc_t rre, rim; unsigned vw, vx; lds_t lds; int lw, k0;
  __device__ __forceinline__ PLoader(lds_t lds_, const Ptrs& p, int tb, int et, int lw_, int lane, int rot) : lds(lds_), lw(lw_), k0((rot * et) & 31) {
    rre = mk_rsrc(p.sre + (size_t)tb * 64 * SEQ, 64 * SEQ * 2); rim = mk_rsrc(p.sim + (size_t)tb * 64 * SEQ, 64 * SEQ * 2);
    vw = km_src(lw, lane, SEQ * 2);
    vx = (lane >> 5) * 512 + (((lane & 31) ^ (4 * ((2 * lw + (lane >> 5)) & 3))) << 4);
  }
  template <class S> __device__ __forceinline__ void W(S s) {
    const int ks = (s.v + k0) & 31;
    static_for<0, 4>([&](auto n) { dma16<0>(n.v < 2 ? rre : rim, lds + wslot<GeoP>(s.v) * 16384 + (lw + 4 * n.v) * 1024, vw, ks * 128 + (n.v & 1) * 32 * SEQ * 2); });
  }
  template <class S> __device__ __forceinline__ void X(S s, rsrc_t rx) {
    const int ks = (s.v + k0) & 31;
    static_for<0, 2>([&](auto n) { dma16<SC1>(rx, lds + XB + (s.v % 8) * 8192 + n.v * 4096 + lw * 1024, vx, (2 * ks + n.v) * 64 * 512); });
  }
  __device__ __forceinline__ rsrc_t xr(const u16* xn, int et) { return mk_rsrc((const char*)xn + (8 * et + 2 * lw) * 512, SEQ * DM * 2 - (8 * et + 2 * lw) * 512); }
};
struct YLoader {
  rsrc_t rre, rim; unsigned vw, vx; lds_t lds; int lw;
  __device__ __forceinline__ YLoader(lds_t lds_, const Ptrs& p, int et, int lw_, int lane) : lds(lds_), lw(lw_) {
    rre = mk_rsrc(p.dre + (size_t)et * 64 * DM, 64 * DM * 2); rim = mk_rsrc(p.dim + (size_t)et * 64 * DM, 64 * DM * 2);
    vw = km_src(lw, lane, DM * 2); vx = lane * 16;
  }
  template <class S> __device__ __forceinline__ void W(S s) {
    static_for<0, 4>([&](auto i) {
      constexpr int kb = i.v >> 1, n = i.v & 1;
      dma16<0>(s.v < 4 ? rre : rim, lds + wslot<GeoY>(s.v) * 16384 + kb * 8192 + (lw + 4 * n) * 1024, vw, (2 * (s.v & 3) + kb) * 128 + n * 32 * DM * 2);
    });
  }
  template <class S> __device__ __forceinline__ void X(S s, rsrc_t rx) {
    static_for<0, 4>([&](auto i) {
      constexpr int blk = i.v >> 1, n = i.v & 1;
      dma16<SC1>(rx, lds + XB + (s.v % 4) * 16384 + blk * 8192 + n * 1024 + lw * 2048, vx, blk * 128 * 512 + (16 * s.v + 2 * n) * 512);
    });
  }
  __device__ __forceinline__ rsrc_t xr(const u16* pq, int tb) { return mk_rsrc((const char*)pq + ((size_t)2 * tb * 128 + 4 * lw) * 512, 2 * 128 * 512 - 4 * lw * 512); }
};
constexpr int C_WS = 28672;
struct CLoader {
  const char* wsrc[7]; unsigned vx; lds_t lds; int lw;
  __device__ __forceinline__ CLoader(lds_t lds_, const Ptrs& p, int fc, int lw_, int lane) : lds(lds_), lw(lw_) {
    vx = lane * 16;
#pragma unroll
    for (int n = 0; n < 7; ++n) {
      int idx = 64 * n + lane, kr = idx / 28, c = idx - kr * 28;
      const u16* base = c < 14 ? p.wg : p.wu;
      int cc = c < 14 ? c : c - 14;
      wsrc[n] = (const char*)(base + (size_t)(16 * lw + kr) * FF + 112 * fc + 8 * cc);
    }
  }
  template <class S> __device__ __forceinline__ void W(S s) {
    static_for<0, 7>([&](auto n) {
      __builtin_amdgcn_global_load_lds((const unsigned*)(wsrc[n.v] + (size_t)s.v * 64 * FF * 2), (LAS unsigned*)(lds + wslot<GeoC>(s.v) * C_WS + lw * 7168 + n.v * 1024), 16, 0, 0);
    });
  }
  template <class S> __device__ __forceinline__ void X(S s, rsrc_t rx) {
    static_for<0, 4>([&](auto n) { dma16<SC1>(rx, lds + XB + (s.v % 4) * 16384 + lw * 4096 + n.v * 1024, vx, (8 * s.v + 2 * n.v) * 512); });
  }
  __device__ __forceinline__ rsrc_t xr(const u16* xa, int rb) { return mk_rsrc((const char*)xa + (size_t)(4 * rb + lw) * 64 * 512, 64 * 512); }
};
struct DLoader {
  rsrc_t rw; unsigned vw, vx; lds_t lds; int lw, k0;
  __device__ __forceinline__ DLoader(lds_t lds_, const Ptrs& p, int tb, int et, int lw_, int lane) : lds(lds_), lw(lw_), k0(ROTD ? (tb * 3) % 14 : 0) {
    rw = mk_rsrc(p.wd + et * 64, FF * DM * 2 - et * 128);
    vw = kn_src(lw, lane, DM * 2); vx = lane * 16;
  }
  template <class S> __device__ __forceinline__ void W(S s) {
    const int ks = s.v + k0 < 14 ? s.v + k0 : s.v + k0 - 14;
    static_for<0, 4>([&](auto i) {
      constexpr int kb = i.v >> 1, n = i.v & 1;
      dma16<0>(rw, lds + wslot<GeoD>(s.v) * 16384 + kb * 8192 + (lw + 4 * n) * 1024, vw, ((2 * ks + kb) * 64 + 32 * n) * DM * 2);
    });
  }
  template <class S> __device__ __forceinline__ void X(S s, rsrc_t rx) {
    const int ks = s.v + k0 < 14 ? s.v + k0 : s.v + k0 - 14;
    static_for<0, 4>([&](auto i) {
      constexpr int blk = i.v >> 1, n = i.v & 1;
      dma16<SC1>(rx, lds + XB + (s.v % 4) * 16384 + blk * 8192 + n * 1024 + lw * 2048, vx, blk * 224 * 512 + (16 * ks + 2 * n) * 512);
    });
  }
  __device__ __forceinline__ rsrc_t xr(const u16* hh, int tb) { return mk_rsrc((const char*)hh + ((size_t)2 * tb * 224 + 4 * lw) * 512, 2 * 224 * 512 - 4 * lw * 512); }
};
template <class G, class MINE, class LD> __device__ __forceinline__ void l_seam(LD& L) {
  bar(); bar();
  static_for<0, G::NST>([&](auto j) { if constexpr (MINE::at(j.v) == GAP) L.W(j); });
  bar();
}
template <class G, class MINE, class GN, class NEXT, class LD, class LN>
__device__ __forceinline__ void l_phase(LD& L, rsrc_t rx, LN& N) {
  l_run<G, MINE, GN, NEXT>([&](auto s) { L.X(s, rx); }, [&](auto s) { L.W(s); }, [&](auto s) { N.W(s); });
}

__device__ __forceinline__ void p_compute(lds_t lds, u16* pq, int tb, int et, int w, int lane, const char* t0, const char* t1, const char* t2, const char* t3, bool touch, unsigned& dummy STAMP_ARG) {
  const int r = lane & 31, h = lane >> 5;
  const unsigned a0 = km_read_base(32 * w + r, h);
  const int bl = (lane >> 4) & 1, q4 = (lane & 15) >> 2, pp = ((lane & 1) << 1) | ((lane >> 1) & 1), cc0 = 2 * bl + (pp >> 1);
  const unsigned f0 = cc0 * 512 + (((8 * h + q4) ^ (4 * cc0)) << 4) + 8 * (pp & 1), f1 = f0 ^ 64;
  f32x16 acc0 = {}, acc1 = {};
  unsigned wo = GeoP::OW * 16384, xo = XB;
  struct Fr { bf16x8 tok[4], fa[4], fb[4]; };
  Fr A, B;
  auto rdt = [&](Fr& f, int k) { f.tok[k] = ld_b128(lds + wo + (a0 ^ (k << 5))); };
  auto rda = [&](Fr& f, int k) { lds_t xb = lds + xo + (k >> 1) * 4096 + (k & 1) * 256; f.fa[k] = ld_tr(xb + f0, xb + f1); };
  auto rdb = [&](Fr& f, int k) { lds_t xb = lds + xo + (k >> 1) * 4096 + (k & 1) * 256 + 2048; f.fb[k] = ld_tr(xb + f0, xb + f1); };
  auto adv = [&]() {
    wo = wo + 16384 == 6 * 16384 ? 0 : wo + 16384;
    xo = xo + 8192 == LDS_BYTES ? XB : xo + 8192;
  };
  auto step = [&](Fr& nx, const Fr& cu) {
    cbar();
#pragma unroll
    for (int k = 0; k < 4; ++k) {
      rdt(nx, k); rda(nx, k); SCHED();
      acc0 = mfma(cu.fa[k], cu.tok[k], acc0); SCHED();
      rdb(nx, k); SCHED();
      acc1 = mfma(cu.fb[k], cu.tok[k], acc1); SCHED();
    }
    adv();
  };
  cbar();
  STAMP(1);
#pragma unroll
  for (int k = 0; k < 4; ++k) { rdt(A, k); rda(A, k); rdb(A, k); }
  adv();
#pragma unroll 1
  for (int s = 1; s < 31; s += 2) {
    step(B, A);
    if (touch && s == TS) {
      asm volatile("global_load_dword %0, %1, off" : "+v"(dummy) : "v"(t0) : "memory");
      asm volatile("global_load_dword %0, %1, off" : "+v"(dummy) : "v"(t1) : "memory");
    }
    if (touch && s == TS2) {
      asm volatile("global_load_dword %0, %1, off" : "+v"(dummy) : "v"(t2) : "memory");
      asm volatile("global_load_dword %0, %1, off" : "+v"(dummy) : "v"(t3) : "memory");
    }
    step(A, B);
  }
  step(B, A);
  cbar();
  STAMP(2);
#pragma unroll
  for (int k = 0; k < 4; ++k) { acc0 = mfma(B.fa[k], B.tok[k], acc0); acc1 = mfma(B.fb[k], B.tok[k], acc1); }
  const float sg = w >= 2 ? -1.f : 1.f;
  const rsrc_t ro = mk_rsrc((char*)pq + ((size_t)(2 * tb + (w & 1)) * 128 + (w >> 1) * 64 + 8 * et) * 512, 4096);
#pragma unroll
  for (int ft = 0; ft < 2; ++ft)
#pragma unroll
    for (int q = 0; q < 2; ++q) {
      const f32x16& a = ft ? acc1 : acc0;
      u32x4 v = {pk2(sg * a[8 * q], sg * a[8 * q + 1]), pk2(sg * a[8 * q + 2], sg * a[8 * q + 3]), pk2(sg * a[8 * q + 4], sg * a[8 * q + 5]), pk2(sg * a[8 * q + 6], sg * a[8 * q + 7])};
      __builtin_amdgcn_raw_buffer_store_b128(v, ro, lane * 16 + ft * 2048 + q * 1024, 0, SC1);
    }
}

__device__ __forceinline__ void add_res(const f32x16& acc, u32x4& r0, u32x4& r1) {
#pragma unroll
  for (int i = 0; i < 4; ++i) {
    r0[i] = pk2(acc[2 * i] + bflo(r0[i]), acc[2 * i + 1] + bfhi(r0[i]));
    r1[i] = pk2(acc[8 + 2 * i] + bflo(r1[i]), acc[8 + 2 * i + 1] + bfhi(r1[i]));
  }
}
__device__ __forceinline__ void store_rowmajor(u16* xo, const u32x4& o0, const u32x4& o1, int row0, int col0, int lane) {
  const rsrc_t ro = mk_rsrc(xo + (size_t)row0 * DM + col0, 32 * DM * 2);
  const unsigned vo = (lane & 31) * DM * 2 + (lane >> 5) * 16;
  __builtin_amdgcn_raw_buffer_store_b128(o0, ro, vo, 0, 0);
  __builtin_amdgcn_raw_buffer_store_b128(o1, ro, vo + 32, 0, 0);
}
__device__ __forceinline__ void store_blocked(u16* xo, const u32x4& o0, const u32x4& o1, int row0, int col0, int lane) {
  const rsrc_t ro = mk_rsrc((char*)xo + ((size_t)(row0 >> 5) * 64 + (col0 >> 3)) * 512, 2048);
  __builtin_amdgcn_raw_buffer_store_b128(o0, ro, lane * 16, 0, SC1);
  __builtin_amdgcn_raw_buffer_store_b128(o1, ro, lane * 16 + 1024, 0, SC1);
}
__device__ __forceinline__ void part_stats(const u32x4& o0, const u32x4& o1, float (&v)[16], float& m, float& m2) {
  float s0 = 0.f, s1 = 0.f;
#pragma unroll
  for (int i = 0; i < 4; ++i) { v[2 * i] = bflo(o0[i]); v[2 * i + 1] = bfhi(o0[i]); v[8 + 2 * i] = bflo(o1[i]); v[8 + 2 * i + 1] = bfhi(o1[i]); }
#pragma unroll
  for (int i = 0; i < 8; ++i) { s0 += v[i]; s1 += v[8 + i]; }
  m = xhalf_sum(s0 + s1) * (1.f / 32.f);
  float q0 = 0.f, q1 = 0.f;
#pragma unroll
  for (int i = 0; i < 8; ++i) { float d = v[i] - m, e = v[8 + i] - m; q0 += d * d; q1 += e * e; }
  m2 = xhalf_sum(q0 + q1);
}
__device__ __forceinline__ void store_stats(float* stats, float m, float m2, int row0, int col0, int lane) {
  const rsrc_t rs = mk_rsrc(stats + ((size_t)(col0 >> 5) * SEQ + row0) * 2, 32 * 8);
  if (lane < 32) { u32x2 sv = {__float_as_uint(m), __float_as_uint(m2)}; __builtin_amdgcn_raw_buffer_store_b64(sv, rs, lane * 8, 0, SC1); }
}
__device__ __forceinline__ void load_stats(const float* stats, int row0, int lane, u32x2 (&t)[8]) {
  gu64* q = (gu64*)stats + row0 + (lane & 31) + (size_t)(8 * (lane >> 5)) * SEQ;
#pragma unroll
  for (int i = 0; i < 8; ++i) { unsigned long long x = __hip_atomic_load(q + i * SEQ, RLX_AGENT); t[i] = u32x2{(unsigned)x, (unsigned)(x >> 32)}; }
}
__device__ __forceinline__ void store_gran(unsigned* gran, u64 tag, float m, float m2, int row0, int col0, int lane) {
  const rsrc_t rs = mk_rsrc(gran + ((size_t)(col0 >> 5) * SEQ + row0) * 4, 32 * 16);
  const unsigned a = __float_as_uint(m), b = __float_as_uint(m2);
  if (lane < 32) __builtin_amdgcn_raw_buffer_store_b128(u32x4{a, a ^ (unsigned)tag, b, b ^ (unsigned)(tag >> 32)}, rs, lane * 16, 0, SC1);
}
__device__ __forceinline__ bool load_gran(const unsigned* gran, u64 tag, int row0, int lane, u32x2 (&t)[8]) {
  const rsrc_t rs = mk_rsrc(gran + (size_t)row0 * 4, 16 * SEQ * 16 - row0 * 16);
  bool ok = true;
#pragma unroll
  for (int i = 0; i < 8; ++i) {
    const u32x4 g = __builtin_amdgcn_raw_buffer_load_b128(rs, (lane & 31) * 16 + (8 * (lane >> 5) + i) * SEQ * 16, 0, SC1);
    ok = ok && (g[0] ^ g[1]) == (unsigned)tag && (g[2] ^ g[3]) == (unsigned)(tag >> 32);
    t[i] = u32x2{g[0], g[2]};
  }
  return ok;
}
__device__ __forceinline__ void combine_stats(const u32x2 (&t)[8], float& mean, float& rstd) {
  float ms = 0.f, m2 = 0.f;
#pragma unroll
  for (int i = 0; i < 8; ++i) { ms += __uint_as_float(t[i][0]); m2 += __uint_as_float(t[i][1]); }
  mean = xhalf_sum(ms) * (1.f / 16.f);
#pragma unroll
  for (int i = 0; i < 8; ++i) { float d = __uint_as_float(t[i][0]) - mean; m2 += 32.f * d * d; }
  rstd = rsqrtf(xhalf_sum(m2) * (1.f / 512.f) + 1e-5f);
}

template <class G, class FF_> __device__ __forceinline__ f32x16 tile64_loop(lds_t lds, unsigned a0, FF_ feat, int w, int lane STAMP_ARG) {
  f32x16 acc = {};
  unsigned wo = G::OW * 16384, xo = XB;
  struct Fr { bf16x8 tok[8], f[8]; };
  Fr A, B;
  auto rd = [&](Fr& fr, int k) {
    fr.tok[k] = ld_b128(lds + xo + a0 + k * 1024);
    fr.f[k] = feat(lds + wo, k);
  };
  auto adv = [&]() {
    wo = wo + 16384 == 6 * 16384 ? 0 : wo + 16384;
    xo = xo + 16384 == LDS_BYTES ? XB : xo + 16384;
  };
  auto step = [&](Fr& nx, const Fr& cu) {
    cbar();
#pragma unroll
    for (int k = 0; k < 8; ++k) { rd(nx, k); SCHED(); acc = mfma(cu.f[k], cu.tok[k], acc); SCHED(); }
    adv();
  };
  cbar();
  STAMP(1);
#pragma unroll
  for (int k = 0; k < 8; ++k) rd(A, k);
  adv();
#pragma unroll 1
  for (int s = 1; s < G::NST - 1; s += 2) { step(B, A); step(A, B); }
  step(B, A);
  cbar();
  STAMP(2);
#pragma unroll
  for (int k = 0; k < 8; ++k) acc = mfma(B.f[k], B.tok[k], acc);
  return acc;
}

__device__ __forceinline__ void c_compute(lds_t lds, const float* stats, u16* hh, int rb, int fc, int w, int lane STAMP_ARG) {
  const int r = lane & 31, h = lane >> 5;
  const unsigned a0 = w * 4096 + lane * 16;
  const int blk = (lane >> 4) & 1, q = (lane & 15) >> 2, pp = ((lane & 1) << 1) | ((lane >> 1) & 1);
  const unsigned f0 = (8 * h + q) * 448 + blk * 224 + 8 * pp;
  f32x16 acc[7];
#pragma unroll
  for (int j = 0; j < 7; ++j) acc[j] = f32x16{};
  unsigned wo = GeoC::OW * C_WS, xo = XB;
  struct Fr { bf16x8 tok, f[7]; };
  Fr A, B;
  auto rdt = [&](Fr& fr, int k) { fr.tok = ld_b128(lds + xo + a0 + k * 1024); };
  auto rdf = [&](Fr& fr, int k, int j) { lds_t wb = lds + wo + f0; fr.f[j] = ld_tr(wb + 16 * k * 448 + 32 * j, wb + (16 * k + 4) * 448 + 32 * j); };
  float mean, rstd;
  auto center = [&](bf16x8& t, int e) {
    u32x4 v = __builtin_bit_cast(u32x4, t);
    const unsigned u = v[e];
    v[e] = pk2(bflo(u) - mean, bfhi(u) - mean);
    t = __builtin_bit_cast(bf16x8, v);
  };
  auto grp = [&](Fr& nx, const Fr& cu, int k, bool rdn) {
#pragma unroll
    for (int j = 0; j < 7; ++j) {
      if (rdn) { if (j == 0) rdt(nx, k); rdf(nx, k, j); if (j >= 3) center(nx.tok, j - 3); }
      SCHED();
      acc[j] = mfma(cu.f[j], cu.tok, acc[j]);
      SCHED();
    }
  };
  auto adv = [&]() {
    wo = wo + C_WS == 3 * C_WS ? 0 : wo + C_WS;
    xo = xo + 16384 == LDS_BYTES ? XB : xo + 16384;
  };
  {
    u32x2 st[8];
    load_stats(stats, 128 * rb + 32 * w, lane, st);
    combine_stats(st, mean, rstd);
    asm volatile("" : "+v"(mean), "+v"(rstd));
  }
  cbar();
  STAMP(1);
  rdt(A, 0);
#pragma unroll
  for (int j = 0; j < 7; ++j) rdf(A, 0, j);
#pragma unroll
  for (int e = 0; e < 4; ++e) center(A.tok, e);
#pragma unroll 1
  for (int s = 0; s < 7; ++s) {
    grp(B, A, 1, true);
    grp(A, B, 2, true);
    grp(B, A, 3, true);
    adv();
    cbar();
    grp(A, B, 0, true);
  }
  grp(B, A, 1, true);
  grp(A, B, 2, true);
  grp(B, A, 3, true);
  cbar();
  STAMP(2);
  grp(A, B, 0, false);
  const rsrc_t ro = mk_rsrc((char*)hh + ((size_t)(4 * rb + w) * 224 + 14 * fc) * 512, 7168);
  const float ng = -1.4426950408889634f * rstd;
#pragma unroll
  for (int j = 0; j < 7; ++j) {
    float o[8];
#pragma unroll
    for (int i = 0; i < 8; i += 2) {
      f32x2 g = {acc[j][i], acc[j][i + 1]}, u = {acc[j][8 + i], acc[j][9 + i]};
      g *= ng; u *= rstd;
      f32x2 e = {__builtin_amdgcn_exp2f(g[0]), __builtin_amdgcn_exp2f(g[1])};
      e += 1.f;
      f32x2 sg = {__builtin_amdgcn_rcpf(e[0]), __builtin_amdgcn_rcpf(e[1])};
      u *= sg;
      o[i] = u[0]; o[i + 1] = u[1];
    }
    u32x4 v = {pk2(o[0], o[1]), pk2(o[2], o[3]), pk2(o[4], o[5]), pk2(o[6], o[7])};
    __builtin_amdgcn_raw_buffer_store_b128(v, ro, lane * 16 + 1024 * j, 0, SC1);
    SCHED();
  }
}

__device__ __forceinline__ void ln0_tile(const u16* x, u16* xn, int tb, int et, int w, int lane STAMP_ARG) {
  STAMP(0);
#define LN0 1
  const bf16x2 one = __builtin_bit_cast(bf16x2, 0x3f803f80u);
  const rsrc_t rs = mk_rsrc(x + (size_t)(64 * tb + 16 * w) * DM, 16 * DM * 2);
  const rsrc_t ro = mk_rsrc((char*)xn + ((size_t)(2 * tb + (w >> 1)) * 64 + 8 * et) * 512 + (w & 1) * 256, 4096);
  const int r = lane >> 3, c = lane & 7;
  u32x4 t[2][8];
#pragma unroll
  for (int i = 0; i < 8; ++i)
#pragma unroll
    for (int g = 0; g < 2; ++g) t[g][i] = __builtin_amdgcn_raw_buffer_load_b128(rs, (8 * g + r) * DM * 2 + c * 16, ((i + et) & 7) * 128, 0);
  SCHED();
  bar();
  STAMP(1);
  float mean[2], rstd[2];
#pragma unroll
  for (int g = 0; g < 2; ++g) {
    float s0 = 0.f, s1 = 0.f, q0 = 0.f, q1 = 0.f;
#pragma unroll
    for (int i = 0; i < 8; ++i)
#pragma unroll
      for (int e = 0; e < 4; e += 2) {
        const unsigned ua = t[g][i][e], ub = t[g][i][e + 1];
        const bf16x2 a = __builtin_bit_cast(bf16x2, ua), b = __builtin_bit_cast(bf16x2, ub);
        s0 = __builtin_amdgcn_fdot2_f32_bf16(a, one, s0, false); s1 = __builtin_amdgcn_fdot2_f32_bf16(b, one, s1, false);
        q0 = __builtin_amdgcn_fdot2_f32_bf16(a, a, q0, false);   q1 = __builtin_amdgcn_fdot2_f32_bf16(b, b, q1, false);
      }
    const float s = oct_sum(s0 + s1), q = oct_sum(q0 + q1);
    mean[g] = s * (1.f / 512.f);
    rstd[g] = rsqrtf(fmaxf(q * (1.f / 512.f) - mean[g] * mean[g], 0.f) + 1e-5f);
  }
  STAMP(2);
#pragma unroll
  for (int g = 0; g < 2; ++g) {
    u32x4 o;
#pragma unroll
    for (int e = 0; e < 4; ++e) o[e] = pk2((bflo(t[g][0][e]) - mean[g]) * rstd[g], (bfhi(t[g][0][e]) - mean[g]) * rstd[g]);
    __builtin_amdgcn_raw_buffer_store_b128(o, ro, c * 512 + (8 * g + r) * 16, 0, SC1);
  }
}
struct EarlyP0 { static constexpr int at(int j) { return j < NA + NG ? 0 : -1; } };

__global__ __launch_bounds__(512, 2) __attribute__((amdgpu_waves_per_eu(2, 2))) void k_main(Ptrs p) {
  extern __shared__ __attribute__((aligned(16))) char smem[];
  lds_t lds = (lds_t)smem;
  const int lane = threadIdx.x & 63, wid = __builtin_amdgcn_readfirstlane(threadIdx.x >> 6);
  const int xcd = blockIdx.x & 7, j = blockIdx.x >> 3;
  const int tb = xcd * 4 + (j & 3), et = j >> 2;
  const int rb = (xcd & 1) * 8 + (j & 7), fc = (xcd >> 1) * 4 + (j >> 3);
  SPAN_BEGIN();
  SPAN_MAIN();
  if (wid >= 4) {
    {
      PLoader L(lds, p, fresh_s(tb), fresh_s(et), fresh_s(wid - 4), fresh_v(lane), ROT);
      YLoader N(lds, p, fresh_s(et), fresh_s(wid - 4), fresh_v(lane));
      const rsrc_t rx = L.xr(p.xn, et);
      unsigned dm = 0;
#define TLBT 0
      if (wid == 4) {
        const char* a = lane < 20 ? (const char*)p.xn + (size_t)lane * (2u << 20) + TLBT : (const char*)p.out + (lane & 1) * 1048576;
        asm volatile("global_load_dword %0, %1, off" : "+v"(dm) : "v"(a) : "memory");
      }
      bar();
      static_for<0, NA>([&](auto j) { L.W(j); });
      bar(); bar();
      static_for<NA, NA + NG>([&](auto j) { L.W(j); });
      bar();
      l_phase<GeoP, EarlyP0, GeoY, EarlyY>(L, rx, N);
      asm volatile("" :: "v"(dm));
    }
#pragma unroll 1
    for (int l = 0; l < 2; ++l) {
      {
        YLoader L(lds, p, fresh_s(et), fresh_s(wid - 4), fresh_v(lane));
        CLoader N(lds, p, fresh_s(fc), fresh_s(wid - 4), fresh_v(lane));
        const rsrc_t rx = L.xr(p.pq + l * PQ_L, fresh_s(tb));
        l_seam<GeoY, EarlyY>(L);
        l_phase<GeoY, EarlyY, GeoC, EarlyC>(L, rx, N);
      }
      {
        CLoader L(lds, p, fresh_s(fc), fresh_s(wid - 4), fresh_v(lane));
        DLoader N(lds, p, fresh_s(tb), fresh_s(et), fresh_s(wid - 4), fresh_v(lane));
        const rsrc_t rx = L.xr(p.xa + l * XN_L, fresh_s(rb));
        l_seam<GeoC, EarlyC>(L);
        l_phase<GeoC, EarlyC, GeoD, EarlyD>(L, rx, N);
      }
      if (l == 0) {
        {
          DLoader L(lds, p, fresh_s(tb), fresh_s(et), fresh_s(wid - 4), fresh_v(lane));
          PLoader N(lds, p, fresh_s(tb), fresh_s(et), fresh_s(wid - 4), fresh_v(lane), ROT2);
          const rsrc_t rx = L.xr(p.hh, fresh_s(tb));
          l_seam<GeoD, EarlyD>(L);
          l_phase<GeoD, EarlyD, GeoP, EarlyP>(L, rx, N);
        }
        PLoader L(lds, p, fresh_s(tb), fresh_s(et), fresh_s(wid - 4), fresh_v(lane), ROT2);
        YLoader N(lds, p, fresh_s(et), fresh_s(wid - 4), fresh_v(lane));
        const rsrc_t rx = L.xr(p.xn + XN_L, fresh_s(et));
        l_seam<GeoP, EarlyP>(L);
        l_phase<GeoP, EarlyP, GeoY, EarlyY>(L, rx, N);
      } else {
        DLoader L(lds, p, fresh_s(tb), fresh_s(et), fresh_s(wid - 4), fresh_v(lane));
        const rsrc_t rx = L.xr(p.hh + HH_L, fresh_s(tb));
        l_seam<GeoD, EarlyD>(L);
        l_phase<GeoD, EarlyD, GeoP, EarlyNone>(L, rx, L);
      }
    }
  } else {
    const int w = wid, tw = w & 1, fw = w >> 1;
    const int row0 = 64 * tb + 32 * tw, col0 = 64 * et + 32 * fw;
    unsigned dummy = 0;
    const u64 found = __hip_atomic_load((gu64*)p.epoch, RLX_AGENT);
    u32x4 r0, r1;
    {
      const int r = lane & 31, h = lane >> 5;
      const rsrc_t rr = mk_rsrc(p.x0 + (size_t)row0 * DM + col0, 32 * DM * 2);
      r0 = __builtin_amdgcn_raw_buffer_load_b128(rr, r * DM * 2 + h * 16, 0, 0);
      r1 = __builtin_amdgcn_raw_buffer_load_b128(rr, r * DM * 2 + h * 16 + 32, 0, 0);
    }
    STAMP_PTR(9);
    ln0_tile(p.x0, p.xn, tb, et, w, lane STAMP_PASS(9));
    STAMP(3);
    const u64 e = found + 0x100000001ull;
    const u64 tag = ((u64)(unsigned)__builtin_amdgcn_readfirstlane((unsigned)(e >> 32)) << 32) | (unsigned)__builtin_amdgcn_readfirstlane((unsigned)e);
    WAIT_V(0); STAMP(4); bar(); STAMP(5);
    if (w == 0) signal(p.mbox, tag, 0, (lane & 31) * 8 + et, tb, lane < 32);
    bar();
    if (w == 0) await(p.mbox, tag, p.tmo, 0, tb * 8 + et, 32, lane);
    STAMP(6);
    bar(); STAMP(7);
#pragma unroll 1
    for (int l = 0; l < 2; ++l) {
      const int sb = 4 * l + 1;
      {
        STAMP_PTR(4 * l + 0);
        STAMP(0);
        const int ln = fresh_v(lane), li = blockIdx.x * 64 + ln, lj = blockIdx.x * 32 + (ln & 31);
        const char* t0 = li < 14336 ? (const char*)p.wd + (size_t)li * 128 : (const char*)p.wd;
        const char* t1 = (ln < 32 ? (const char*)p.dre : (const char*)p.dim) + (size_t)(lj & 4095) * 128;
        const char* t2 = (const char*)p.wg + (size_t)(li < 14336 ? li : 0) * 128;
        const char* t3 = (const char*)p.wu + (size_t)(li < 14336 ? li : 0) * 128;
        p_compute(lds, p.pq + l * PQ_L, fresh_s(tb), fresh_s(et), fresh_s(w), fresh_v(lane), t0, t1, t2, t3, l == 0 && w == 0 && blockIdx.x < 224, dummy STAMP_PASS(4 * l + 0));
        STAMP(5); WAIT_V(0); STAMP(6); bar();
        asm volatile("" :: "v"(dummy));
        if (w == 0) signal(p.mbox, tag, sb + 0, tb * 8 + (lane & 7), et, lane < 8);
        bar();
        if (w == 0) await(p.mbox, tag, p.tmo, sb + 0, tb * 8 + et, 8, lane);
        bar(); STAMP(7);
      }
      {
        STAMP_PTR(4 * l + 1);
        STAMP(0);
        const int ln = fresh_v(lane), r = ln & 31, h = ln >> 5;
        const unsigned a0 = tw * 8192 + ln * 16, b0 = km_read_base(32 * fw + perm23(r), h);
        f32x16 acc = tile64_loop<GeoY>(lds, a0, [&](lds_t wb, int k) { return ld_b128(wb + (k >> 2) * 8192 + (b0 ^ ((k & 3) << 5))); }, w, lane STAMP_PASS(4 * l + 1));
        add_res(acc, r0, r1);
        store_blocked(p.xa + l * XN_L, r0, r1, row0, col0, fresh_v(lane));
        float v[16], m, m2;
        part_stats(r0, r1, v, m, m2);
        store_stats(p.stats + l * ST_L, m, m2, row0, col0, fresh_v(lane));
        STAMP(5); WAIT_V(0); STAMP(6); bar();
        if (w == 0) signal(p.mbox, tag, sb + 1, (tb >> 1) * 16 + (lane & 15), (tb & 1) * 8 + et, lane < 16);
        bar();
        if (w == 0) await(p.mbox, tag, p.tmo, sb + 1, rb * 16 + fc, 16, lane);
        bar(); STAMP(7);
      }
      {
        STAMP_PTR(4 * l + 2);
        STAMP(0);
        c_compute(lds, p.stats + l * ST_L, p.hh + l * HH_L, fresh_s(rb), fresh_s(fc), fresh_s(w), fresh_v(lane) STAMP_PASS(4 * l + 2));
        STAMP(5); WAIT_V(0); STAMP(6); bar();
        if (w == 0) signal(p.mbox, tag, sb + 2, (2 * rb + ((lane >> 3) & 1)) * 8 + (lane & 7), fc, lane < 16);
        bar();
        if (w == 0) await(p.mbox, tag, p.tmo, sb + 2, tb * 8 + et, 16, lane);
        bar(); STAMP(7);
      }
      {
        STAMP_PTR(4 * l + 3);
        STAMP(0);
        const int ln = fresh_v(lane);
        const unsigned a0 = tw * 8192 + ln * 16, f0 = kn_read_base(ln, fw);
        f32x16 acc = tile64_loop<GeoD>(lds, a0, [&](lds_t wb, int k) { return ld_tr(wb + f0 + k * 2048, wb + f0 + k * 2048 + 512); }, w, lane STAMP_PASS(4 * l + 3));
        add_res(acc, r0, r1);
        if (l == 1) {
          store_rowmajor(p.out, r0, r1, row0, col0, fresh_v(lane));
          if (w == 0 && blockIdx.x == 0 && lane == 0) __hip_atomic_store((gu64*)p.epoch, tag, RLX_AGENT);
          if (w == 0) SPAN_END();
        } else {
          float v[16], m, m2;
          part_stats(r0, r1, v, m, m2);
          const int le = fresh_v(lane);
          store_gran(p.gran, tag, m, m2, row0, col0, le);
          STAMP(3);
          u32x2 st[8];
          for (unsigned spins = 0;; ++spins) {
            asm volatile("" ::: "memory");
            if (__all(load_gran(p.gran, tag, row0, le, st))) break;
            if (spins > SPIN_CAP) { if (lane == 0) __hip_atomic_store((gu32*)p.tmo, 2u, RLX_AGENT); break; }
            __builtin_amdgcn_s_sleep(1);
          }
          STAMP(4);
          float mean, rstd;
          combine_stats(st, mean, rstd);
          u32x4 n0, n1;
#pragma unroll
          for (int i = 0; i < 4; ++i) {
            n0[i] = pk2((v[2 * i] - mean) * rstd, (v[2 * i + 1] - mean) * rstd);
            n1[i] = pk2((v[8 + 2 * i] - mean) * rstd, (v[8 + 2 * i + 1] - mean) * rstd);
          }
          store_blocked(p.xn + XN_L, n0, n1, row0, col0, le);
          STAMP(5); WAIT_V(0); STAMP(6); bar();
          if (w == 0) signal(p.mbox, tag, 4, (lane & 31) * 8 + et, tb, lane < 32);
          bar();
          if (w == 0) await(p.mbox, tag, p.tmo, 4, tb * 8 + et, 32, lane);
          bar(); STAMP(7);
        }
      }
    }
  }
}

}

extern "C" void kernel_launch(void* const* d_in, const int* in_sizes, int n_in, void* d_out, int out_size, void* d_ws, size_t ws_size, hipStream_t stream) {
  Ptrs p;
  p.x0 = (const u16*)d_in[0]; p.sre = (const u16*)d_in[1]; p.sim = (const u16*)d_in[2]; p.dre = (const u16*)d_in[3]; p.dim = (const u16*)d_in[4];
  p.wg = (const u16*)d_in[6]; p.wu = (const u16*)d_in[7]; p.wd = (const u16*)d_in[8];
  char* ws = (char*)d_ws;
  const size_t MB = 1 << 20;
  p.out = (u16*)d_out;
  p.xn = (u16*)ws;
  p.pq = (u16*)(ws + 4 * MB);
  p.xa = (u16*)(ws + 12 * MB);
  p.hh = (u16*)(ws + 16 * MB);
  p.stats = (float*)(ws + 31 * MB);
  p.stamps = (unsigned long long*)(ws + 32 * MB);
  p.gran = (unsigned*)(ws + 34 * MB);
  p.mbox = (unsigned long long*)(ws + 35 * MB);
  p.tmo = (unsigned*)(ws + 36 * MB);
  p.epoch = (unsigned long long*)(ws + 36 * MB + 256);
  (void)hipFuncSetAttribute((const void*)k_main, hipFuncAttributeMaxDynamicSharedMemorySize, LDS_BYTES);
  k_main<<<256, 512, LDS_BYTES, stream>>>(p);
}
```

```cpp
#include <hip/hip_runtime.h>
#include <stdint.h>

namespace {
typedef unsigned short u16;
typedef short s16x4 __attribute__((ext_vector_type(4)));
typedef short s16x8 __attribute__((ext_vector_type(8)));
typedef __bf16 bf16x8 __attribute__((ext_vector_type(8)));
typedef __bf16 bf16x2 __attribute__((ext_vector_type(2)));
typedef float f32x2 __attribute__((ext_vector_type(2)));
typedef float f32x4 __attribute__((ext_vector_type(4)));
typedef float f32x16 __attribute__((ext_vector_type(16)));
typedef unsigned u32x4 __attribute__((ext_vector_type(4)));
typedef unsigned u32x2 __attribute__((ext_vector_type(2)));
#define LAS __attribute__((address_space(3)))
typedef __amdgpu_buffer_rsrc_t rsrc_t;
typedef LAS char* lds_t;
typedef __attribute__((address_space(1))) unsigned gu32;
typedef __attribute__((address_space(1))) unsigned long long gu64;
#define RLX_AGENT __ATOMIC_RELAXED, __HIP_MEMORY_SCOPE_AGENT

constexpr int SEQ = 2048, DM = 512, FF = 1792;
constexpr int LDS_BYTES = 163840, XB = 98304;
constexpr int SC1 = 16;
constexpr unsigned SPIN_CAP = 1u << 18;

__device__ __forceinline__ rsrc_t mk_rsrc(const void* p, unsigned bytes) { return __builtin_amdgcn_make_buffer_rsrc((void*)p, 0, bytes, 0x00020000); }
template <int AUX> __device__ __forceinline__ void dma16(rsrc_t rs, lds_t dst, unsigned voff, unsigned soff) {
  __builtin_amdgcn_raw_ptr_buffer_load_lds(rs, (LAS void*)dst, 16, voff, soff, 0, AUX);
}
#define WAIT_V(n) asm volatile("s_waitcnt vmcnt(%0)" ::"n"(n) : "memory")
#define SCHED() __builtin_amdgcn_sched_barrier(0)
#define WAIT_L0() __builtin_amdgcn_s_waitcnt(0xC07F)
__device__ __forceinline__ void bar() { asm volatile("" ::: "memory"); __builtin_amdgcn_s_barrier(); asm volatile("" ::: "memory"); }
__device__ __forceinline__ void cbar() { WAIT_L0(); bar(); }
__device__ __forceinline__ int fresh_v(int x) { asm volatile("" : "+v"(x)); return x; }
__device__ __forceinline__ int fresh_s(int x) { asm volatile("" : "+s"(x)); return x; }

template <int I> struct IC { static constexpr int v = I; };
template <int A, int B, class F> __device__ __forceinline__ void static_for(F&& f) {
  if constexpr (A < B) { f(IC<A>{}); static_for<A + 1, B>(f); }
}

#define PXI 3
#define ROT 8
#define TS 17
#define NA 0
#define NG 2
#define TS2 21
#define ROT2 0
#define ROTD 1
struct GeoP { static constexpr int NST = 32, NX = 8, nX = 2, NW = 6, nW = 4, OW = 0; };
struct GeoY { static constexpr int NST = 8,  NX = 4, nX = 4, NW = 6, nW = 4, OW = 4; };
struct GeoC { static constexpr int NST = 8,  NX = 4, nX = 4, NW = 3, nW = 7, OW = 0; };
struct GeoD { static constexpr int NST = 14, NX = 4, nX = 4, NW = 6, nW = 4, OW = 4; };
constexpr int GAP = 39;
#define L6(a, b, c, d, e, f) {a, b, c, d, e, f}
#define L3(a, b, c) {a, b, c}
#define EY L6(29, 30, 31, GAP, 27, 28)
#define EC L3(4, 6, GAP)
#define ED L6(6, 7, 7, GAP, GAP, GAP)
#define EP L6(9, 10, 11, 12, 13, GAP)
struct EarlyNone { static constexpr int at(int) { return -1; } };
struct EarlyY { static constexpr int at(int j) { constexpr int t[6] = EY; return j < 6 ? t[j] : -1; } };
struct EarlyC { static constexpr int at(int j) { constexpr int t[3] = EC; return j < 3 ? t[j] : -1; } };
struct EarlyD { static constexpr int at(int j) { constexpr int t[6] = ED; return j < 6 ? t[j] : -1; } };
struct EarlyP { static constexpr int at(int j) { constexpr int t[6] = EP; return j < 6 ? t[j] : -1; } };

constexpr int PLAN_MAX = 40;
template <class G, class MINE, class GN, class NEXT> struct Plan {
  int xstep[PLAN_MAX], wstep[PLAN_MAX], waitn[PLAN_MAX];
  constexpr Plan() : xstep{}, wstep{}, waitn{} {
    int nx = 0;
    for (int j = 0; j < G::NST; ++j) { xstep[j] = -2; wstep[j] = MINE::at(j) >= 0 ? -3 : -2; }
    for (int s = -1; s < G::NST; ++s) {
      const int lx = s - 1 + G::NX < G::NX - 1 ? G::NX - 1 : s - 1 + G::NX, lw = s - 1 + G::NW < G::NW - 1 ? G::NW - 1 : s - 1 + G::NW;
      for (int c = 0; c < (s < 0 ? PXI : 2); ++c) if (nx < G::NST && nx <= lx) xstep[nx++] = s;
      int c = 0;
      for (int j = 0; j < G::NST; ++j)
        if (wstep[j] == -2 && j <= lw && (s < 0 ? (j < PXI) : c < 2)) { wstep[j] = s; ++c; }
    }
    for (int s = 0; s < G::NST; ++s) {
      int total = 0, last = 0;
      for (int b = 0; b < PLAN_MAX; ++b)
        for (int j = 0; j < G::NST; ++j) if (MINE::at(j) == b) { total += G::nW; if (j == s) last = total; }
      for (int t = -1; t < s; ++t) {
        for (int j = 0; j < G::NST; ++j) {
          if (xstep[j] == t) { total += G::nX; if (j == s) last = total; }
          if (t < 0 && wstep[j] == t) { total += G::nW; if (j == s) last = total; }
        }
        if (t >= 0) {
          for (int j = 0; j < G::NST; ++j) if (wstep[j] == t) { total += G::nW; if (j == s) last = total; }
          for (int j = 0; j < GN::NST; ++j) if (NEXT::at(j) == t) total += GN::nW;
        }
      }
      waitn[s] = total - last > 63 ? 63 : total - last;
    }
  }
  constexpr bool ok() const {
    for (int j = 0; j < G::NST; ++j) if (xstep[j] >= j || xstep[j] < -1 || wstep[j] >= j || wstep[j] == -2) return false;
    return true;
  }
};
template <class G, class MINE, class GN, class NEXT> struct PlanOf { static constexpr Plan<G, MINE, GN, NEXT> v{}; };
struct NoHook { template <class S> __device__ __forceinline__ void operator()(S) const {} };
template <class G, class MINE, class GN, class NEXT, class FX, class FW, class FN, class FH = NoHook>
__device__ __forceinline__ void l_run(FX issueX, FW issueW, FN issueNext, FH hook = NoHook{}) {
  using PL = PlanOf<G, MINE, GN, NEXT>;
  static_assert(PL::v.ok(), "a stage would be waited for before it is issued");
  static_for<0, G::NST>([&](auto j) {
    if constexpr (PL::v.xstep[j.v] == -1) issueX(j);
    if constexpr (PL::v.wstep[j.v] == -1) issueW(j);
  });
  static_for<0, G::NST>([&](auto s) {
    WAIT_V(PL::v.waitn[s.v]);
    bar();
    hook(s);
    static_for<0, G::NST>([&](auto j) { if constexpr (PL::v.xstep[j.v] == s.v) issueX(j); });
    static_for<0, G::NST>([&](auto j) { if constexpr (PL::v.wstep[j.v] == s.v) issueW(j); });
    static_for<0, GN::NST>([&](auto j) { if constexpr (NEXT::at(j.v) == s.v) issueNext(j); });
  });
  bar();
  static_for<0, GN::NST>([&](auto j) { if constexpr (NEXT::at(j.v) == G::NST) issueNext(j); });
}
template <class G> constexpr int wslot(int j) { return (G::OW + j) % G::NW; }

__device__ __forceinline__ bf16x8 ld_b128(lds_t p) { return *(LAS bf16x8*)p; }
__device__ __forceinline__ bf16x8 ld_tr(lds_t p0, lds_t p1) {
  s16x4 a = __builtin_amdgcn_ds_read_tr16_b64_v4i16((LAS s16x4*)p0);
  s16x4 b = __builtin_amdgcn_ds_read_tr16_b64_v4i16((LAS s16x4*)p1);
  s16x8 f = __builtin_shufflevector(a, b, 0, 1, 2, 3, 4, 5, 6, 7);
  return __builtin_bit_cast(bf16x8, f);
}
__device__ __forceinline__ f32x16 mfma(bf16x8 a, bf16x8 b, f32x16 c) { return __builtin_amdgcn_mfma_f32_32x32x16_bf16(a, b, c, 0, 0, 0); }
__device__ __forceinline__ unsigned pk2(float a, float b) { f32x2 v = {a, b}; return __builtin_bit_cast(unsigned, __builtin_convertvector(v, bf16x2)); }
__device__ __forceinline__ float bflo(unsigned u) { return __uint_as_float(u << 16); }
__device__ __forceinline__ float bfhi(unsigned u) { return __uint_as_float(u & 0xffff0000u); }
__device__ __forceinline__ float xhalf_sum(float v) {
  auto r = __builtin_amdgcn_permlane32_swap(__float_as_uint(v), __float_as_uint(v), false, false);
  return __uint_as_float(r[0]) + __uint_as_float(r[1]);
}
template <int N> __device__ __forceinline__ float ror16(float v) {
  return __int_as_float(__builtin_amdgcn_update_dpp(0, __float_as_int(v), 0x120 + N, 0xf, 0xf, false));
}
__device__ __forceinline__ float xrow_sum(float v) {
  auto r = __builtin_amdgcn_permlane16_swap(__float_as_uint(v), __float_as_uint(v), false, false);
  return __uint_as_float(r[0]) + __uint_as_float(r[1]);
}
template <int CTRL> __device__ __forceinline__ float dpp(float v) { return __int_as_float(__builtin_amdgcn_update_dpp(0, __float_as_int(v), CTRL, 0xf, 0xf, false)); }
__device__ __forceinline__ float oct_sum(float v) {
  v += dpp<0xB1>(v);
  v += dpp<0x4E>(v);
  return v + dpp<0x141>(v);
}
__device__ __forceinline__ unsigned km_read_base(int row, int h) { return row * 128 + ((h ^ ((row >> 1) & 7)) << 4); }
__device__ __forceinline__ int perm23(int i) { return (i & ~12) | ((i & 4) << 1) | ((i & 8) >> 1); }
__device__ __forceinline__ unsigned km_src(int lw, int lane, int pitch) {
  return (8 * lw + (lane >> 3)) * pitch + (((lane & 7) ^ (((lw & 1) << 2) | (lane >> 4))) << 4);
}
__device__ __forceinline__ unsigned kn_src(int lw, int lane, int pitch) {
  return (8 * lw + (lane >> 3)) * pitch + (((lane & 7) ^ (((lane >> 4) & 1) << 2)) << 4);
}
__device__ __forceinline__ unsigned kn_read_base(int lane, int ft) {
  int h = lane >> 5, blk = (lane >> 4) & 1, q = (lane & 15) >> 2, p = lane & 3, pp = ((p & 1) << 1) | (p >> 1);
  int c = 4 * ft + 2 * blk + (pp >> 1);
  return (8 * h + q) * 128 + ((c ^ (((q >> 1) & 1) << 2)) << 4) + 8 * (pp & 1);
}

#define SPAN_BEGIN()
#define SPAN_MAIN()
#define SPAN_END()
#define STAMP(i)
#define STAMP_ARG
#define STAMP_PASS(ph)
#define STAMP_PTR(ph)

#define PA 0
#define PB 0
#define PM 0
#define PEV(i) do { if (PA >= 0 && blockIdx.x == 0 && wid == 0 && lane == 0) __hip_atomic_store((gu64*)p.stamps + (i), __builtin_amdgcn_s_memrealtime(), RLX_AGENT); } while (0)
struct Ptrs {
  unsigned long long* stamps;
  const u16 *x0, *sre, *sim, *dre, *dim, *wg, *wu, *wd;
  u16 *out, *xn, *pq, *xa, *hh;
  float* stats;
  unsigned *gran, *tmo;
  unsigned long long *mbox, *epoch;
};
constexpr size_t XN_L = (size_t)SEQ * DM, PQ_L = (size_t)SEQ * 1024, HH_L = (size_t)SEQ * FF, ST_L = (size_t)16 * SEQ * 2;

typedef unsigned long long u64;
__device__ __forceinline__ void signal(u64* mbox, u64 tag, int seam, int consumer, int slot, bool active) {
  if (active) __hip_atomic_store((gu64*)mbox + ((size_t)seam * 256 + consumer) * 32 + slot, tag, RLX_AGENT);
}
__device__ __forceinline__ void await(u64* mbox, u64 tag, unsigned* tmo, int seam, int me, int nprod, int lane) {
  gu64* q = (gu64*)mbox + ((size_t)seam * 256 + me) * 32 + (lane < nprod ? lane : 0);
  for (unsigned spins = 0;; ++spins) {
    const u64 x = __hip_atomic_load(q, RLX_AGENT);
    if (__all(x == tag)) break;
    if (spins > SPIN_CAP) { if (lane == 0) __hip_atomic_store((gu32*)tmo, 1u, RLX_AGENT); break; }
    __builtin_amdgcn_s_sleep(1);
  }
}

struct PLoader {
  rsrc_t rre, rim; unsigned vw, vx; lds_t lds; int lw, k0;
  __device__ __forceinline__ PLoader(lds_t lds_, const Ptrs& p, int tb, int et, int lw_, int lane, int rot) : lds(lds_), lw(lw_), k0((rot * et) & 31) {
    rre = mk_rsrc(p.sre + (size_t)tb * 64 * SEQ, 64 * SEQ * 2); rim = mk_rsrc(p.sim + (size_t)tb * 64 * SEQ, 64 * SEQ * 2);
    vw = km_src(lw, lane, SEQ * 2);
    vx = (lane >> 5) * 512 + (((lane & 31) ^ (4 * ((2 * lw + (lane >> 5)) & 3))) << 4);
  }
  template <class S> __device__ __forceinline__ void W(S s) {
    const int ks = (s.v + k0) & 31;
    static_for<0, 4>([&](auto n) { dma16<0>(n.v < 2 ? rre : rim, lds + wslot<GeoP>(s.v) * 16384 + (lw + 4 * n.v) * 1024, vw, ks * 128 + (n.v & 1) * 32 * SEQ * 2); });
  }
  template <class S> __device__ __forceinline__ void X(S s, rsrc_t rx) {
    const int ks = (s.v + k0) & 31;
    static_for<0, 2>([&](auto n) { dma16<SC1>(rx, lds + XB + (s.v % 8) * 8192 + n.v * 4096 + lw * 1024, vx, (2 * ks + n.v) * 64 * 512); });
  }
  __device__ __forceinline__ rsrc_t xr(const u16* xn, int et) { return mk_rsrc((const char*)xn + (8 * et + 2 * lw) * 512, SEQ * DM * 2 - (8 * et + 2 * lw) * 512); }
};
struct YLoader {
  rsrc_t rre, rim; unsigned vw, vx; lds_t lds; int lw;
  __device__ __forceinline__ YLoader(lds_t lds_, const Ptrs& p, int et, int lw_, int lane) : lds(lds_), lw(lw_) {
    rre = mk_rsrc(p.dre + (size_t)et * 64 * DM, 64 * DM * 2); rim = mk_rsrc(p.dim + (size_t)et * 64 * DM, 64 * DM * 2);
    vw = km_src(lw, lane, DM * 2); vx = lane * 16;
  }
  template <class S> __device__ __forceinline__ void W(S s) {
    static_for<0, 4>([&](auto i) {
      constexpr int kb = i.v >> 1, n = i.v & 1;
      dma16<0>(s.v < 4 ? rre : rim, lds + wslot<GeoY>(s.v) * 16384 + kb * 8192 + (lw + 4 * n) * 1024, vw, (2 * (s.v & 3) + kb) * 128 + n * 32 * DM * 2);
    });
  }
  template <class S> __device__ __forceinline__ void X(S s, rsrc_t rx) {
    static_for<0, 4>([&](auto i) {
      constexpr int blk = i.v >> 1, n = i.v & 1;
      dma16<SC1>(rx, lds + XB + (s.v % 4) * 16384 + blk * 8192 + n * 1024 + lw * 2048, vx, blk * 128 * 512 + (16 * s.v + 2 * n) * 512);
    });
  }
  __device__ __forceinline__ rsrc_t xr(const u16* pq, int tb) { return mk_rsrc((const char*)pq + ((size_t)2 * tb * 128 + 4 * lw) * 512, 2 * 128 * 512 - 4 * lw * 512); }
};
constexpr int C_WS = 28672;
struct CLoader {
  const char* wsrc[7]; unsigned vx; lds_t lds; int lw;
  __device__ __forceinline__ CLoader(lds_t lds_, const Ptrs& p, int fc, int lw_, int lane) : lds(lds_), lw(lw_) {
    vx = lane * 16;
#pragma unroll
    for (int n = 0; n < 7; ++n) {
      int idx = 64 * n + lane, kr = idx / 28, c = idx - kr * 28;
      const u16* base = c < 14 ? p.wg : p.wu;
      int cc = c < 14 ? c : c - 14;
      wsrc[n] = (const char*)(base + (size_t)(16 * lw + kr) * FF + 112 * fc + 8 * cc);
    }
  }
  template <class S> __device__ __forceinline__ void W(S s) {
    static_for<0, 7>([&](auto n) {
      __builtin_amdgcn_global_load_lds((const unsigned*)(wsrc[n.v] + (size_t)s.v * 64 * FF * 2), (LAS unsigned*)(lds + wslot<GeoC>(s.v) * C_WS + lw * 7168 + n.v * 1024), 16, 0, 0);
    });
  }
  template <class S> __device__ __forceinline__ void X(S s, rsrc_t rx) {
    static_for<0, 4>([&](auto n) { dma16<SC1>(rx, lds + XB + (s.v % 4) * 16384 + lw * 4096 + n.v * 1024, vx, (8 * s.v + 2 * n.v) * 512); });
  }
  __device__ __forceinline__ rsrc_t xr(const u16* xa, int rb) { return mk_rsrc((const char*)xa + (size_t)(4 * rb + lw) * 64 * 512, 64 * 512); }
};
struct DLoader {
  rsrc_t rw; unsigned vw, vx; lds_t lds; int lw, k0;
  __device__ __forceinline__ DLoader(lds_t lds_, const Ptrs& p, int tb, int et, int lw_, int lane) : lds(lds_), lw(lw_), k0(ROTD ? (tb * 3) % 14 : 0) {
    rw = mk_rsrc(p.wd + et * 64, FF * DM * 2 - et * 128);
    vw = kn_src(lw, lane, DM * 2); vx = lane * 16;
  }
  template <class S> __device__ __forceinline__ void W(S s) {
    const int ks = s.v + k0 < 14 ? s.v + k0 : s.v + k0 - 14;
    static_for<0, 4>([&](auto i) {
      constexpr int kb = i.v >> 1, n = i.v & 1;
      dma16<0>(rw, lds + wslot<GeoD>(s.v) * 16384 + kb * 8192 + (lw + 4 * n) * 1024, vw, ((2 * ks + kb) * 64 + 32 * n) * DM * 2);
    });
  }
  template <class S> __device__ __forceinline__ void X(S s, rsrc_t rx) {
    const int ks = s.v + k0 < 14 ? s.v + k0 : s.v + k0 - 14;
    static_for<0, 4>([&](auto i) {
      constexpr int blk = i.v >> 1, n = i.v & 1;
      dma16<SC1>(rx, lds + XB + (s.v % 4) * 16384 + blk * 8192 + n * 1024 + lw * 2048, vx, blk * 224 * 512 + (16 * ks + 2 * n) * 512);
    });
  }
  __device__ __forceinline__ rsrc_t xr(const u16* hh, int tb) { return mk_rsrc((const char*)hh + ((size_t)2 * tb * 224 + 4 * lw) * 512, 2 * 224 * 512 - 4 * lw * 512); }
};
template <class G, class MINE, class LD> __device__ __forceinline__ void l_seam(LD& L) {
  bar(); bar();
  static_for<0, G::NST>([&](auto j) { if constexpr (MINE::at(j.v) == GAP) L.W(j); });
  bar();
}
template <class G, class MINE, class GN, class NEXT, class LD, class LN>
__device__ __forceinline__ void l_phase(LD& L, rsrc_t rx, LN& N) {
  l_run<G, MINE, GN, NEXT>([&](auto s) { L.X(s, rx); }, [&](auto s) { L.W(s); }, [&](auto s) { N.W(s); });
}

__device__ __forceinline__ void p_compute(lds_t lds, u16* pq, int tb, int et, int w, int lane, const char* t0, const char* t1, const char* t2, const char* t3, bool touch, unsigned& dummy STAMP_ARG) {
  const int r = lane & 31, h = lane >> 5;
  const unsigned a0 = km_read_base(32 * w + r, h);
  const int bl = (lane >> 4) & 1, q4 = (lane & 15) >> 2, pp = ((lane & 1) << 1) | ((lane >> 1) & 1), cc0 = 2 * bl + (pp >> 1);
  const unsigned f0 = cc0 * 512 + (((8 * h + q4) ^ (4 * cc0)) << 4) + 8 * (pp & 1), f1 = f0 ^ 64;
  f32x16 acc0 = {}, acc1 = {};
  unsigned wo = GeoP::OW * 16384, xo = XB;
  struct Fr { bf16x8 tok[4], fa[4], fb[4]; };
  Fr A, B;
  auto rdt = [&](Fr& f, int k) { f.tok[k] = ld_b128(lds + wo + (a0 ^ (k << 5))); };
  auto rda = [&](Fr& f, int k) { lds_t xb = lds + xo + (k >> 1) * 4096 + (k & 1) * 256; f.fa[k] = ld_tr(xb + f0, xb + f1); };
  auto rdb = [&](Fr& f, int k) { lds_t xb = lds + xo + (k >> 1) * 4096 + (k & 1) * 256 + 2048; f.fb[k] = ld_tr(xb + f0, xb + f1); };
  auto adv = [&]() {
    wo = wo + 16384 == 6 * 16384 ? 0 : wo + 16384;
    xo = xo + 8192 == LDS_BYTES ? XB : xo + 8192;
  };
  auto step = [&](Fr& nx, const Fr& cu) {
    cbar();
#pragma unroll
    for (int k = 0; k < 4; ++k) {
      rdt(nx, k); rda(nx, k); SCHED();
      acc0 = mfma(cu.fa[k], cu.tok[k], acc0); SCHED();
      rdb(nx, k); SCHED();
      acc1 = mfma(cu.fb[k], cu.tok[k], acc1); SCHED();
    }
    adv();
  };
  cbar();
  STAMP(1);
#pragma unroll
  for (int k = 0; k < 4; ++k) { rdt(A, k); rda(A, k); rdb(A, k); }
  adv();
#pragma unroll 1
  for (int s = 1; s < 31; s += 2) {
    step(B, A);
    if (touch && s == TS) {
      asm volatile("global_load_dword %0, %1, off" : "+v"(dummy) : "v"(t0) : "memory");
      asm volatile("global_load_dword %0, %1, off" : "+v"(dummy) : "v"(t1) : "memory");
    }
    if (touch && s == TS2) {
      asm volatile("global_load_dword %0, %1, off" : "+v"(dummy) : "v"(t2) : "memory");
      asm volatile("global_load_dword %0, %1, off" : "+v"(dummy) : "v"(t3) : "memory");
    }
    step(A, B);
  }
  step(B, A);
  cbar();
  STAMP(2);
#pragma unroll
  for (int k = 0; k < 4; ++k) { acc0 = mfma(B.fa[k], B.tok[k], acc0); acc1 = mfma(B.fb[k], B.tok[k], acc1); }
  const float sg = w >= 2 ? -1.f : 1.f;
  const rsrc_t ro = mk_rsrc((char*)pq + ((size_t)(2 * tb + (w & 1)) * 128 + (w >> 1) * 64 + 8 * et) * 512, 4096);
#pragma unroll
  for (int ft = 0; ft < 2; ++ft)
#pragma unroll
    for (int q = 0; q < 2; ++q) {
      const f32x16& a = ft ? acc1 : acc0;
      u32x4 v = {pk2(sg * a[8 * q], sg * a[8 * q + 1]), pk2(sg * a[8 * q + 2], sg * a[8 * q + 3]), pk2(sg * a[8 * q + 4], sg * a[8 * q + 5]), pk2(sg * a[8 * q + 6], sg * a[8 * q + 7])};
      __builtin_amdgcn_raw_buffer_store_b128(v, ro, lane * 16 + ft * 2048 + q * 1024, 0, SC1);
    }
}

__device__ __forceinline__ void add_res(const f32x16& acc, u32x4& r0, u32x4& r1) {
#pragma unroll
  for (int i = 0; i < 4; ++i) {
    r0[i] = pk2(acc[2 * i] + bflo(r0[i]), acc[2 * i + 1] + bfhi(r0[i]));
    r1[i] = pk2(acc[8 + 2 * i] + bflo(r1[i]), acc[8 + 2 * i + 1] + bfhi(r1[i]));
  }
}
__device__ __forceinline__ void store_rowmajor(u16* xo, const u32x4& o0, const u32x4& o1, int row0, int col0, int lane) {
  const rsrc_t ro = mk_rsrc(xo + (size_t)row0 * DM + col0, 32 * DM * 2);
  const unsigned vo = (lane & 31) * DM * 2 + (lane >> 5) * 16;
  __builtin_amdgcn_raw_buffer_store_b128(o0, ro, vo, 0, 0);
  __builtin_amdgcn_raw_buffer_store_b128(o1, ro, vo + 32, 0, 0);
}
__device__ __forceinline__ void store_blocked(u16* xo, const u32x4& o0, const u32x4& o1, int row0, int col0, int lane) {
  const rsrc_t ro = mk_rsrc((char*)xo + ((size_t)(row0 >> 5) * 64 + (col0 >> 3)) * 512, 2048);
  __builtin_amdgcn_raw_buffer_store_b128(o0, ro, lane * 16, 0, SC1);
  __builtin_amdgcn_raw_buffer_store_b128(o1, ro, lane * 16 + 1024, 0, SC1);
}
__device__ __forceinline__ void part_stats(const u32x4& o0, const u32x4& o1, float (&v)[16], float& m, float& m2) {
  float s0 = 0.f, s1 = 0.f;
#pragma unroll
  for (int i = 0; i < 4; ++i) { v[2 * i] = bflo(o0[i]); v[2 * i + 1] = bfhi(o0[i]); v[8 + 2 * i] = bflo(o1[i]); v[8 + 2 * i + 1] = bfhi(o1[i]); }
#pragma unroll
  for (int i = 0; i < 8; ++i) { s0 += v[i]; s1 += v[8 + i]; }
  m = xhalf_sum(s0 + s1) * (1.f / 32.f);
  float q0 = 0.f, q1 = 0.f;
#pragma unroll
  for (int i = 0; i < 8; ++i) { float d = v[i] - m, e = v[8 + i] - m; q0 += d * d; q1 += e * e; }
  m2 = xhalf_sum(q0 + q1);
}
__device__ __forceinline__ void store_stats(float* stats, float m, float m2, int row0, int col0, int lane) {
  const rsrc_t rs = mk_rsrc(stats + ((size_t)(col0 >> 5) * SEQ + row0) * 2, 32 * 8);
  if (lane < 32) { u32x2 sv = {__float_as_uint(m), __float_as_uint(m2)}; __builtin_amdgcn_raw_buffer_store_b64(sv, rs, lane * 8, 0, SC1); }
}
__device__ __forceinline__ void load_stats(const float* stats, int row0, int lane, u32x2 (&t)[8]) {
  gu64* q = (gu64*)stats + row0 + (lane & 31) + (size_t)(8 * (lane >> 5)) * SEQ;
#pragma unroll
  for (int i = 0; i < 8; ++i) { unsigned long long x = __hip_atomic_load(q + i * SEQ, RLX_AGENT); t[i] = u32x2{(unsigned)x, (unsigned)(x >> 32)}; }
}
__device__ __forceinline__ void store_gran(unsigned* gran, u64 tag, float m, float m2, int row0, int col0, int lane) {
  const rsrc_t rs = mk_rsrc(gran + ((size_t)(col0 >> 5) * SEQ + row0) * 4, 32 * 16);
  const unsigned a = __float_as_uint(m), b = __float_as_uint(m2);
  if (lane < 32) __builtin_amdgcn_raw_buffer_store_b128(u32x4{a, a ^ (unsigned)tag, b, b ^ (unsigned)(tag >> 32)}, rs, lane * 16, 0, SC1);
}
__device__ __forceinline__ bool load_gran(const unsigned* gran, u64 tag, int row0, int lane, u32x2 (&t)[8]) {
  const rsrc_t rs = mk_rsrc(gran + (size_t)row0 * 4, 16 * SEQ * 16 - row0 * 16);
  bool ok = true;
#pragma unroll
  for (int i = 0; i < 8; ++i) {
    const u32x4 g = __builtin_amdgcn_raw_buffer_load_b128(rs, (lane & 31) * 16 + (8 * (lane >> 5) + i) * SEQ * 16, 0, SC1);
    ok = ok && (g[0] ^ g[1]) == (unsigned)tag && (g[2] ^ g[3]) == (unsigned)(tag >> 32);
    t[i] = u32x2{g[0], g[2]};
  }
  return ok;
}
__device__ __forceinline__ void combine_stats(const u32x2 (&t)[8], float& mean, float& rstd) {
  float ms = 0.f, m2 = 0.f;
#pragma unroll
  for (int i = 0; i < 8; ++i) { ms += __uint_as_float(t[i][0]); m2 += __uint_as_float(t[i][1]); }
  mean = xhalf_sum(ms) * (1.f / 16.f);
#pragma unroll
  for (int i = 0; i < 8; ++i) { float d = __uint_as_float(t[i][0]) - mean; m2 += 32.f * d * d; }
  rstd = rsqrtf(xhalf_sum(m2) * (1.f / 512.f) + 1e-5f);
}

template <class G, class FF_> __device__ __forceinline__ f32x16 tile64_loop(lds_t lds, unsigned a0, FF_ feat, int w, int lane STAMP_ARG) {
  f32x16 acc = {};
  unsigned wo = G::OW * 16384, xo = XB;
  struct Fr { bf16x8 tok[8], f[8]; };
  Fr A, B;
  auto rd = [&](Fr& fr, int k) {
    fr.tok[k] = ld_b128(lds + xo + a0 + k * 1024);
    fr.f[k] = feat(lds + wo, k);
  };
  auto adv = [&]() {
    wo = wo + 16384 == 6 * 16384 ? 0 : wo + 16384;
    xo = xo + 16384 == LDS_BYTES ? XB : xo + 16384;
  };
  auto step = [&](Fr& nx, const Fr& cu) {
    cbar();
#pragma unroll
    for (int k = 0; k < 8; ++k) { rd(nx, k); SCHED(); acc = mfma(cu.f[k], cu.tok[k], acc); SCHED(); }
    adv();
  };
  cbar();
  STAMP(1);
#pragma unroll
  for (int k = 0; k < 8; ++k) rd(A, k);
  adv();
#pragma unroll 1
  for (int s = 1; s < G::NST - 1; s += 2) { step(B, A); step(A, B); }
  step(B, A);
  cbar();
  STAMP(2);
#pragma unroll
  for (int k = 0; k < 8; ++k) acc = mfma(B.f[k], B.tok[k], acc);
  return acc;
}

__device__ __forceinline__ void c_compute(lds_t lds, const float* stats, u16* hh, int rb, int fc, int w, int lane STAMP_ARG) {
  const int r = lane & 31, h = lane >> 5;
  const unsigned a0 = w * 4096 + lane * 16;
  const int blk = (lane >> 4) & 1, q = (lane & 15) >> 2, pp = ((lane & 1) << 1) | ((lane >> 1) & 1);
  const unsigned f0 = (8 * h + q) * 448 + blk * 224 + 8 * pp;
  f32x16 acc[7];
#pragma unroll
  for (int j = 0; j < 7; ++j) acc[j] = f32x16{};
  unsigned wo = GeoC::OW * C_WS, xo = XB;
  struct Fr { bf16x8 tok, f[7]; };
  Fr A, B;
  auto rdt = [&](Fr& fr, int k) { fr.tok = ld_b128(lds + xo + a0 + k * 1024); };
  auto rdf = [&](Fr& fr, int k, int j) { lds_t wb = lds + wo + f0; fr.f[j] = ld_tr(wb + 16 * k * 448 + 32 * j, wb + (16 * k + 4) * 448 + 32 * j); };
  float mean, rstd;
  auto center = [&](bf16x8& t, int e) {
    u32x4 v = __builtin_bit_cast(u32x4, t);
    const unsigned u = v[e];
    v[e] = pk2(bflo(u) - mean, bfhi(u) - mean);
    t = __builtin_bit_cast(bf16x8, v);
  };
  auto grp = [&](Fr& nx, const Fr& cu, int k, bool rdn) {
#pragma unroll
    for (int j = 0; j < 7; ++j) {
      if (rdn) { if (j == 0) rdt(nx, k); rdf(nx, k, j); if (j >= 3) center(nx.tok, j - 3); }
      SCHED();
      acc[j] = mfma(cu.f[j], cu.tok, acc[j]);
      SCHED();
    }
  };
  auto adv = [&]() {
    wo = wo + C_WS == 3 * C_WS ? 0 : wo + C_WS;
    xo = xo + 16384 == LDS_BYTES ? XB : xo + 16384;
  };
  {
    u32x2 st[8];
    load_stats(stats, 128 * rb + 32 * w, lane, st);
    combine_stats(st, mean, rstd);
    asm volatile("" : "+v"(mean), "+v"(rstd));
  }
  cbar();
  STAMP(1);
  rdt(A, 0);
#pragma unroll
  for (int j = 0; j < 7; ++j) rdf(A, 0, j);
#pragma unroll
  for (int e = 0; e < 4; ++e) center(A.tok, e);
#pragma unroll 1
  for (int s = 0; s < 7; ++s) {
    grp(B, A, 1, true);
    grp(A, B, 2, true);
    grp(B, A, 3, true);
    adv();
    cbar();
    grp(A, B, 0, true);
  }
  grp(B, A, 1, true);
  grp(A, B, 2, true);
  grp(B, A, 3, true);
  cbar();
  STAMP(2);
  grp(A, B, 0, false);
  const rsrc_t ro = mk_rsrc((char*)hh + ((size_t)(4 * rb + w) * 224 + 14 * fc) * 512, 7168);
  const float ng = -1.4426950408889634f * rstd;
#pragma unroll
  for (int j = 0; j < 7; ++j) {
    float o[8];
#pragma unroll
    for (int i = 0; i < 8; i += 2) {
      f32x2 g = {acc[j][i], acc[j][i + 1]}, u = {acc[j][8 + i], acc[j][9 + i]};
      g *= ng; u *= rstd;
      f32x2 e = {__builtin_amdgcn_exp2f(g[0]), __builtin_amdgcn_exp2f(g[1])};
      e += 1.f;
      f32x2 sg = {__builtin_amdgcn_rcpf(e[0]), __builtin_amdgcn_rcpf(e[1])};
      u *= sg;
      o[i] = u[0]; o[i + 1] = u[1];
    }
    u32x4 v = {pk2(o[0], o[1]), pk2(o[2], o[3]), pk2(o[4], o[5]), pk2(o[6], o[7])};
    __builtin_amdgcn_raw_buffer_store_b128(v, ro, lane * 16 + 1024 * j, 0, SC1);
    SCHED();
  }
}

__device__ __forceinline__ void ln0_tile(const u16* x, u16* xn, int tb, int et, int w, int lane STAMP_ARG) {
  STAMP(0);
#define LN0 1
  const bf16x2 one = __builtin_bit_cast(bf16x2, 0x3f803f80u);
  const rsrc_t rs = mk_rsrc(x + (size_t)(64 * tb + 16 * w) * DM, 16 * DM * 2);
  const rsrc_t ro = mk_rsrc((char*)xn + ((size_t)(2 * tb + (w >> 1)) * 64 + 8 * et) * 512 + (w & 1) * 256, 4096);
  const int r = lane >> 3, c = lane & 7;
  u32x4 t[2][8];
#pragma unroll
  for (int i = 0; i < 8; ++i)
#pragma unroll
    for (int g = 0; g < 2; ++g) t[g][i] = __builtin_amdgcn_raw_buffer_load_b128(rs, (8 * g + r) * DM * 2 + c * 16, ((i + et) & 7) * 128, 0);
  SCHED();
  bar();
  STAMP(1);
  float mean[2], rstd[2];
#pragma unroll
  for (int g = 0; g < 2; ++g) {
    float s0 = 0.f, s1 = 0.f, q0 = 0.f, q1 = 0.f;
#pragma unroll
    for (int i = 0; i < 8; ++i)
#pragma unroll
      for (int e = 0; e < 4; e += 2) {
        const unsigned ua = t[g][i][e], ub = t[g][i][e + 1];
        const bf16x2 a = __builtin_bit_cast(bf16x2, ua), b = __builtin_bit_cast(bf16x2, ub);
        s0 = __builtin_amdgcn_fdot2_f32_bf16(a, one, s0, false); s1 = __builtin_amdgcn_fdot2_f32_bf16(b, one, s1, false);
        q0 = __builtin_amdgcn_fdot2_f32_bf16(a, a, q0, false);   q1 = __builtin_amdgcn_fdot2_f32_bf16(b, b, q1, false);
      }
    const float s = oct_sum(s0 + s1), q = oct_sum(q0 + q1);
    mean[g] = s * (1.f / 512.f);
    rstd[g] = rsqrtf(fmaxf(q * (1.f / 512.f) - mean[g] * mean[g], 0.f) + 1e-5f);
  }
  STAMP(2);
#pragma unroll
  for (int g = 0; g < 2; ++g) {
    u32x4 o;
#pragma unroll
    for (int e = 0; e < 4; ++e) o[e] = pk2((bflo(t[g][0][e]) - mean[g]) * rstd[g], (bfhi(t[g][0][e]) - mean[g]) * rstd[g]);
    __builtin_amdgcn_raw_buffer_store_b128(o, ro, c * 512 + (8 * g + r) * 16, 0, SC1);
  }
}
struct EarlyP0 { static constexpr int at(int j) { return j < NA + NG ? 0 : -1; } };

__global__ __launch_bounds__(512, 2) __attribute__((amdgpu_waves_per_eu(2, 2))) void k_main(Ptrs p) {
  extern __shared__ __attribute__((aligned(16))) char smem[];
  lds_t lds = (lds_t)smem;
  const int lane = threadIdx.x & 63, wid = __builtin_amdgcn_readfirstlane(threadIdx.x >> 6);
  const int xcd = blockIdx.x & 7, j = blockIdx.x >> 3;
  const int tb = xcd * 4 + (j & 3), et = j >> 2;
  const int rb = (xcd & 1) * 8 + (j & 7), fc = (xcd >> 1) * 4 + (j >> 3);
  SPAN_BEGIN();
  SPAN_MAIN();
  PEV(0);
  if (wid >= 4) {
    {
      PLoader L(lds, p, fresh_s(tb), fresh_s(et), fresh_s(wid - 4), fresh_v(lane), ROT);
      YLoader N(lds, p, fresh_s(et), fresh_s(wid - 4), fresh_v(lane));
      const rsrc_t rx = L.xr(p.xn, et);
      bar();
      static_for<0, NA>([&](auto j) { L.W(j); });
      bar(); bar();
      static_for<NA, NA + NG>([&](auto j) { L.W(j); });
      bar();
      l_phase<GeoP, EarlyP0, GeoY, EarlyY>(L, rx, N);
    }
#pragma unroll 1
    for (int l = 0; l < 2; ++l) {
      {
        YLoader L(lds, p, fresh_s(et), fresh_s(wid - 4), fresh_v(lane));
        CLoader N(lds, p, fresh_s(fc), fresh_s(wid - 4), fresh_v(lane));
        const rsrc_t rx = L.xr(p.pq + l * PQ_L, fresh_s(tb));
        l_seam<GeoY, EarlyY>(L);
        l_phase<GeoY, EarlyY, GeoC, EarlyC>(L, rx, N);
      }
      {
        CLoader L(lds, p, fresh_s(fc), fresh_s(wid - 4), fresh_v(lane));
        DLoader N(lds, p, fresh_s(tb), fresh_s(et), fresh_s(wid - 4), fresh_v(lane));
        const rsrc_t rx = L.xr(p.xa + l * XN_L, fresh_s(rb));
        l_seam<GeoC, EarlyC>(L);
        l_phase<GeoC, EarlyC, GeoD, EarlyD>(L, rx, N);
      }
      if (l == 0) {
        {
          DLoader L(lds, p, fresh_s(tb), fresh_s(et), fresh_s(wid - 4), fresh_v(lane));
          PLoader N(lds, p, fresh_s(tb), fresh_s(et), fresh_s(wid - 4), fresh_v(lane), ROT2);
          const rsrc_t rx = L.xr(p.hh, fresh_s(tb));
          l_seam<GeoD, EarlyD>(L);
          l_phase<GeoD, EarlyD, GeoP, EarlyP>(L, rx, N);
        }
        PLoader L(lds, p, fresh_s(tb), fresh_s(et), fresh_s(wid - 4), fresh_v(lane), ROT2);
        YLoader N(lds, p, fresh_s(et), fresh_s(wid - 4), fresh_v(lane));
        const rsrc_t rx = L.xr(p.xn + XN_L, fresh_s(et));
        l_seam<GeoP, EarlyP>(L);
        l_phase<GeoP, EarlyP, GeoY, EarlyY>(L, rx, N);
      } else {
        DLoader L(lds, p, fresh_s(tb), fresh_s(et), fresh_s(wid - 4), fresh_v(lane));
        const rsrc_t rx = L.xr(p.hh + HH_L, fresh_s(tb));
        l_seam<GeoD, EarlyD>(L);
        l_phase<GeoD, EarlyD, GeoP, EarlyNone>(L, rx, L);
      }
    }
  } else {
    const int w = wid, tw = w & 1, fw = w >> 1;
    const int row0 = 64 * tb + 32 * tw, col0 = 64 * et + 32 * fw;
    unsigned dummy = 0;
    const u64 found = __hip_atomic_load((gu64*)p.epoch, RLX_AGENT);
    u32x4 r0, r1;
    {
      const int r = lane & 31, h = lane >> 5;
      const rsrc_t rr = mk_rsrc(p.x0 + (size_t)row0 * DM + col0, 32 * DM * 2);
      r0 = __builtin_amdgcn_raw_buffer_load_b128(rr, r * DM * 2 + h * 16, 0, 0);
      r1 = __builtin_amdgcn_raw_buffer_load_b128(rr, r * DM * 2 + h * 16 + 32, 0, 0);
    }
    STAMP_PTR(9);
    ln0_tile(p.x0, p.xn, tb, et, w, lane STAMP_PASS(9));
    STAMP(3);
    const u64 e = found + 0x100000001ull;
    const u64 tag = ((u64)(unsigned)__builtin_amdgcn_readfirstlane((unsigned)(e >> 32)) << 32) | (unsigned)__builtin_amdgcn_readfirstlane((unsigned)e);
    WAIT_V(0); STAMP(4); bar(); STAMP(5);
    if (w == 0) signal(p.mbox, tag, 0, (lane & 31) * 8 + et, tb, lane < 32);
    bar();
    if (w == 0) await(p.mbox, tag, p.tmo, 0, tb * 8 + et, 32, lane);
    STAMP(6);
    bar(); STAMP(7);
#pragma unroll 1
    for (int l = 0; l < 2; ++l) {
      const int sb = 4 * l + 1;
      {
        PEV(1 + 2 * (4 * l + 0));
        STAMP_PTR(4 * l + 0);
        STAMP(0);
        const int ln = fresh_v(lane), li = blockIdx.x * 64 + ln, lj = blockIdx.x * 32 + (ln & 31);
        const char* t0 = li < 14336 ? (const char*)p.wd + (size_t)li * 128 : (const char*)p.wd;
        const char* t1 = (ln < 32 ? (const char*)p.dre : (const char*)p.dim) + (size_t)(lj & 4095) * 128;
        const char* t2 = (const char*)p.wg + (size_t)(li < 14336 ? li : 0) * 128;
        const char* t3 = (const char*)p.wu + (size_t)(li < 14336 ? li : 0) * 128;
        p_compute(lds, p.pq + l * PQ_L, fresh_s(tb), fresh_s(et), fresh_s(w), fresh_v(lane), t0, t1, t2, t3, l == 0 && w == 0 && blockIdx.x < 224, dummy STAMP_PASS(4 * l + 0));
        PEV(2 + 2 * (4 * l + 0));
        STAMP(5); WAIT_V(0); STAMP(6); bar();
        asm volatile("" :: "v"(dummy));
        if (w == 0) signal(p.mbox, tag, sb + 0, tb * 8 + (lane & 7), et, lane < 8);
        bar();
        if (w == 0) await(p.mbox, tag, p.tmo, sb + 0, tb * 8 + et, 8, lane);
        bar(); STAMP(7);
      }
      {
        PEV(1 + 2 * (4 * l + 1));
        STAMP_PTR(4 * l + 1);
        STAMP(0);
        const int ln = fresh_v(lane), r = ln & 31, h = ln >> 5;
        const unsigned a0 = tw * 8192 + ln * 16, b0 = km_read_base(32 * fw + perm23(r), h);
        f32x16 acc = tile64_loop<GeoY>(lds, a0, [&](lds_t wb, int k) { return ld_b128(wb + (k >> 2) * 8192 + (b0 ^ ((k & 3) << 5))); }, w, lane STAMP_PASS(4 * l + 1));
        add_res(acc, r0, r1);
        store_blocked(p.xa + l * XN_L, r0, r1, row0, col0, fresh_v(lane));
        float v[16], m, m2;
        part_stats(r0, r1, v, m, m2);
        store_stats(p.stats + l * ST_L, m, m2, row0, col0, fresh_v(lane));
        PEV(2 + 2 * (4 * l + 1));
        STAMP(5); WAIT_V(0); STAMP(6); bar();
        if (w == 0) signal(p.mbox, tag, sb + 1, (tb >> 1) * 16 + (lane & 15), (tb & 1) * 8 + et, lane < 16);
        bar();
        if (w == 0) await(p.mbox, tag, p.tmo, sb + 1, rb * 16 + fc, 16, lane);
        bar(); STAMP(7);
      }
      {
        PEV(1 + 2 * (4 * l + 2));
        STAMP_PTR(4 * l + 2);
        STAMP(0);
        c_compute(lds, p.stats + l * ST_L, p.hh + l * HH_L, fresh_s(rb), fresh_s(fc), fresh_s(w), fresh_v(lane) STAMP_PASS(4 * l + 2));
        PEV(2 + 2 * (4 * l + 2));
        STAMP(5); WAIT_V(0); STAMP(6); bar();
        if (w == 0) signal(p.mbox, tag, sb + 2, (2 * rb + ((lane >> 3) & 1)) * 8 + (lane & 7), fc, lane < 16);
        bar();
        if (w == 0) await(p.mbox, tag, p.tmo, sb + 2, tb * 8 + et, 16, lane);
        bar(); STAMP(7);
      }
      {
        PEV(1 + 2 * (4 * l + 3));
        STAMP_PTR(4 * l + 3);
        STAMP(0);
        const int ln = fresh_v(lane);
        const unsigned a0 = tw * 8192 + ln * 16, f0 = kn_read_base(ln, fw);
        f32x16 acc = tile64_loop<GeoD>(lds, a0, [&](lds_t wb, int k) { return ld_tr(wb + f0 + k * 2048, wb + f0 + k * 2048 + 512); }, w, lane STAMP_PASS(4 * l + 3));
        add_res(acc, r0, r1);
        if (l == 1) {
          store_rowmajor(p.out, r0, r1, row0, col0, fresh_v(lane));
          if (w == 0 && blockIdx.x == 0 && lane == 0) __hip_atomic_store((gu64*)p.epoch, tag, RLX_AGENT);
          PEV(2 + 2 * 7);
          if (PA >= 0 && blockIdx.x == 0 && w == 0) {
            WAIT_V(0);
            const unsigned long long now = __builtin_amdgcn_s_memrealtime();
            unsigned long long T = __hip_atomic_load((gu64*)p.stamps + (PB < 0 ? 0 : PB), RLX_AGENT) - __hip_atomic_load((gu64*)p.stamps + (PA < 0 ? 0 : PA), RLX_AGENT);
            if (T > 10000) T = 10000;
            while (__builtin_amdgcn_s_memrealtime() < now + PM * T) __builtin_amdgcn_s_sleep(1);
          }
          if (w == 0) SPAN_END();
        } else {
          float v[16], m, m2;
          part_stats(r0, r1, v, m, m2);
          const int le = fresh_v(lane);
          store_gran(p.gran, tag, m, m2, row0, col0, le);
          STAMP(3);
          u32x2 st[8];
          for (unsigned spins = 0;; ++spins) {
            asm volatile("" ::: "memory");
            if (__all(load_gran(p.gran, tag, row0, le, st))) break;
            if (spins > SPIN_CAP) { if (lane == 0) __hip_atomic_store((gu32*)p.tmo, 2u, RLX_AGENT); break; }
            __builtin_amdgcn_s_sleep(1);
          }
          STAMP(4);
          float mean, rstd;
          combine_stats(st, mean, rstd);
          u32x4 n0, n1;
#pragma unroll
          for (int i = 0; i < 4; ++i) {
            n0[i] = pk2((v[2 * i] - mean) * rstd, (v[2 * i + 1] - mean) * rstd);
            n1[i] = pk2((v[8 + 2 * i] - mean) * rstd, (v[8 + 2 * i + 1] - mean) * rstd);
          }
          store_blocked(p.xn + XN_L, n0, n1, row0, col0, le);
          PEV(2 + 2 * 3);
          STAMP(5); WAIT_V(0); STAMP(6); bar();
          if (w == 0) signal(p.mbox, tag, 4, (lane & 31) * 8 + et, tb, lane < 32);
          bar();
          if (w == 0) await(p.mbox, tag, p.tmo, 4, tb * 8 + et, 32, lane);
          bar(); STAMP(7);
        }
      }
    }
  }
}

}

extern "C" void kernel_launch(void* const* d_in, const int* in_sizes, int n_in, void* d_out, int out_size, void* d_ws, size_t ws_size, hipStream_t stream) {
  Ptrs p;
  p.x0 = (const u16*)d_in[0]; p.sre = (const u16*)d_in[1]; p.sim = (const u16*)d_in[2]; p.dre = (const u16*)d_in[3]; p.dim = (const u16*)d_in[4];
  p.wg = (const u16*)d_in[6]; p.wu = (const u16*)d_in[7]; p.wd = (const u16*)d_in[8];
  char* ws = (char*)d_ws;
  const size_t MB = 1 << 20;
  p.out = (u16*)d_out;
  p.xn = (u16*)ws;
  p.pq = (u16*)(ws + 4 * MB);
  p.xa = (u16*)(ws + 12 * MB);
  p.hh = (u16*)(ws + 16 * MB);
  p.stats = (float*)(ws + 31 * MB);
  p.stamps = (unsigned long long*)(ws + 32 * MB);
  p.gran = (unsigned*)(ws + 34 * MB);
  p.mbox = (unsigned long long*)(ws + 35 * MB);
  p.tmo = (unsigned*)(ws + 36 * MB);
  p.epoch = (unsigned long long*)(ws + 36 * MB + 256);
  (void)hipFuncSetAttribute((const void*)k_main, hipFuncAttributeMaxDynamicSharedMemorySize, LDS_BYTES);
  k_main<<<256, 512, LDS_BYTES, stream>>>(p);
}
```

```cpp
#include <hip/hip_runtime.h>
#include <stdint.h>

namespace {
typedef unsigned short u16;
typedef short s16x4 __attribute__((ext_vector_type(4)));
typedef short s16x8 __attribute__((ext_vector_type(8)));
typedef __bf16 bf16x8 __attribute__((ext_vector_type(8)));
typedef __bf16 bf16x2 __attribute__((ext_vector_type(2)));
typedef float f32x2 __attribute__((ext_vector_type(2)));
typedef float f32x4 __attribute__((ext_vector_type(4)));
typedef float f32x16 __attribute__((ext_vector_type(16)));
typedef unsigned u32x4 __attribute__((ext_vector_type(4)));
typedef unsigned u32x2 __attribute__((ext_vector_type(2)));
#define LAS __attribute__((address_space(3)))
typedef __amdgpu_buffer_rsrc_t rsrc_t;
typedef LAS char* lds_t;
typedef __attribute__((address_space(1))) unsigned gu32;
typedef __attribute__((address_space(1))) unsigned long long gu64;
#define RLX_AGENT __ATOMIC_RELAXED, __HIP_MEMORY_SCOPE_AGENT

constexpr int SEQ = 2048, DM = 512, FF = 1792;
constexpr int LDS_BYTES = 163840, XB = 98304;
constexpr int SC1 = 16;
constexpr unsigned SPIN_CAP = 1u << 18;

__device__ __forceinline__ rsrc_t mk_rsrc(const void* p, unsigned bytes) { return __builtin_amdgcn_make_buffer_rsrc((void*)p, 0, bytes, 0x00020000); }
template <int AUX> __device__ __forceinline__ void dma16(rsrc_t rs, lds_t dst, unsigned voff, unsigned soff) {
  __builtin_amdgcn_raw_ptr_buffer_load_lds(rs, (LAS void*)dst, 16, voff, soff, 0, AUX);
}
#define WAIT_V(n) asm volatile("s_waitcnt vmcnt(%0)" ::"n"(n) : "memory")
#define SCHED() __builtin_amdgcn_sched_barrier(0)
#define WAIT_L0() __builtin_amdgcn_s_waitcnt(0xC07F)
__device__ __forceinline__ void bar() { asm volatile("" ::: "memory"); __builtin_amdgcn_s_barrier(); asm volatile("" ::: "memory"); }
__device__ __forceinline__ void cbar() { WAIT_L0(); bar(); }
__device__ __forceinline__ int fresh_v(int x) { asm volatile("" : "+v"(x)); return x; }
__device__ __forceinline__ int fresh_s(int x) { asm volatile("" : "+s"(x)); return x; }

template <int I> struct IC { static constexpr int v = I; };
template <int A, int B, class F> __device__ __forceinline__ void static_for(F&& f) {
  if constexpr (A < B) { f(IC<A>{}); static_for<A + 1, B>(f); }
}

#define PXI 3
#define ROT 8
#define TS 17
#define KT 1
#define ROT2 0
#define ROTD 1
struct GeoP { static constexpr int NST = 32, NX = 8, nX = 2, NW = 6, nW = 4, OW = 0; };
struct GeoY { static constexpr int NST = 8,  NX = 4, nX = 4, NW = 6, nW = 4, OW = 4; };
struct GeoC { static constexpr int NST = 8,  NX = 4, nX = 4, NW = 3, nW = 7, OW = 0; };
struct GeoD { static constexpr int NST = 14, NX = 4, nX = 4, NW = 6, nW = 4, OW = 4; };
constexpr int GAP = 39;
#define L6(a, b, c, d, e, f) {a, b, c, d, e, f}
#define L3(a, b, c) {a, b, c}
#define EY L6(29, 30, 31, GAP, 27, 28)
#define EC L3(4, 6, GAP)
#define ED L6(6, 7, 7, GAP, GAP, GAP)
#define EP L6(9, 10, 11, 12, 13, GAP)
struct EarlyNone { static constexpr int at(int) { return -1; } };
struct EarlyY { static constexpr int at(int j) { constexpr int t[6] = EY; return j < 6 ? t[j] : -1; } };
struct EarlyC { static constexpr int at(int j) { constexpr int t[3] = EC; return j < 3 ? t[j] : -1; } };
struct EarlyD { static constexpr int at(int j) { constexpr int t[6] = ED; return j < 6 ? t[j] : -1; } };
struct EarlyP { static constexpr int at(int j) { constexpr int t[6] = EP; return j < 6 ? t[j] : -1; } };

constexpr int PLAN_MAX = 40;
template <class G, class MINE, class GN, class NEXT> struct Plan {
  int xstep[PLAN_MAX], wstep[PLAN_MAX], waitn[PLAN_MAX];
  constexpr Plan() : xstep{}, wstep{}, waitn{} {
    int nx = 0;
    for (int j = 0; j < G::NST; ++j) { xstep[j] = -2; wstep[j] = MINE::at(j) >= 0 ? -3 : -2; }
    for (int s = -1; s < G::NST; ++s) {
      const int lx = s - 1 + G::NX < G::NX - 1 ? G::NX - 1 : s - 1 + G::NX, lw = s - 1 + G::NW < G::NW - 1 ? G::NW - 1 : s - 1 + G::NW;
      for (int c = 0; c < (s < 0 ? PXI : 2); ++c) if (nx < G::NST && nx <= lx) xstep[nx++] = s;
      int c = 0;
      for (int j = 0; j < G::NST; ++j)
        if (wstep[j] == -2 && j <= lw && (s < 0 ? (j < PXI) : c < 2)) { wstep[j] = s; ++c; }
    }
    for (int s = 0; s < G::NST; ++s) {
      int total = 0, last = 0;
      for (int b = 0; b < PLAN_MAX; ++b)
        for (int j = 0; j < G::NST; ++j) if (MINE::at(j) == b) { total += G::nW; if (j == s) last = total; }
      for (int t = -1; t < s; ++t) {
        for (int j = 0; j < G::NST; ++j) {
          if (xstep[j] == t) { total += G::nX; if (j == s) last = total; }
          if (t < 0 && wstep[j] == t) { total += G::nW; if (j == s) last = total; }
        }
        if (t >= 0) {
          for (int j = 0; j < G::NST; ++j) if (wstep[j] == t) { total += G::nW; if (j == s) last = total; }
          for (int j = 0; j < GN::NST; ++j) if (NEXT::at(j) == t) total += GN::nW;
        }
      }
      waitn[s] = total - last > 63 ? 63 : total - last;
    }
  }
  constexpr bool ok() const {
    for (int j = 0; j < G::NST; ++j) if (xstep[j] >= j || xstep[j] < -1 || wstep[j] >= j || wstep[j] == -2) return false;
    return true;
  }
};
template <class G, class MINE, class GN, class NEXT> struct PlanOf { static constexpr Plan<G, MINE, GN, NEXT> v{}; };
template <class G, class MINE, class GN, class NEXT, class FX, class FW, class FN>
__device__ __forceinline__ void l_run(FX issueX, FW issueW, FN issueNext) {
  using PL = PlanOf<G, MINE, GN, NEXT>;
  static_assert(PL::v.ok(), "a stage would be waited for before it is issued");
  static_for<0, G::NST>([&](auto j) {
    if constexpr (PL::v.xstep[j.v] == -1) issueX(j);
    if constexpr (PL::v.wstep[j.v] == -1) issueW(j);
  });
  static_for<0, G::NST>([&](auto s) {
    WAIT_V(PL::v.waitn[s.v]);
    bar();
    static_for<0, G::NST>([&](auto j) { if constexpr (PL::v.xstep[j.v] == s.v) issueX(j); });
    static_for<0, G::NST>([&](auto j) { if constexpr (PL::v.wstep[j.v] == s.v) issueW(j); });
    static_for<0, GN::NST>([&](auto j) { if constexpr (NEXT::at(j.v) == s.v) issueNext(j); });
  });
  bar();
  static_for<0, GN::NST>([&](auto j) { if constexpr (NEXT::at(j.v) == G::NST) issueNext(j); });
}
template <class G> constexpr int wslot(int j) { return (G::OW + j) % G::NW; }

__device__ __forceinline__ bf16x8 ld_b128(lds_t p) { return *(LAS bf16x8*)p; }
__device__ __forceinline__ bf16x8 ld_tr(lds_t p0, lds_t p1) {
  s16x4 a = __builtin_amdgcn_ds_read_tr16_b64_v4i16((LAS s16x4*)p0);
  s16x4 b = __builtin_amdgcn_ds_read_tr16_b64_v4i16((LAS s16x4*)p1);
  s16x8 f = __builtin_shufflevector(a, b, 0, 1, 2, 3, 4, 5, 6, 7);
  return __builtin_bit_cast(bf16x8, f);
}
__device__ __forceinline__ f32x16 mfma(bf16x8 a, bf16x8 b, f32x16 c) { return __builtin_amdgcn_mfma_f32_32x32x16_bf16(a, b, c, 0, 0, 0); }
__device__ __forceinline__ unsigned pk2(float a, float b) { f32x2 v = {a, b}; return __builtin_bit_cast(unsigned, __builtin_convertvector(v, bf16x2)); }
__device__ __forceinline__ float bflo(unsigned u) { return __uint_as_float(u << 16); }
__device__ __forceinline__ float bfhi(unsigned u) { return __uint_as_float(u & 0xffff0000u); }
__device__ __forceinline__ float xhalf_sum(float v) {
  auto r = __builtin_amdgcn_permlane32_swap(__float_as_uint(v), __float_as_uint(v), false, false);
  return __uint_as_float(r[0]) + __uint_as_float(r[1]);
}
__device__ __forceinline__ unsigned km_read_base(int row, int h) { return row * 128 + ((h ^ ((row >> 1) & 7)) << 4); }
__device__ __forceinline__ int perm23(int i) { return (i & ~12) | ((i & 4) << 1) | ((i & 8) >> 1); }
__device__ __forceinline__ unsigned km_src(int lw, int lane, int pitch) {
  return (8 * lw + (lane >> 3)) * pitch + (((lane & 7) ^ (((lw & 1) << 2) | (lane >> 4))) << 4);
}
__device__ __forceinline__ unsigned kn_src(int lw, int lane, int pitch) {
  return (8 * lw + (lane >> 3)) * pitch + (((lane & 7) ^ (((lane >> 4) & 1) << 2)) << 4);
}
__device__ __forceinline__ unsigned kn_read_base(int lane, int ft) {
  int h = lane >> 5, blk = (lane >> 4) & 1, q = (lane & 15) >> 2, p = lane & 3, pp = ((p & 1) << 1) | (p >> 1);
  int c = 4 * ft + 2 * blk + (pp >> 1);
  return (8 * h + q) * 128 + ((c ^ (((q >> 1) & 1) << 2)) << 4) + 8 * (pp & 1);
}

#define SPAN_BEGIN()
#define SPAN_MAIN()
#define SPAN_END()
#define STAMP(i)
#define STAMP_ARG
#define STAMP_PASS(ph)
#define STAMP_PTR(ph)

struct Ptrs {
  unsigned long long* stamps;
  const u16 *x0, *sre, *sim, *dre, *dim, *wg, *wu, *wd;
  u16 *out, *xn, *pq, *xa, *hh;
  float* stats;
  unsigned *csf, *gran, *mbox, *tmo;
};
constexpr size_t XN_L = (size_t)SEQ * DM, PQ_L = (size_t)SEQ * 1024, HH_L = (size_t)SEQ * FF, ST_L = (size_t)16 * SEQ * 2;

__device__ __forceinline__ void signal(unsigned* mbox, int seam, int consumer, int slot, bool active) {
  if (active) __hip_atomic_store((gu32*)mbox + ((size_t)seam * 256 + consumer) * 32 + slot, 1u, RLX_AGENT);
}
__device__ __forceinline__ void await(unsigned* mbox, unsigned* tmo, int seam, int me, int nprod, int lane) {
  gu32* q = (gu32*)mbox + ((size_t)seam * 256 + me) * 32 + (lane < nprod ? lane : 0);
  for (unsigned spins = 0;; ++spins) {
    unsigned x = __hip_atomic_load(q, RLX_AGENT);
    if (__all(x == 1u)) break;
    if (spins > SPIN_CAP) { if (lane == 0) __hip_atomic_store((gu32*)tmo, 1u, RLX_AGENT); break; }
    __builtin_amdgcn_s_sleep(1);
  }
}

struct PLoader {
  rsrc_t rre, rim; unsigned vw, vx; lds_t lds; int lw, k0;
  __device__ __forceinline__ PLoader(lds_t lds_, const Ptrs& p, int tb, int et, int lw_, int lane, int rot) : lds(lds_), lw(lw_), k0((rot * et) & 31) {
    rre = mk_rsrc(p.sre + (size_t)tb * 64 * SEQ, 64 * SEQ * 2); rim = mk_rsrc(p.sim + (size_t)tb * 64 * SEQ, 64 * SEQ * 2);
    vw = km_src(lw, lane, SEQ * 2);
    vx = (lane >> 5) * 512 + (((lane & 31) ^ (4 * ((2 * lw + (lane >> 5)) & 3))) << 4);
  }
  template <class S> __device__ __forceinline__ void W(S s) {
    const int ks = (s.v + k0) & 31;
    static_for<0, 4>([&](auto n) { dma16<0>(n.v < 2 ? rre : rim, lds + wslot<GeoP>(s.v) * 16384 + (lw + 4 * n.v) * 1024, vw, ks * 128 + (n.v & 1) * 32 * SEQ * 2); });
  }
  template <class S> __device__ __forceinline__ void X(S s, rsrc_t rx) {
    const int ks = (s.v + k0) & 31;
    static_for<0, 2>([&](auto n) { dma16<SC1>(rx, lds + XB + (s.v % 8) * 8192 + n.v * 4096 + lw * 1024, vx, (2 * ks + n.v) * 64 * 512); });
  }
  __device__ __forceinline__ rsrc_t xr(const u16* xn, int et) { return mk_rsrc((const char*)xn + (8 * et + 2 * lw) * 512, SEQ * DM * 2 - (8 * et + 2 * lw) * 512); }
};
struct YLoader {
  rsrc_t rre, rim; unsigned vw, vx; lds_t lds; int lw;
  __device__ __forceinline__ YLoader(lds_t lds_, const Ptrs& p, int et, int lw_, int lane) : lds(lds_), lw(lw_) {
    rre = mk_rsrc(p.dre + (size_t)et * 64 * DM, 64 * DM * 2); rim = mk_rsrc(p.dim + (size_t)et * 64 * DM, 64 * DM * 2);
    vw = km_src(lw, lane, DM * 2); vx = lane * 16;
  }
  template <class S> __device__ __forceinline__ void W(S s) {
    static_for<0, 4>([&](auto i) {
      constexpr int kb = i.v >> 1, n = i.v & 1;
      dma16<0>(s.v < 4 ? rre : rim, lds + wslot<GeoY>(s.v) * 16384 + kb * 8192 + (lw + 4 * n) * 1024, vw, (2 * (s.v & 3) + kb) * 128 + n * 32 * DM * 2);
    });
  }
  template <class S> __device__ __forceinline__ void X(S s, rsrc_t rx) {
    static_for<0, 4>([&](auto i) {
      constexpr int blk = i.v >> 1, n = i.v & 1;
      dma16<SC1>(rx, lds + XB + (s.v % 4) * 16384 + blk * 8192 + n * 1024 + lw * 2048, vx, blk * 128 * 512 + (16 * s.v + 2 * n) * 512);
    });
  }
  __device__ __forceinline__ rsrc_t xr(const u16* pq, int tb) { return mk_rsrc((const char*)pq + ((size_t)2 * tb * 128 + 4 * lw) * 512, 2 * 128 * 512 - 4 * lw * 512); }
};
constexpr int C_WS = 28672;
struct CLoader {
  const char* wsrc[7]; unsigned vx; lds_t lds; int lw;
  __device__ __forceinline__ CLoader(lds_t lds_, const Ptrs& p, int fc, int lw_, int lane) : lds(lds_), lw(lw_) {
    vx = lane * 16;
#pragma unroll
    for (int n = 0; n < 7; ++n) {
      int idx = 64 * n + lane, kr = idx / 28, c = idx - kr * 28;
      const u16* base = c < 14 ? p.wg : p.wu;
      int cc = c < 14 ? c : c - 14;
      wsrc[n] = (const char*)(base + (size_t)(16 * lw + kr) * FF + 112 * fc + 8 * cc);
    }
  }
  template <class S> __device__ __forceinline__ void W(S s) {
    static_for<0, 7>([&](auto n) {
      __builtin_amdgcn_global_load_lds((const unsigned*)(wsrc[n.v] + (size_t)s.v * 64 * FF * 2), (LAS unsigned*)(lds + wslot<GeoC>(s.v) * C_WS + lw * 7168 + n.v * 1024), 16, 0, 0);
    });
  }
  template <class S> __device__ __forceinline__ void X(S s, rsrc_t rx) {
    static_for<0, 4>([&](auto n) { dma16<SC1>(rx, lds + XB + (s.v % 4) * 16384 + lw * 4096 + n.v * 1024, vx, (8 * s.v + 2 * n.v) * 512); });
  }
  __device__ __forceinline__ rsrc_t xr(const u16* xa, int rb) { return mk_rsrc((const char*)xa + (size_t)(4 * rb + lw) * 64 * 512, 64 * 512); }
};
struct DLoader {
  rsrc_t rw; unsigned vw, vx; lds_t lds; int lw, k0;
  __device__ __forceinline__ DLoader(lds_t lds_, const Ptrs& p, int tb, int et, int lw_, int lane) : lds(lds_), lw(lw_), k0(ROTD ? (tb * 3) % 14 : 0) {
    rw = mk_rsrc(p.wd + et * 64, FF * DM * 2 - et * 128);
    vw = kn_src(lw, lane, DM * 2); vx = lane * 16;
  }
  template <class S> __device__ __forceinline__ void W(S s) {
    const int ks = s.v + k0 < 14 ? s.v + k0 : s.v + k0 - 14;
    static_for<0, 4>([&](auto i) {
      constexpr int kb = i.v >> 1, n = i.v & 1;
      dma16<0>(rw, lds + wslot<GeoD>(s.v) * 16384 + kb * 8192 + (lw + 4 * n) * 1024, vw, ((2 * ks + kb) * 64 + 32 * n) * DM * 2);
    });
  }
  template <class S> __device__ __forceinline__ void X(S s, rsrc_t rx) {
    const int ks = s.v + k0 < 14 ? s.v + k0 : s.v + k0 - 14;
    static_for<0, 4>([&](auto i) {
      constexpr int blk = i.v >> 1, n = i.v & 1;
      dma16<SC1>(rx, lds + XB + (s.v % 4) * 16384 + blk * 8192 + n * 1024 + lw * 2048, vx, blk * 224 * 512 + (16 * ks + 2 * n) * 512);
    });
  }
  __device__ __forceinline__ rsrc_t xr(const u16* hh, int tb) { return mk_rsrc((const char*)hh + ((size_t)2 * tb * 224 + 4 * lw) * 512, 2 * 224 * 512 - 4 * lw * 512); }
};
template <class G, class MINE, class LD> __device__ __forceinline__ void l_seam(LD& L) {
  bar(); bar();
  static_for<0, G::NST>([&](auto j) { if constexpr (MINE::at(j.v) == GAP) L.W(j); });
  bar();
}
template <class G, class MINE, class GN, class NEXT, class LD, class LN>
__device__ __forceinline__ void l_phase(LD& L, rsrc_t rx, LN& N) {
  l_run<G, MINE, GN, NEXT>([&](auto s) { L.X(s, rx); }, [&](auto s) { L.W(s); }, [&](auto s) { N.W(s); });
}

__device__ __forceinline__ void p_compute(lds_t lds, u16* pq, int tb, int et, int w, int lane, const char* t0, const char* t1, bool touch, unsigned& dummy STAMP_ARG) {
  const int r = lane & 31, h = lane >> 5;
  const unsigned a0 = km_read_base(32 * w + r, h);
  const int bl = (lane >> 4) & 1, q4 = (lane & 15) >> 2, pp = ((lane & 1) << 1) | ((lane >> 1) & 1), cc0 = 2 * bl + (pp >> 1);
  const unsigned f0 = cc0 * 512 + (((8 * h + q4) ^ (4 * cc0)) << 4) + 8 * (pp & 1), f1 = f0 ^ 64;
  f32x16 acc0 = {}, acc1 = {};
  unsigned wo = GeoP::OW * 16384, xo = XB;
  struct Fr { bf16x8 tok[4], fa[4], fb[4]; };
  Fr A, B;
  auto rdt = [&](Fr& f, int k) { f.tok[k] = ld_b128(lds + wo + (a0 ^ (k << 5))); };
  auto rda = [&](Fr& f, int k) { lds_t xb = lds + xo + (k >> 1) * 4096 + (k & 1) * 256; f.fa[k] = ld_tr(xb + f0, xb + f1); };
  auto rdb = [&](Fr& f, int k) { lds_t xb = lds + xo + (k >> 1) * 4096 + (k & 1) * 256 + 2048; f.fb[k] = ld_tr(xb + f0, xb + f1); };
  auto adv = [&]() {
    wo = wo + 16384 == 6 * 16384 ? 0 : wo + 16384;
    xo = xo + 8192 == LDS_BYTES ? XB : xo + 8192;
  };
  auto step = [&](Fr& nx, const Fr& cu) {
    cbar();
#pragma unroll
    for (int k = 0; k < 4; ++k) {
      rdt(nx, k); rda(nx, k); SCHED();
      acc0 = mfma(cu.fa[k], cu.tok[k], acc0); SCHED();
      rdb(nx, k); SCHED();
      acc1 = mfma(cu.fb[k], cu.tok[k], acc1); SCHED();
    }
    adv();
  };
  cbar();
  STAMP(1);
#pragma unroll
  for (int k = 0; k < 4; ++k) { rdt(A, k); rda(A, k); rdb(A, k); }
  adv();
#pragma unroll 1
  for (int s = 1; s < 31; s += 2) {
    step(B, A);
    if (touch && s == TS) {
      asm volatile("global_load_dword %0, %1, off" : "+v"(dummy) : "v"(t0) : "memory");
      asm volatile("global_load_dword %0, %1, off" : "+v"(dummy) : "v"(t1) : "memory");
    }
    step(A, B);
  }
  step(B, A);
  cbar();
  STAMP(2);
#pragma unroll
  for (int k = 0; k < 4; ++k) { acc0 = mfma(B.fa[k], B.tok[k], acc0); acc1 = mfma(B.fb[k], B.tok[k], acc1); }
  const float sg = w >= 2 ? -1.f : 1.f;
  const rsrc_t ro = mk_rsrc((char*)pq + ((size_t)(2 * tb + (w & 1)) * 128 + (w >> 1) * 64 + 8 * et) * 512, 4096);
#pragma unroll
  for (int ft = 0; ft < 2; ++ft)
#pragma unroll
    for (int q = 0; q < 2; ++q) {
      const f32x16& a = ft ? acc1 : acc0;
      u32x4 v = {pk2(sg * a[8 * q], sg * a[8 * q + 1]), pk2(sg * a[8 * q + 2], sg * a[8 * q + 3]), pk2(sg * a[8 * q + 4], sg * a[8 * q + 5]), pk2(sg * a[8 * q + 6], sg * a[8 * q + 7])};
      __builtin_amdgcn_raw_buffer_store_b128(v, ro, lane * 16 + ft * 2048 + q * 1024, 0, SC1);
    }
}

__device__ __forceinline__ void add_res(const f32x16& acc, u32x4& r0, u32x4& r1) {
#pragma unroll
  for (int i = 0; i < 4; ++i) {
    r0[i] = pk2(acc[2 * i] + bflo(r0[i]), acc[2 * i + 1] + bfhi(r0[i]));
    r1[i] = pk2(acc[8 + 2 * i] + bflo(r1[i]), acc[8 + 2 * i + 1] + bfhi(r1[i]));
  }
}
__device__ __forceinline__ void store_rowmajor(u16* xo, const u32x4& o0, const u32x4& o1, int row0, int col0, int lane) {
  const rsrc_t ro = mk_rsrc(xo + (size_t)row0 * DM + col0, 32 * DM * 2);
  const unsigned vo = (lane & 31) * DM * 2 + (lane >> 5) * 16;
  __builtin_amdgcn_raw_buffer_store_b128(o0, ro, vo, 0, 0);
  __builtin_amdgcn_raw_buffer_store_b128(o1, ro, vo + 32, 0, 0);
}
__device__ __forceinline__ void store_blocked(u16* xo, const u32x4& o0, const u32x4& o1, int row0, int col0, int lane) {
  const rsrc_t ro = mk_rsrc((char*)xo + ((size_t)(row0 >> 5) * 64 + (col0 >> 3)) * 512, 2048);
  __builtin_amdgcn_raw_buffer_store_b128(o0, ro, lane * 16, 0, SC1);
  __builtin_amdgcn_raw_buffer_store_b128(o1, ro, lane * 16 + 1024, 0, SC1);
}
__device__ __forceinline__ void part_stats(const u32x4& o0, const u32x4& o1, float (&v)[16], float& m, float& m2) {
  float s0 = 0.f, s1 = 0.f;
#pragma unroll
  for (int i = 0; i < 4; ++i) { v[2 * i] = bflo(o0[i]); v[2 * i + 1] = bfhi(o0[i]); v[8 + 2 * i] = bflo(o1[i]); v[8 + 2 * i + 1] = bfhi(o1[i]); }
#pragma unroll
  for (int i = 0; i < 8; ++i) { s0 += v[i]; s1 += v[8 + i]; }
  m = xhalf_sum(s0 + s1) * (1.f / 32.f);
  float q0 = 0.f, q1 = 0.f;
#pragma unroll
  for (int i = 0; i < 8; ++i) { float d = v[i] - m, e = v[8 + i] - m; q0 += d * d; q1 += e * e; }
  m2 = xhalf_sum(q0 + q1);
}
__device__ __forceinline__ void store_stats(float* stats, float m, float m2, int row0, int col0, int lane) {
  const rsrc_t rs = mk_rsrc(stats + ((size_t)(col0 >> 5) * SEQ + row0) * 2, 32 * 8);
  if (lane < 32) { u32x2 sv = {__float_as_uint(m), __float_as_uint(m2)}; __builtin_amdgcn_raw_buffer_store_b64(sv, rs, lane * 8, 0, SC1); }
}
__device__ __forceinline__ void load_stats(const float* stats, int row0, int lane, u32x2 (&t)[8]) {
  gu64* q = (gu64*)stats + row0 + (lane & 31) + (size_t)(8 * (lane >> 5)) * SEQ;
#pragma unroll
  for (int i = 0; i < 8; ++i) { unsigned long long x = __hip_atomic_load(q + i * SEQ, RLX_AGENT); t[i] = u32x2{(unsigned)x, (unsigned)(x >> 32)}; }
}
__device__ __forceinline__ void combine_stats(const u32x2 (&t)[8], float& mean, float& rstd) {
  float ms = 0.f, m2 = 0.f;
#pragma unroll
  for (int i = 0; i < 8; ++i) { ms += __uint_as_float(t[i][0]); m2 += __uint_as_float(t[i][1]); }
  mean = xhalf_sum(ms) * (1.f / 16.f);
#pragma unroll
  for (int i = 0; i < 8; ++i) { float d = __uint_as_float(t[i][0]) - mean; m2 += 32.f * d * d; }
  rstd = rsqrtf(xhalf_sum(m2) * (1.f / 512.f) + 1e-5f);
}

template <class G, class FF_> __device__ __forceinline__ f32x16 tile64_loop(lds_t lds, unsigned a0, FF_ feat, int w, int lane STAMP_ARG) {
  f32x16 acc = {};
  unsigned wo = G::OW * 16384, xo = XB;
  struct Fr { bf16x8 tok[8], f[8]; };
  Fr A, B;
  auto rd = [&](Fr& fr, int k) {
    fr.tok[k] = ld_b128(lds + xo + a0 + k * 1024);
    fr.f[k] = feat(lds + wo, k);
  };
  auto adv = [&]() {
    wo = wo + 16384 == 6 * 16384 ? 0 : wo + 16384;
    xo = xo + 16384 == LDS_BYTES ? XB : xo + 16384;
  };
  auto step = [&](Fr& nx, const Fr& cu) {
    cbar();
#pragma unroll
    for (int k = 0; k < 8; ++k) { rd(nx, k); SCHED(); acc = mfma(cu.f[k], cu.tok[k], acc); SCHED(); }
    adv();
  };
  cbar();
  STAMP(1);
#pragma unroll
  for (int k = 0; k < 8; ++k) rd(A, k);
  adv();
#pragma unroll 1
  for (int s = 1; s < G::NST - 1; s += 2) { step(B, A); step(A, B); }
  step(B, A);
  cbar();
  STAMP(2);
#pragma unroll
  for (int k = 0; k < 8; ++k) acc = mfma(B.f[k], B.tok[k], acc);
  return acc;
}

__device__ __forceinline__ void c_compute(lds_t lds, const float* stats, const unsigned* csf, u16* hh, int rb, int fc, int w, int lane STAMP_ARG) {
  const int r = lane & 31, h = lane >> 5;
  const unsigned a0 = w * 4096 + lane * 16;
  const int blk = (lane >> 4) & 1, q = (lane & 15) >> 2, pp = ((lane & 1) << 1) | ((lane >> 1) & 1);
  const unsigned f0 = (8 * h + q) * 448 + blk * 224 + 8 * pp;
  f32x16 acc[7];
#pragma unroll
  for (int j = 0; j < 7; ++j) acc[j] = f32x16{};
  unsigned wo = GeoC::OW * C_WS, xo = XB;
  struct Fr { bf16x8 tok, f[7]; };
  Fr A, B;
  auto rdt = [&](Fr& fr, int k) { fr.tok = ld_b128(lds + xo + a0 + k * 1024); };
  auto rdf = [&](Fr& fr, int k, int j) { lds_t wb = lds + wo + f0; fr.f[j] = ld_tr(wb + 16 * k * 448 + 32 * j, wb + (16 * k + 4) * 448 + 32 * j); };
  auto grp = [&](Fr& nx, const Fr& cu, int k, bool rdn) {
#pragma unroll
    for (int j = 0; j < 7; ++j) {
      if (rdn) { if (j == 0) rdt(nx, k); rdf(nx, k, j); }
      SCHED();
      acc[j] = mfma(cu.f[j], cu.tok, acc[j]);
      SCHED();
    }
  };
  auto adv = [&]() {
    wo = wo + C_WS == 3 * C_WS ? 0 : wo + C_WS;
    xo = xo + 16384 == LDS_BYTES ? XB : xo + 16384;
  };
  cbar();
  STAMP(1);
  u32x2 st[8];
  load_stats(stats, 128 * rb + 32 * w, lane, st);
  unsigned long long cs[7];
#pragma unroll
  for (int j = 0; j < 7; ++j) cs[j] = __hip_atomic_load((gu64*)csf + ((size_t)fc * 32 + r) * 8 + j, __ATOMIC_RELAXED, __HIP_MEMORY_SCOPE_WORKGROUP);
  rdt(A, 0);
#pragma unroll
  for (int j = 0; j < 7; ++j) rdf(A, 0, j);
  auto stage = [&]() {
    grp(B, A, 1, true);
    grp(A, B, 2, true);
    grp(B, A, 3, true);
    adv();
    cbar();
    grp(A, B, 0, true);
  };
  stage();
  float mean, rstd;
  combine_stats(st, mean, rstd);
  {
    const unsigned mh = pk2(-mean, 0.f) & 0xffffu;
    const unsigned ml = pk2(-mean - bflo(mh), 0.f) & 0xffffu;
    u32x4 bt = {h ? 0u : (mh | (ml << 16)), h ? 0u : mh, 0u, 0u};
#pragma unroll
    for (int j = 0; j < 7; ++j) {
      u32x4 at = {h ? 0u : (unsigned)cs[j], h ? 0u : (unsigned)(cs[j] >> 32), 0u, 0u};
      acc[j] = mfma(__builtin_bit_cast(bf16x8, at), __builtin_bit_cast(bf16x8, bt), acc[j]);
    }
  }
  SCHED();
#pragma unroll 1
  for (int s = 1; s < 7; ++s) stage();
  grp(B, A, 1, true);
  grp(A, B, 2, true);
  grp(B, A, 3, true);
  cbar();
  STAMP(2);
  grp(A, B, 0, false);
  const rsrc_t ro = mk_rsrc((char*)hh + ((size_t)(4 * rb + w) * 224 + 14 * fc) * 512, 7168);
  const float ng = -1.4426950408889634f * rstd;
#pragma unroll
  for (int j = 0; j < 7; ++j) {
    float o[8];
#pragma unroll
    for (int i = 0; i < 8; i += 2) {
      f32x2 g = {acc[j][i], acc[j][i + 1]}, u = {acc[j][8 + i], acc[j][9 + i]};
      g *= ng; u *= rstd;
      f32x2 e = {__builtin_amdgcn_exp2f(g[0]), __builtin_amdgcn_exp2f(g[1])};
      e += 1.f;
      f32x2 sg = {__builtin_amdgcn_rcpf(e[0]), __builtin_amdgcn_rcpf(e[1])};
      u *= sg;
      o[i] = u[0]; o[i + 1] = u[1];
    }
    u32x4 v = {pk2(o[0], o[1]), pk2(o[2], o[3]), pk2(o[4], o[5]), pk2(o[6], o[7])};
    __builtin_amdgcn_raw_buffer_store_b128(v, ro, lane * 16 + 1024 * j, 0, SC1);
    SCHED();
  }
}

__global__ __launch_bounds__(512, 2) __attribute__((amdgpu_waves_per_eu(2, 2))) void k_main(Ptrs p) {
  extern __shared__ __attribute__((aligned(16))) char smem[];
  lds_t lds = (lds_t)smem;
  const int lane = threadIdx.x & 63, wid = __builtin_amdgcn_readfirstlane(threadIdx.x >> 6);
  const int xcd = blockIdx.x & 7, j = blockIdx.x >> 3;
  const int tb = xcd * 4 + (j & 3), et = j >> 2;
  const int rb = (xcd & 1) * 8 + (j & 7), fc = (xcd >> 1) * 4 + (j >> 3);
  SPAN_MAIN();
  if (wid >= 4) {
    {
      PLoader L(lds, p, fresh_s(tb), fresh_s(et), fresh_s(wid - 4), fresh_v(lane), ROT);
      YLoader N(lds, p, fresh_s(et), fresh_s(wid - 4), fresh_v(lane));
      l_phase<GeoP, EarlyNone, GeoY, EarlyY>(L, L.xr(p.xn, et), N);
    }
#pragma unroll 1
    for (int l = 0; l < 2; ++l) {
      {
        YLoader L(lds, p, fresh_s(et), fresh_s(wid - 4), fresh_v(lane));
        CLoader N(lds, p, fresh_s(fc), fresh_s(wid - 4), fresh_v(lane));
        const rsrc_t rx = L.xr(p.pq + l * PQ_L, fresh_s(tb));
        l_seam<GeoY, EarlyY>(L);
        l_phase<GeoY, EarlyY, GeoC, EarlyC>(L, rx, N);
      }
      {
        CLoader L(lds, p, fresh_s(fc), fresh_s(wid - 4), fresh_v(lane));
        DLoader N(lds, p, fresh_s(tb), fresh_s(et), fresh_s(wid - 4), fresh_v(lane));
        const rsrc_t rx = L.xr(p.xa + l * XN_L, fresh_s(rb));
        l_seam<GeoC, EarlyC>(L);
        l_phase<GeoC, EarlyC, GeoD, EarlyD>(L, rx, N);
      }
      if (l == 0) {
        {
          DLoader L(lds, p, fresh_s(tb), fresh_s(et), fresh_s(wid - 4), fresh_v(lane));
          PLoader N(lds, p, fresh_s(tb), fresh_s(et), fresh_s(wid - 4), fresh_v(lane), ROT2);
          const rsrc_t rx = L.xr(p.hh, fresh_s(tb));
          l_seam<GeoD, EarlyD>(L);
          l_phase<GeoD, EarlyD, GeoP, EarlyP>(L, rx, N);
        }
        PLoader L(lds, p, fresh_s(tb), fresh_s(et), fresh_s(wid - 4), fresh_v(lane), ROT2);
        YLoader N(lds, p, fresh_s(et), fresh_s(wid - 4), fresh_v(lane));
        const rsrc_t rx = L.xr(p.xn + XN_L, fresh_s(et));
        l_seam<GeoP, EarlyP>(L);
        l_phase<GeoP, EarlyP, GeoY, EarlyY>(L, rx, N);
      } else {
        DLoader L(lds, p, fresh_s(tb), fresh_s(et), fresh_s(wid - 4), fresh_v(lane));
        const rsrc_t rx = L.xr(p.hh + HH_L, fresh_s(tb));
        l_seam<GeoD, EarlyD>(L);
        l_phase<GeoD, EarlyD, GeoP, EarlyNone>(L, rx, L);
      }
    }
  } else {
    const int w = wid, tw = w & 1, fw = w >> 1;
    const int row0 = 64 * tb + 32 * tw, col0 = 64 * et + 32 * fw;
    unsigned dummy = 0;
    u32x4 r0, r1;
    {
      const int r = lane & 31, h = lane >> 5;
      const rsrc_t rr = mk_rsrc(p.x0 + (size_t)row0 * DM + col0, 32 * DM * 2);
      r0 = __builtin_amdgcn_raw_buffer_load_b128(rr, r * DM * 2 + h * 16, 0, 0);
      r1 = __builtin_amdgcn_raw_buffer_load_b128(rr, r * DM * 2 + h * 16 + 32, 0, 0);
    }
#pragma unroll 1
    for (int l = 0; l < 2; ++l) {
      const int sb = 4 * l;
      {
        STAMP_PTR(4 * l + 0);
        STAMP(0);
        const int ln = fresh_v(lane), li = blockIdx.x * 64 + ln, lj = blockIdx.x * 32 + (ln & 31);
        const char* t0 = li < 14336 ? (const char*)p.wd + (size_t)li * 128 : (const char*)p.wd;
        const char* t1 = (ln < 32 ? (const char*)p.dre : (const char*)p.dim) + (size_t)(lj & 4095) * 128;
        p_compute(lds, p.pq + l * PQ_L, fresh_s(tb), fresh_s(et), fresh_s(w), fresh_v(lane), t0, t1, l == 0 && w == 0 && blockIdx.x < 224, dummy STAMP_PASS(4 * l + 0));
        STAMP(5); WAIT_V(0); STAMP(6); bar();
        asm volatile("" :: "v"(dummy));
        if (w == 0) signal(p.mbox, sb + 0, tb * 8 + (lane & 7), et, lane < 8);
        bar();
        if (w == 0) await(p.mbox, p.tmo, sb + 0, tb * 8 + et, 8, lane);
        bar(); STAMP(7);
      }
      {
        STAMP_PTR(4 * l + 1);
        STAMP(0);
        const int ln = fresh_v(lane), r = ln & 31, h = ln >> 5;
        const unsigned a0 = tw * 8192 + ln * 16, b0 = km_read_base(32 * fw + perm23(r), h);
        f32x16 acc = tile64_loop<GeoY>(lds, a0, [&](lds_t wb, int k) { return ld_b128(wb + (k >> 2) * 8192 + (b0 ^ ((k & 3) << 5))); }, w, lane STAMP_PASS(4 * l + 1));
        add_res(acc, r0, r1);
        store_blocked(p.xa + l * XN_L, r0, r1, row0, col0, fresh_v(lane));
        float v[16], m, m2;
        part_stats(r0, r1, v, m, m2);
        store_stats(p.stats + l * ST_L, m, m2, row0, col0, fresh_v(lane));
        STAMP(5); WAIT_V(0); STAMP(6); bar();
        if (w == 0) signal(p.mbox, sb + 1, (tb >> 1) * 16 + (lane & 15), (tb & 1) * 8 + et, lane < 16);
        bar();
        if (w == 0) await(p.mbox, p.tmo, sb + 1, rb * 16 + fc, 16, lane);
        bar(); STAMP(7);
      }
      {
        STAMP_PTR(4 * l + 2);
        STAMP(0);
        c_compute(lds, p.stats + l * ST_L, p.csf, p.hh + l * HH_L, fresh_s(rb), fresh_s(fc), fresh_s(w), fresh_v(lane) STAMP_PASS(4 * l + 2));
        STAMP(5); WAIT_V(0); STAMP(6); bar();
        if (w == 0) signal(p.mbox, sb + 2, (2 * rb + ((lane >> 3) & 1)) * 8 + (lane & 7), fc, lane < 16);
        bar();
        if (w == 0) await(p.mbox, p.tmo, sb + 2, tb * 8 + et, 16, lane);
        bar(); STAMP(7);
      }
      {
        STAMP_PTR(4 * l + 3);
        STAMP(0);
        const int ln = fresh_v(lane);
        const unsigned a0 = tw * 8192 + ln * 16, f0 = kn_read_base(ln, fw);
        f32x16 acc = tile64_loop<GeoD>(lds, a0, [&](lds_t wb, int k) { return ld_tr(wb + f0 + k * 2048, wb + f0 + k * 2048 + 512); }, w, lane STAMP_PASS(4 * l + 3));
        add_res(acc, r0, r1);
        if (l == 1) {
          store_rowmajor(p.out, r0, r1, row0, col0, fresh_v(lane));
          if (w == 0) SPAN_END();
        } else {
          float v[16], m, m2;
          part_stats(r0, r1, v, m, m2);
          const int le = fresh_v(lane);
          store_stats((float*)p.gran, m, m2, row0, col0, le);
          STAMP(3);
          u32x2 st[8];
          for (unsigned spins = 0;; ++spins) {
            load_stats((const float*)p.gran, row0, le, st);
            bool ok = true;
#pragma unroll
            for (int i = 0; i < 8; ++i) ok = ok && st[i][1] != 0xffffffffu;
            if (__all(ok)) break;
            if (spins > SPIN_CAP) { if (lane == 0) __hip_atomic_store((gu32*)p.tmo, 2u, RLX_AGENT); break; }
            __builtin_amdgcn_s_sleep(1);
          }
          STAMP(4);
          float mean, rstd;
          combine_stats(st, mean, rstd);
          u32x4 n0, n1;
#pragma unroll
          for (int i = 0; i < 4; ++i) {
            n0[i] = pk2((v[2 * i] - mean) * rstd, (v[2 * i + 1] - mean) * rstd);
            n1[i] = pk2((v[8 + 2 * i] - mean) * rstd, (v[8 + 2 * i + 1] - mean) * rstd);
          }
          store_blocked(p.xn + XN_L, n0, n1, row0, col0, le);
          STAMP(5); WAIT_V(0); STAMP(6); bar();
          if (w == 0) signal(p.mbox, 3, (lane & 31) * 8 + et, tb, lane < 32);
          bar();
          if (w == 0) await(p.mbox, p.tmo, 3, tb * 8 + et, 32, lane);
          bar(); STAMP(7);
        }
      }
    }
  }
}

__device__ __forceinline__ float wave_sum(float v) {
#pragma unroll
  for (int o = 1; o < 64; o <<= 1) v += __shfl_xor(v, o);
  return v;
}
__global__ __launch_bounds__(512) void k_pre(Ptrs p) {
  const int lane = threadIdx.x & 63, wid = threadIdx.x >> 6;
  if (blockIdx.x < 256) {
    SPAN_BEGIN();
    const int row = blockIdx.x * 8 + wid;
    u32x4 t = *(const u32x4*)(p.x0 + (size_t)row * DM + lane * 8);
    const int gid = blockIdx.x * 512 + threadIdx.x;
    if (gid < 7 * 256 * 8) ((u32x4*)p.mbox)[gid] = u32x4{0u, 0u, 0u, 0u};
    if (gid < 16 * SEQ / 2) ((u32x4*)p.gran)[gid] = u32x4{~0u, ~0u, ~0u, ~0u};
    if (gid == 0) *p.tmo = 0u;
    float v[8], s = 0.f;
#pragma unroll
    for (int i = 0; i < 4; ++i) { v[2 * i] = bflo(t[i]); v[2 * i + 1] = bfhi(t[i]); s += v[2 * i] + v[2 * i + 1]; }
    const float m = wave_sum(s) * (1.f / 512.f);
    float m2 = 0.f;
#pragma unroll
    for (int i = 0; i < 8; ++i) { v[i] -= m; m2 += v[i] * v[i]; }
    const float rs = rsqrtf(wave_sum(m2) * (1.f / 512.f) + 1e-5f);
    u32x4 o = {pk2(v[0] * rs, v[1] * rs), pk2(v[2] * rs, v[3] * rs), pk2(v[4] * rs, v[5] * rs), pk2(v[6] * rs, v[7] * rs)};
    *(u32x4*)((char*)p.xn + ((size_t)(row >> 5) * 64 + lane) * 512 + (row & 31) * 16) = o;
  } else if (blockIdx.x >= 312) {
    const int q = blockIdx.x - 312, xcd = q & 7, i = q >> 3, tb = xcd * 4 + (i & 3);
    const int r = 32 * (i >> 2) + 4 * wid + (lane >> 4);
    const char* a = (const char*)(r < 64 ? p.sre : p.sim) + (size_t)(64 * tb + (r & 63)) * (SEQ * 2) + (lane & 15) * 128;
    unsigned d0, d1 = 0;
    asm volatile("global_load_dword %0, %1, off" : "=v"(d0) : "v"(a) : "memory");
    if (KT == 2) asm volatile("global_load_dword %0, %1, off offset:2048" : "=v"(d1) : "v"(a) : "memory");
    asm volatile("s_waitcnt vmcnt(0)" : "+v"(d0), "+v"(d1) :: "memory");
  } else {
    __shared__ float red[8][64];
    const int b = blockIdx.x - 256;
    const u16* w = (b < 28 ? p.wg : p.wu) + (b % 28) * 64 + (lane & 7) * 8;
    float a[8] = {};
#pragma unroll
    for (int i = 0; i < 8; ++i) {
      u32x4 t = *(const u32x4*)(w + (size_t)(i * 64 + wid * 8 + (lane >> 3)) * FF);
#pragma unroll
      for (int e = 0; e < 4; ++e) { a[2 * e] += bflo(t[e]); a[2 * e + 1] += bfhi(t[e]); }
    }
#pragma unroll
    for (int e = 0; e < 8; ++e) { a[e] += __shfl_xor(a[e], 8); a[e] += __shfl_xor(a[e], 16); a[e] += __shfl_xor(a[e], 32); }
    if (lane < 8) {
#pragma unroll
      for (int e = 0; e < 8; ++e) red[wid][lane * 8 + e] = a[e];
    }
    __syncthreads();
    if (threadIdx.x < 64) {
      float s = 0.f;
#pragma unroll
      for (int i = 0; i < 8; ++i) s += red[i][threadIdx.x];
      const int c = (b % 28) * 64 + threadIdx.x, fc = c / 112, rem = c - fc * 112, jj = rem >> 4, i = perm23(rem & 15) + (b < 28 ? 0 : 16);
      const unsigned hi = pk2(s, 0.f) & 0xffffu, lo = pk2(s - bflo(hi), 0.f) & 0xffffu;
      u32x2 o = {hi | (hi << 16), lo};
      *(u32x2*)(p.csf + ((size_t)fc * 32 + i) * 16 + 2 * jj) = o;
    }
  }
}
}

extern "C" void kernel_launch(void* const* d_in, const int* in_sizes, int n_in, void* d_out, int out_size, void* d_ws, size_t ws_size, hipStream_t stream) {
  Ptrs p;
  p.x0 = (const u16*)d_in[0]; p.sre = (const u16*)d_in[1]; p.sim = (const u16*)d_in[2]; p.dre = (const u16*)d_in[3]; p.dim = (const u16*)d_in[4];
  p.wg = (const u16*)d_in[6]; p.wu = (const u16*)d_in[7]; p.wd = (const u16*)d_in[8];
  char* ws = (char*)d_ws;
  const size_t MB = 1 << 20;
  p.out = (u16*)d_out;
  p.xn = (u16*)ws;
  p.pq = (u16*)(ws + 4 * MB);
  p.xa = (u16*)(ws + 12 * MB);
  p.hh = (u16*)(ws + 16 * MB);
  p.stats = (float*)(ws + 31 * MB);
  p.stamps = (unsigned long long*)(ws + 32 * MB);
  p.csf = (unsigned*)(ws + 33 * MB);
  p.gran = (unsigned*)(ws + 34 * MB);
  p.mbox = (unsigned*)(ws + 35 * MB);
  p.tmo = (unsigned*)(ws + 36 * MB);
  (void)hipFuncSetAttribute((const void*)k_main, hipFuncAttributeMaxDynamicSharedMemorySize, LDS_BYTES);
  k_pre<<<256 + 56 + (KT ? 128 : 0), 512, 0, stream>>>(p);
  k_main<<<256, 512, LDS_BYTES, stream>>>(p);
}
```

```cpp
#include <hip/hip_runtime.h>
#include <stdint.h>

namespace {
typedef unsigned short u16;
typedef short s16x4 __attribute__((ext_vector_type(4)));
typedef short s16x8 __attribute__((ext_vector_type(8)));
typedef __bf16 bf16x8 __attribute__((ext_vector_type(8)));
typedef __bf16 bf16x2 __attribute__((ext_vector_type(2)));
typedef float f32x2 __attribute__((ext_vector_type(2)));
typedef float f32x4 __attribute__((ext_vector_type(4)));
typedef float f32x16 __attribute__((ext_vector_type(16)));
typedef unsigned u32x4 __attribute__((ext_vector_type(4)));
typedef unsigned u32x2 __attribute__((ext_vector_type(2)));
#define LAS __attribute__((address_space(3)))
typedef __amdgpu_buffer_rsrc_t rsrc_t;
typedef LAS char* lds_t;
typedef __attribute__((address_space(1))) unsigned gu32;
typedef __attribute__((address_space(1))) unsigned long long gu64;
#define RLX_AGENT __ATOMIC_RELAXED, __HIP_MEMORY_SCOPE_AGENT

constexpr int SEQ = 2048, DM = 512, FF = 1792;
constexpr int LDS_BYTES = 163840, XB = 98304;
constexpr int SC1 = 16;
constexpr unsigned SPIN_CAP = 1u << 18;

__device__ __forceinline__ rsrc_t mk_rsrc(const void* p, unsigned bytes) { return __builtin_amdgcn_make_buffer_rsrc((void*)p, 0, bytes, 0x00020000); }
template <int AUX> __device__ __forceinline__ void dma16(rsrc_t rs, lds_t dst, unsigned voff, unsigned soff) {
  __builtin_amdgcn_raw_ptr_buffer_load_lds(rs, (LAS void*)dst, 16, voff, soff, 0, AUX);
}
#define WAIT_V(n) asm volatile("s_waitcnt vmcnt(%0)" ::"n"(n) : "memory")
#define SCHED() __builtin_amdgcn_sched_barrier(0)
#define WAIT_L0() __builtin_amdgcn_s_waitcnt(0xC07F)
__device__ __forceinline__ void bar() { asm volatile("" ::: "memory"); __builtin_amdgcn_s_barrier(); asm volatile("" ::: "memory"); }
__device__ __forceinline__ void cbar() { WAIT_L0(); bar(); }
__device__ __forceinline__ int fresh_v(int x) { asm volatile("" : "+v"(x)); return x; }
__device__ __forceinline__ int fresh_s(int x) { asm volatile("" : "+s"(x)); return x; }

template <int I> struct IC { static constexpr int v = I; };
template <int A, int B, class F> __device__ __forceinline__ void static_for(F&& f) {
  if constexpr (A < B) { f(IC<A>{}); static_for<A + 1, B>(f); }
}

#define PXI 3
#define ROT 8
#define TS 17
#define KT 2
#define KW 0
#define ROT2 0
#define ROTD 1
struct GeoP { static constexpr int NST = 32, NX = 8, nX = 2, NW = 6, nW = 4, OW = 0; };
struct GeoY { static constexpr int NST = 8,  NX = 4, nX = 4, NW = 6, nW = 4, OW = 4; };
struct GeoC { static constexpr int NST = 8,  NX = 4, nX = 4, NW = 3, nW = 7, OW = 0; };
struct GeoD { static constexpr int NST = 14, NX = 4, nX = 4, NW = 6, nW = 4, OW = 4; };
constexpr int GAP = 39;
#define L6(a, b, c, d, e, f) {a, b, c, d, e, f}
#define L3(a, b, c) {a, b, c}
#define EY L6(29, 30, 31, GAP, 27, 28)
#define EC L3(4, 6, GAP)
#define ED L6(6, 7, 7, GAP, GAP, GAP)
#define EP L6(9, 10, 11, 12, 13, GAP)
struct EarlyNone { static constexpr int at(int) { return -1; } };
struct EarlyY { static constexpr int at(int j) { constexpr int t[6] = EY; return j < 6 ? t[j] : -1; } };
struct EarlyC { static constexpr int at(int j) { constexpr int t[3] = EC; return j < 3 ? t[j] : -1; } };
struct EarlyD { static constexpr int at(int j) { constexpr int t[6] = ED; return j < 6 ? t[j] : -1; } };
struct EarlyP { static constexpr int at(int j) { constexpr int t[6] = EP; return j < 6 ? t[j] : -1; } };

constexpr int PLAN_MAX = 40;
template <class G, class MINE, class GN, class NEXT> struct Plan {
  int xstep[PLAN_MAX], wstep[PLAN_MAX], waitn[PLAN_MAX];
  constexpr Plan() : xstep{}, wstep{}, waitn{} {
    int nx = 0;
    for (int j = 0; j < G::NST; ++j) { xstep[j] = -2; wstep[j] = MINE::at(j) >= 0 ? -3 : -2; }
    for (int s = -1; s < G::NST; ++s) {
      const int lx = s - 1 + G::NX < G::NX - 1 ? G::NX - 1 : s - 1 + G::NX, lw = s - 1 + G::NW < G::NW - 1 ? G::NW - 1 : s - 1 + G::NW;
      for (int c = 0; c < (s < 0 ? PXI : 2); ++c) if (nx < G::NST && nx <= lx) xstep[nx++] = s;
      int c = 0;
      for (int j = 0; j < G::NST; ++j)
        if (wstep[j] == -2 && j <= lw && (s < 0 ? (j < PXI) : c < 2)) { wstep[j] = s; ++c; }
    }
    for (int s = 0; s < G::NST; ++s) {
      int total = 0, last = 0;
      for (int b = 0; b < PLAN_MAX; ++b)
        for (int j = 0; j < G::NST; ++j) if (MINE::at(j) == b) { total += G::nW; if (j == s) last = total; }
      for (int t = -1; t < s; ++t) {
        for (int j = 0; j < G::NST; ++j) {
          if (xstep[j] == t) { total += G::nX; if (j == s) last = total; }
          if (t < 0 && wstep[j] == t) { total += G::nW; if (j == s) last = total; }
        }
        if (t >= 0) {
          for (int j = 0; j < G::NST; ++j) if (wstep[j] == t) { total += G::nW; if (j == s) last = total; }
          for (int j = 0; j < GN::NST; ++j) if (NEXT::at(j) == t) total += GN::nW;
        }
      }
      waitn[s] = total - last > 63 ? 63 : total - last;
    }
  }
  constexpr bool ok() const {
    for (int j = 0; j < G::NST; ++j) if (xstep[j] >= j || xstep[j] < -1 || wstep[j] >= j || wstep[j] == -2) return false;
    return true;
  }
};
template <class G, class MINE, class GN, class NEXT> struct PlanOf { static constexpr Plan<G, MINE, GN, NEXT> v{}; };
template <class G, class MINE, class GN, class NEXT, class FX, class FW, class FN>
__device__ __forceinline__ void l_run(FX issueX, FW issueW, FN issueNext) {
  using PL = PlanOf<G, MINE, GN, NEXT>;
  static_assert(PL::v.ok(), "a stage would be waited for before it is issued");
  static_for<0, G::NST>([&](auto j) {
    if constexpr (PL::v.xstep[j.v] == -1) issueX(j);
    if constexpr (PL::v.wstep[j.v] == -1) issueW(j);
  });
  static_for<0, G::NST>([&](auto s) {
    WAIT_V(PL::v.waitn[s.v]);
    bar();
    static_for<0, G::NST>([&](auto j) { if constexpr (PL::v.xstep[j.v] == s.v) issueX(j); });
    static_for<0, G::NST>([&](auto j) { if constexpr (PL::v.wstep[j.v] == s.v) issueW(j); });
    static_for<0, GN::NST>([&](auto j) { if constexpr (NEXT::at(j.v) == s.v) issueNext(j); });
  });
  bar();
  static_for<0, GN::NST>([&](auto j) { if constexpr (NEXT::at(j.v) == G::NST) issueNext(j); });
}
template <class G> constexpr int wslot(int j) { return (G::OW + j) % G::NW; }

__device__ __forceinline__ bf16x8 ld_b128(lds_t p) { return *(LAS bf16x8*)p; }
__device__ __forceinline__ bf16x8 ld_tr(lds_t p0, lds_t p1) {
  s16x4 a = __builtin_amdgcn_ds_read_tr16_b64_v4i16((LAS s16x4*)p0);
  s16x4 b = __builtin_amdgcn_ds_read_tr16_b64_v4i16((LAS s16x4*)p1);
  s16x8 f = __builtin_shufflevector(a, b, 0, 1, 2, 3, 4, 5, 6, 7);
  return __builtin_bit_cast(bf16x8, f);
}
__device__ __forceinline__ f32x16 mfma(bf16x8 a, bf16x8 b, f32x16 c) { return __builtin_amdgcn_mfma_f32_32x32x16_bf16(a, b, c, 0, 0, 0); }
__device__ __forceinline__ unsigned pk2(float a, float b) { f32x2 v = {a, b}; return __builtin_bit_cast(unsigned, __builtin_convertvector(v, bf16x2)); }
__device__ __forceinline__ float bflo(unsigned u) { return __uint_as_float(u << 16); }
__device__ __forceinline__ float bfhi(unsigned u) { return __uint_as_float(u & 0xffff0000u); }
__device__ __forceinline__ float xhalf_sum(float v) {
  auto r = __builtin_amdgcn_permlane32_swap(__float_as_uint(v), __float_as_uint(v), false, false);
  return __uint_as_float(r[0]) + __uint_as_float(r[1]);
}
__device__ __forceinline__ unsigned km_read_base(int row, int h) { return row * 128 + ((h ^ ((row >> 1) & 7)) << 4); }
__device__ __forceinline__ int perm23(int i) { return (i & ~12) | ((i & 4) << 1) | ((i & 8) >> 1); }
__device__ __forceinline__ unsigned km_src(int lw, int lane, int pitch) {
  return (8 * lw + (lane >> 3)) * pitch + (((lane & 7) ^ (((lw & 1) << 2) | (lane >> 4))) << 4);
}
__device__ __forceinline__ unsigned kn_src(int lw, int lane, int pitch) {
  return (8 * lw + (lane >> 3)) * pitch + (((lane & 7) ^ (((lane >> 4) & 1) << 2)) << 4);
}
__device__ __forceinline__ unsigned kn_read_base(int lane, int ft) {
  int h = lane >> 5, blk = (lane >> 4) & 1, q = (lane & 15) >> 2, p = lane & 3, pp = ((p & 1) << 1) | (p >> 1);
  int c = 4 * ft + 2 * blk + (pp >> 1);
  return (8 * h + q) * 128 + ((c ^ (((q >> 1) & 1) << 2)) << 4) + 8 * (pp & 1);
}

#define SPAN_BEGIN()
#define SPAN_MAIN()
#define SPAN_END()
#define STAMP(i)
#define STAMP_ARG
#define STAMP_PASS(ph)
#define STAMP_PTR(ph)

struct Ptrs {
  unsigned long long* stamps;
  const u16 *x0, *sre, *sim, *dre, *dim, *wg, *wu, *wd;
  u16 *out, *xn, *pq, *xa, *hh;
  float* stats;
  unsigned *csf, *gran, *mbox, *tmo;
};
constexpr size_t XN_L = (size_t)SEQ * DM, PQ_L = (size_t)SEQ * 1024, HH_L = (size_t)SEQ * FF, ST_L = (size_t)16 * SEQ * 2;

__device__ __forceinline__ void signal(unsigned* mbox, int seam, int consumer, int slot, bool active) {
  if (active) __hip_atomic_store((gu32*)mbox + ((size_t)seam * 256 + consumer) * 32 + slot, 1u, RLX_AGENT);
}
__device__ __forceinline__ void await(unsigned* mbox, unsigned* tmo, int seam, int me, int nprod, int lane) {
  gu32* q = (gu32*)mbox + ((size_t)seam * 256 + me) * 32 + (lane < nprod ? lane : 0);
  for (unsigned spins = 0;; ++spins) {
    unsigned x = __hip_atomic_load(q, RLX_AGENT);
    if (__all(x == 1u)) break;
    if (spins > SPIN_CAP) { if (lane == 0) __hip_atomic_store((gu32*)tmo, 1u, RLX_AGENT); break; }
    __builtin_amdgcn_s_sleep(1);
  }
}

struct PLoader {
  rsrc_t rre, rim; unsigned vw, vx; lds_t lds; int lw, k0;
  __device__ __forceinline__ PLoader(lds_t lds_, const Ptrs& p, int tb, int et, int lw_, int lane, int rot) : lds(lds_), lw(lw_), k0((rot * et) & 31) {
    rre = mk_rsrc(p.sre + (size_t)tb * 64 * SEQ, 64 * SEQ * 2); rim = mk_rsrc(p.sim + (size_t)tb * 64 * SEQ, 64 * SEQ * 2);
    vw = km_src(lw, lane, SEQ * 2);
    vx = (lane >> 5) * 512 + (((lane & 31) ^ (4 * ((2 * lw + (lane >> 5)) & 3))) << 4);
  }
  template <class S> __device__ __forceinline__ void W(S s) {
    const int ks = (s.v + k0) & 31;
    static_for<0, 4>([&](auto n) { dma16<0>(n.v < 2 ? rre : rim, lds + wslot<GeoP>(s.v) * 16384 + (lw + 4 * n.v) * 1024, vw, ks * 128 + (n.v & 1) * 32 * SEQ * 2); });
  }
  template <class S> __device__ __forceinline__ void X(S s, rsrc_t rx) {
    const int ks = (s.v + k0) & 31;
    static_for<0, 2>([&](auto n) { dma16<SC1>(rx, lds + XB + (s.v % 8) * 8192 + n.v * 4096 + lw * 1024, vx, (2 * ks + n.v) * 64 * 512); });
  }
  __device__ __forceinline__ rsrc_t xr(const u16* xn, int et) { return mk_rsrc((const char*)xn + (8 * et + 2 * lw) * 512, SEQ * DM * 2 - (8 * et + 2 * lw) * 512); }
};
struct YLoader {
  rsrc_t rre, rim; unsigned vw, vx; lds_t lds; int lw;
  __device__ __forceinline__ YLoader(lds_t lds_, const Ptrs& p, int et, int lw_, int lane) : lds(lds_), lw(lw_) {
    rre = mk_rsrc(p.dre + (size_t)et * 64 * DM, 64 * DM * 2); rim = mk_rsrc(p.dim + (size_t)et * 64 * DM, 64 * DM * 2);
    vw = km_src(lw, lane, DM * 2); vx = lane * 16;
  }
  template <class S> __device__ __forceinline__ void W(S s) {
    static_for<0, 4>([&](auto i) {
      constexpr int kb = i.v >> 1, n = i.v & 1;
      dma16<0>(s.v < 4 ? rre : rim, lds + wslot<GeoY>(s.v) * 16384 + kb * 8192 + (lw + 4 * n) * 1024, vw, (2 * (s.v & 3) + kb) * 128 + n * 32 * DM * 2);
    });
  }
  template <class S> __device__ __forceinline__ void X(S s, rsrc_t rx) {
    static_for<0, 4>([&](auto i) {
      constexpr int blk = i.v >> 1, n = i.v & 1;
      dma16<SC1>(rx, lds + XB + (s.v % 4) * 16384 + blk * 8192 + n * 1024 + lw * 2048, vx, blk * 128 * 512 + (16 * s.v + 2 * n) * 512);
    });
  }
  __device__ __forceinline__ rsrc_t xr(const u16* pq, int tb) { return mk_rsrc((const char*)pq + ((size_t)2 * tb * 128 + 4 * lw) * 512, 2 * 128 * 512 - 4 * lw * 512); }
};
constexpr int C_WS = 28672;
struct CLoader {
  const char* wsrc[7]; unsigned vx; lds_t lds; int lw;
  __device__ __forceinline__ CLoader(lds_t lds_, const Ptrs& p, int fc, int lw_, int lane) : lds(lds_), lw(lw_) {
    vx = lane * 16;
#pragma unroll
    for (int n = 0; n < 7; ++n) {
      int idx = 64 * n + lane, kr = idx / 28, c = idx - kr * 28;
      const u16* base = c < 14 ? p.wg : p.wu;
      int cc = c < 14 ? c : c - 14;
      wsrc[n] = (const char*)(base + (size_t)(16 * lw + kr) * FF + 112 * fc + 8 * cc);
    }
  }
  template <class S> __device__ __forceinline__ void W(S s) {
    static_for<0, 7>([&](auto n) {
      __builtin_amdgcn_global_load_lds((const unsigned*)(wsrc[n.v] + (size_t)s.v * 64 * FF * 2), (LAS unsigned*)(lds + wslot<GeoC>(s.v) * C_WS + lw * 7168 + n.v * 1024), 16, 0, 0);
    });
  }
  template <class S> __device__ __forceinline__ void X(S s, rsrc_t rx) {
    static_for<0, 4>([&](auto n) { dma16<SC1>(rx, lds + XB + (s.v % 4) * 16384 + lw * 4096 + n.v * 1024, vx, (8 * s.v + 2 * n.v) * 512); });
  }
  __device__ __forceinline__ rsrc_t xr(const u16* xa, int rb) { return mk_rsrc((const char*)xa + (size_t)(4 * rb + lw) * 64 * 512, 64 * 512); }
};
struct DLoader {
  rsrc_t rw; unsigned vw, vx; lds_t lds; int lw, k0;
  __device__ __forceinline__ DLoader(lds_t lds_, const Ptrs& p, int tb, int et, int lw_, int lane) : lds(lds_), lw(lw_), k0(ROTD ? (tb * 3) % 14 : 0) {
    rw = mk_rsrc(p.wd + et * 64, FF * DM * 2 - et * 128);
    vw = kn_src(lw, lane, DM * 2); vx = lane * 16;
  }
  template <class S> __device__ __forceinline__ void W(S s) {
    const int ks = s.v + k0 < 14 ? s.v + k0 : s.v + k0 - 14;
    static_for<0, 4>([&](auto i) {
      constexpr int kb = i.v >> 1, n = i.v & 1;
      dma16<0>(rw, lds + wslot<GeoD>(s.v) * 16384 + kb * 8192 + (lw + 4 * n) * 1024, vw, ((2 * ks + kb) * 64 + 32 * n) * DM * 2);
    });
  }
  template <class S> __device__ __forceinline__ void X(S s, rsrc_t rx) {
    const int ks = s.v + k0 < 14 ? s.v + k0 : s.v + k0 - 14;
    static_for<0, 4>([&](auto i) {
      constexpr int blk = i.v >> 1, n = i.v & 1;
      dma16<SC1>(rx, lds + XB + (s.v % 4) * 16384 + blk * 8192 + n * 1024 + lw * 2048, vx, blk * 224 * 512 + (16 * ks + 2 * n) * 512);
    });
  }
  __device__ __forceinline__ rsrc_t xr(const u16* hh, int tb) { return mk_rsrc((const char*)hh + ((size_t)2 * tb * 224 + 4 * lw) * 512, 2 * 224 * 512 - 4 * lw * 512); }
};
template <class G, class MINE, class LD> __device__ __forceinline__ void l_seam(LD& L) {
  bar(); bar();
  static_for<0, G::NST>([&](auto j) { if constexpr (MINE::at(j.v) == GAP) L.W(j); });
  bar();
}
template <class G, class MINE, class GN, class NEXT, class LD, class LN>
__device__ __forceinline__ void l_phase(LD& L, rsrc_t rx, LN& N) {
  l_run<G, MINE, GN, NEXT>([&](auto s) { L.X(s, rx); }, [&](auto s) { L.W(s); }, [&](auto s) { N.W(s); });
}

__device__ __forceinline__ void p_compute(lds_t lds, u16* pq, int tb, int et, int w, int lane, const char* t0, const char* t1, bool touch, unsigned& dummy STAMP_ARG) {
  const int r = lane & 31, h = lane >> 5;
  const unsigned a0 = km_read_base(32 * w + r, h);
  const int bl = (lane >> 4) & 1, q4 = (lane & 15) >> 2, pp = ((lane & 1) << 1) | ((lane >> 1) & 1), cc0 = 2 * bl + (pp >> 1);
  const unsigned f0 = cc0 * 512 + (((8 * h + q4) ^ (4 * cc0)) << 4) + 8 * (pp & 1), f1 = f0 ^ 64;
  f32x16 acc0 = {}, acc1 = {};
  unsigned wo = GeoP::OW * 16384, xo = XB;
  struct Fr { bf16x8 tok[4], fa[4], fb[4]; };
  Fr A, B;
  auto rdt = [&](Fr& f, int k) { f.tok[k] = ld_b128(lds + wo + (a0 ^ (k << 5))); };
  auto rda = [&](Fr& f, int k) { lds_t xb = lds + xo + (k >> 1) * 4096 + (k & 1) * 256; f.fa[k] = ld_tr(xb + f0, xb + f1); };
  auto rdb = [&](Fr& f, int k) { lds_t xb = lds + xo + (k >> 1) * 4096 + (k & 1) * 256 + 2048; f.fb[k] = ld_tr(xb + f0, xb + f1); };
  auto adv = [&]() {
    wo = wo + 16384 == 6 * 16384 ? 0 : wo + 16384;
    xo = xo + 8192 == LDS_BYTES ? XB : xo + 8192;
  };
  auto step = [&](Fr& nx, const Fr& cu) {
    cbar();
#pragma unroll
    for (int k = 0; k < 4; ++k) {
      rdt(nx, k); rda(nx, k); SCHED();
      acc0 = mfma(cu.fa[k], cu.tok[k], acc0); SCHED();
      rdb(nx, k); SCHED();
      acc1 = mfma(cu.fb[k], cu.tok[k], acc1); SCHED();
    }
    adv();
  };
  cbar();
  STAMP(1);
#pragma unroll
  for (int k = 0; k < 4; ++k) { rdt(A, k); rda(A, k); rdb(A, k); }
  adv();
#pragma unroll 1
  for (int s = 1; s < 31; s += 2) {
    step(B, A);
    if (touch && s == TS) {
      asm volatile("global_load_dword %0, %1, off" : "+v"(dummy) : "v"(t0) : "memory");
      asm volatile("global_load_dword %0, %1, off" : "+v"(dummy) : "v"(t1) : "memory");
    }
    step(A, B);
  }
  step(B, A);
  cbar();
  STAMP(2);
#pragma unroll
  for (int k = 0; k < 4; ++k) { acc0 = mfma(B.fa[k], B.tok[k], acc0); acc1 = mfma(B.fb[k], B.tok[k], acc1); }
  const float sg = w >= 2 ? -1.f : 1.f;
  const rsrc_t ro = mk_rsrc((char*)pq + ((size_t)(2 * tb + (w & 1)) * 128 + (w >> 1) * 64 + 8 * et) * 512, 4096);
#pragma unroll
  for (int ft = 0; ft < 2; ++ft)
#pragma unroll
    for (int q = 0; q < 2; ++q) {
      const f32x16& a = ft ? acc1 : acc0;
      u32x4 v = {pk2(sg * a[8 * q], sg * a[8 * q + 1]), pk2(sg * a[8 * q + 2], sg * a[8 * q + 3]), pk2(sg * a[8 * q + 4], sg * a[8 * q + 5]), pk2(sg * a[8 * q + 6], sg * a[8 * q + 7])};
      __builtin_amdgcn_raw_buffer_store_b128(v, ro, lane * 16 + ft * 2048 + q * 1024, 0, SC1);
    }
}

__device__ __forceinline__ void add_res(const f32x16& acc, u32x4& r0, u32x4& r1) {
#pragma unroll
  for (int i = 0; i < 4; ++i) {
    r0[i] = pk2(acc[2 * i] + bflo(r0[i]), acc[2 * i + 1] + bfhi(r0[i]));
    r1[i] = pk2(acc[8 + 2 * i] + bflo(r1[i]), acc[8 + 2 * i + 1] + bfhi(r1[i]));
  }
}
__device__ __forceinline__ void store_rowmajor(u16* xo, const u32x4& o0, const u32x4& o1, int row0, int col0, int lane) {
  const rsrc_t ro = mk_rsrc(xo + (size_t)row0 * DM + col0, 32 * DM * 2);
  const unsigned vo = (lane & 31) * DM * 2 + (lane >> 5) * 16;
  __builtin_amdgcn_raw_buffer_store_b128(o0, ro, vo, 0, 0);
  __builtin_amdgcn_raw_buffer_store_b128(o1, ro, vo + 32, 0, 0);
}
__device__ __forceinline__ void store_blocked(u16* xo, const u32x4& o0, const u32x4& o1, int row0, int col0, int lane) {
  const rsrc_t ro = mk_rsrc((char*)xo + ((size_t)(row0 >> 5) * 64 + (col0 >> 3)) * 512, 2048);
  __builtin_amdgcn_raw_buffer_store_b128(o0, ro, lane * 16, 0, SC1);
  __builtin_amdgcn_raw_buffer_store_b128(o1, ro, lane * 16 + 1024, 0, SC1);
}
__device__ __forceinline__ void part_stats(const u32x4& o0, const u32x4& o1, float (&v)[16], float& m, float& m2) {
  float s0 = 0.f, s1 = 0.f;
#pragma unroll
  for (int i = 0; i < 4; ++i) { v[2 * i] = bflo(o0[i]); v[2 * i + 1] = bfhi(o0[i]); v[8 + 2 * i] = bflo(o1[i]); v[8 + 2 * i + 1] = bfhi(o1[i]); }
#pragma unroll
  for (int i = 0; i < 8; ++i) { s0 += v[i]; s1 += v[8 + i]; }
  m = xhalf_sum(s0 + s1) * (1.f / 32.f);
  float q0 = 0.f, q1 = 0.f;
#pragma unroll
  for (int i = 0; i < 8; ++i) { float d = v[i] - m, e = v[8 + i] - m; q0 += d * d; q1 += e * e; }
  m2 = xhalf_sum(q0 + q1);
}
__device__ __forceinline__ void store_stats(float* stats, float m, float m2, int row0, int col0, int lane) {
  const rsrc_t rs = mk_rsrc(stats + ((size_t)(col0 >> 5) * SEQ + row0) * 2, 32 * 8);
  if (lane < 32) { u32x2 sv = {__float_as_uint(m), __float_as_uint(m2)}; __builtin_amdgcn_raw_buffer_store_b64(sv, rs, lane * 8, 0, SC1); }
}
__device__ __forceinline__ void load_stats(const float* stats, int row0, int lane, u32x2 (&t)[8]) {
  gu64* q = (gu64*)stats + row0 + (lane & 31) + (size_t)(8 * (lane >> 5)) * SEQ;
#pragma unroll
  for (int i = 0; i < 8; ++i) { unsigned long long x = __hip_atomic_load(q + i * SEQ, RLX_AGENT); t[i] = u32x2{(unsigned)x, (unsigned)(x >> 32)}; }
}
__device__ __forceinline__ void combine_stats(const u32x2 (&t)[8], float& mean, float& rstd) {
  float ms = 0.f, m2 = 0.f;
#pragma unroll
  for (int i = 0; i < 8; ++i) { ms += __uint_as_float(t[i][0]); m2 += __uint_as_float(t[i][1]); }
  mean = xhalf_sum(ms) * (1.f / 16.f);
#pragma unroll
  for (int i = 0; i < 8; ++i) { float d = __uint_as_float(t[i][0]) - mean; m2 += 32.f * d * d; }
  rstd = rsqrtf(xhalf_sum(m2) * (1.f / 512.f) + 1e-5f);
}

template <class G, class FF_> __device__ __forceinline__ f32x16 tile64_loop(lds_t lds, unsigned a0, FF_ feat, int w, int lane STAMP_ARG) {
  f32x16 acc = {};
  unsigned wo = G::OW * 16384, xo = XB;
  struct Fr { bf16x8 tok[8], f[8]; };
  Fr A, B;
  auto rd = [&](Fr& fr, int k) {
    fr.tok[k] = ld_b128(lds + xo + a0 + k * 1024);
    fr.f[k] = feat(lds + wo, k);
  };
  auto adv = [&]() {
    wo = wo + 16384 == 6 * 16384 ? 0 : wo + 16384;
    xo = xo + 16384 == LDS_BYTES ? XB : xo + 16384;
  };
  auto step = [&](Fr& nx, const Fr& cu) {
    cbar();
#pragma unroll
    for (int k = 0; k < 8; ++k) { rd(nx, k); SCHED(); acc = mfma(cu.f[k], cu.tok[k], acc); SCHED(); }
    adv();
  };
  cbar();
  STAMP(1);
#pragma unroll
  for (int k = 0; k < 8; ++k) rd(A, k);
  adv();
#pragma unroll 1
  for (int s = 1; s < G::NST - 1; s += 2) { step(B, A); step(A, B); }
  step(B, A);
  cbar();
  STAMP(2);
#pragma unroll
  for (int k = 0; k < 8; ++k) acc = mfma(B.f[k], B.tok[k], acc);
  return acc;
}

__device__ __forceinline__ void c_compute(lds_t lds, const float* stats, const unsigned* csf, u16* hh, int rb, int fc, int w, int lane STAMP_ARG) {
  const int r = lane & 31, h = lane >> 5;
  const unsigned a0 = w * 4096 + lane * 16;
  const int blk = (lane >> 4) & 1, q = (lane & 15) >> 2, pp = ((lane & 1) << 1) | ((lane >> 1) & 1);
  const unsigned f0 = (8 * h + q) * 448 + blk * 224 + 8 * pp;
  f32x16 acc[7];
#pragma unroll
  for (int j = 0; j < 7; ++j) acc[j] = f32x16{};
  unsigned wo = GeoC::OW * C_WS, xo = XB;
  struct Fr { bf16x8 tok, f[7]; };
  Fr A, B;
  auto rdt = [&](Fr& fr, int k) { fr.tok = ld_b128(lds + xo + a0 + k * 1024); };
  auto rdf = [&](Fr& fr, int k, int j) { lds_t wb = lds + wo + f0; fr.f[j] = ld_tr(wb + 16 * k * 448 + 32 * j, wb + (16 * k + 4) * 448 + 32 * j); };
  auto grp = [&](Fr& nx, const Fr& cu, int k, bool rdn) {
#pragma unroll
    for (int j = 0; j < 7; ++j) {
      if (rdn) { if (j == 0) rdt(nx, k); rdf(nx, k, j); }
      SCHED();
      acc[j] = mfma(cu.f[j], cu.tok, acc[j]);
      SCHED();
    }
  };
  auto adv = [&]() {
    wo = wo + C_WS == 3 * C_WS ? 0 : wo + C_WS;
    xo = xo + 16384 == LDS_BYTES ? XB : xo + 16384;
  };
  cbar();
  STAMP(1);
  u32x2 st[8];
  load_stats(stats, 128 * rb + 32 * w, lane, st);
  unsigned long long cs[7];
#pragma unroll
  for (int j = 0; j < 7; ++j) cs[j] = __hip_atomic_load((gu64*)csf + ((size_t)fc * 32 + r) * 8 + j, __ATOMIC_RELAXED, __HIP_MEMORY_SCOPE_WORKGROUP);
  rdt(A, 0);
#pragma unroll
  for (int j = 0; j < 7; ++j) rdf(A, 0, j);
  auto stage = [&]() {
    grp(B, A, 1, true);
    grp(A, B, 2, true);
    grp(B, A, 3, true);
    adv();
    cbar();
    grp(A, B, 0, true);
  };
  stage();
  float mean, rstd;
  combine_stats(st, mean, rstd);
  {
    const unsigned mh = pk2(-mean, 0.f) & 0xffffu;
    const unsigned ml = pk2(-mean - bflo(mh), 0.f) & 0xffffu;
    u32x4 bt = {h ? 0u : (mh | (ml << 16)), h ? 0u : mh, 0u, 0u};
#pragma unroll
    for (int j = 0; j < 7; ++j) {
      u32x4 at = {h ? 0u : (unsigned)cs[j], h ? 0u : (unsigned)(cs[j] >> 32), 0u, 0u};
      acc[j] = mfma(__builtin_bit_cast(bf16x8, at), __builtin_bit_cast(bf16x8, bt), acc[j]);
    }
  }
  SCHED();
#pragma unroll 1
  for (int s = 1; s < 7; ++s) stage();
  grp(B, A, 1, true);
  grp(A, B, 2, true);
  grp(B, A, 3, true);
  cbar();
  STAMP(2);
  grp(A, B, 0, false);
  const rsrc_t ro = mk_rsrc((char*)hh + ((size_t)(4 * rb + w) * 224 + 14 * fc) * 512, 7168);
  const float ng = -1.4426950408889634f * rstd;
#pragma unroll
  for (int j = 0; j < 7; ++j) {
    float o[8];
#pragma unroll
    for (int i = 0; i < 8; i += 2) {
      f32x2 g = {acc[j][i], acc[j][i + 1]}, u = {acc[j][8 + i], acc[j][9 + i]};
      g *= ng; u *= rstd;
      f32x2 e = {__builtin_amdgcn_exp2f(g[0]), __builtin_amdgcn_exp2f(g[1])};
      e += 1.f;
      f32x2 sg = {__builtin_amdgcn_rcpf(e[0]), __builtin_amdgcn_rcpf(e[1])};
      u *= sg;
      o[i] = u[0]; o[i + 1] = u[1];
    }
    u32x4 v = {pk2(o[0], o[1]), pk2(o[2], o[3]), pk2(o[4], o[5]), pk2(o[6], o[7])};
    __builtin_amdgcn_raw_buffer_store_b128(v, ro, lane * 16 + 1024 * j, 0, SC1);
    SCHED();
  }
}

__global__ __launch_bounds__(512, 2) __attribute__((amdgpu_waves_per_eu(2, 2))) void k_main(Ptrs p) {
  extern __shared__ __attribute__((aligned(16))) char smem[];
  lds_t lds = (lds_t)smem;
  const int lane = threadIdx.x & 63, wid = __builtin_amdgcn_readfirstlane(threadIdx.x >> 6);
  const int xcd = blockIdx.x & 7, j = blockIdx.x >> 3;
  const int tb = xcd * 4 + (j & 3), et = j >> 2;
  const int rb = (xcd & 1) * 8 + (j & 7), fc = (xcd >> 1) * 4 + (j >> 3);
  SPAN_MAIN();
  if (wid >= 4) {
    {
      PLoader L(lds, p, fresh_s(tb), fresh_s(et), fresh_s(wid - 4), fresh_v(lane), ROT);
      YLoader N(lds, p, fresh_s(et), fresh_s(wid - 4), fresh_v(lane));
      l_phase<GeoP, EarlyNone, GeoY, EarlyY>(L, L.xr(p.xn, et), N);
    }
#pragma unroll 1
    for (int l = 0; l < 2; ++l) {
      {
        YLoader L(lds, p, fresh_s(et), fresh_s(wid - 4), fresh_v(lane));
        CLoader N(lds, p, fresh_s(fc), fresh_s(wid - 4), fresh_v(lane));
        const rsrc_t rx = L.xr(p.pq + l * PQ_L, fresh_s(tb));
        l_seam<GeoY, EarlyY>(L);
        l_phase<GeoY, EarlyY, GeoC, EarlyC>(L, rx, N);
      }
      {
        CLoader L(lds, p, fresh_s(fc), fresh_s(wid - 4), fresh_v(lane));
        DLoader N(lds, p, fresh_s(tb), fresh_s(et), fresh_s(wid - 4), fresh_v(lane));
        const rsrc_t rx = L.xr(p.xa + l * XN_L, fresh_s(rb));
        l_seam<GeoC, EarlyC>(L);
        l_phase<GeoC, EarlyC, GeoD, EarlyD>(L, rx, N);
      }
      if (l == 0) {
        {
          DLoader L(lds, p, fresh_s(tb), fresh_s(et), fresh_s(wid - 4), fresh_v(lane));
          PLoader N(lds, p, fresh_s(tb), fresh_s(et), fresh_s(wid - 4), fresh_v(lane), ROT2);
          const rsrc_t rx = L.xr(p.hh, fresh_s(tb));
          l_seam<GeoD, EarlyD>(L);
          l_phase<GeoD, EarlyD, GeoP, EarlyP>(L, rx, N);
        }
        PLoader L(lds, p, fresh_s(tb), fresh_s(et), fresh_s(wid - 4), fresh_v(lane), ROT2);
        YLoader N(lds, p, fresh_s(et), fresh_s(wid - 4), fresh_v(lane));
        const rsrc_t rx = L.xr(p.xn + XN_L, fresh_s(et));
        l_seam<GeoP, EarlyP>(L);
        l_phase<GeoP, EarlyP, GeoY, EarlyY>(L, rx, N);
      } else {
        DLoader L(lds, p, fresh_s(tb), fresh_s(et), fresh_s(wid - 4), fresh_v(lane));
        const rsrc_t rx = L.xr(p.hh + HH_L, fresh_s(tb));
        l_seam<GeoD, EarlyD>(L);
        l_phase<GeoD, EarlyD, GeoP, EarlyNone>(L, rx, L);
      }
    }
  } else {
    const int w = wid, tw = w & 1, fw = w >> 1;
    const int row0 = 64 * tb + 32 * tw, col0 = 64 * et + 32 * fw;
    unsigned dummy = 0;
    u32x4 r0, r1;
    {
      const int r = lane & 31, h = lane >> 5;
      const rsrc_t rr = mk_rsrc(p.x0 + (size_t)row0 * DM + col0, 32 * DM * 2);
      r0 = __builtin_amdgcn_raw_buffer_load_b128(rr, r * DM * 2 + h * 16, 0, 0);
      r1 = __builtin_amdgcn_raw_buffer_load_b128(rr, r * DM * 2 + h * 16 + 32, 0, 0);
    }
#pragma unroll 1
    for (int l = 0; l < 2; ++l) {
      const int sb = 4 * l;
      {
        STAMP_PTR(4 * l + 0);
        STAMP(0);
        const int ln = fresh_v(lane), li = blockIdx.x * 64 + ln, lj = blockIdx.x * 32 + (ln & 31);
        const char* t0 = li < 14336 ? (const char*)p.wd + (size_t)li * 128 : (const char*)p.wd;
        const char* t1 = (ln < 32 ? (const char*)p.dre : (const char*)p.dim) + (size_t)(lj & 4095) * 128;
        p_compute(lds, p.pq + l * PQ_L, fresh_s(tb), fresh_s(et), fresh_s(w), fresh_v(lane), t0, t1, !KW && l == 0 && w == 0 && blockIdx.x < 224, dummy STAMP_PASS(4 * l + 0));
        STAMP(5); WAIT_V(0); STAMP(6); bar();
        asm volatile("" :: "v"(dummy));
        if (w == 0) signal(p.mbox, sb + 0, tb * 8 + (lane & 7), et, lane < 8);
        bar();
        if (w == 0) await(p.mbox, p.tmo, sb + 0, tb * 8 + et, 8, lane);
        bar(); STAMP(7);
      }
      {
        STAMP_PTR(4 * l + 1);
        STAMP(0);
        const int ln = fresh_v(lane), r = ln & 31, h = ln >> 5;
        const unsigned a0 = tw * 8192 + ln * 16, b0 = km_read_base(32 * fw + perm23(r), h);
        f32x16 acc = tile64_loop<GeoY>(lds, a0, [&](lds_t wb, int k) { return ld_b128(wb + (k >> 2) * 8192 + (b0 ^ ((k & 3) << 5))); }, w, lane STAMP_PASS(4 * l + 1));
        add_res(acc, r0, r1);
        store_blocked(p.xa + l * XN_L, r0, r1, row0, col0, fresh_v(lane));
        float v[16], m, m2;
        part_stats(r0, r1, v, m, m2);
        store_stats(p.stats + l * ST_L, m, m2, row0, col0, fresh_v(lane));
        STAMP(5); WAIT_V(0); STAMP(6); bar();
        if (w == 0) signal(p.mbox, sb + 1, (tb >> 1) * 16 + (lane & 15), (tb & 1) * 8 + et, lane < 16);
        bar();
        if (w == 0) await(p.mbox, p.tmo, sb + 1, rb * 16 + fc, 16, lane);
        bar(); STAMP(7);
      }
      {
        STAMP_PTR(4 * l + 2);
        STAMP(0);
        c_compute(lds, p.stats + l * ST_L, p.csf, p.hh + l * HH_L, fresh_s(rb), fresh_s(fc), fresh_s(w), fresh_v(lane) STAMP_PASS(4 * l + 2));
        STAMP(5); WAIT_V(0); STAMP(6); bar();
        if (w == 0) signal(p.mbox, sb + 2, (2 * rb + ((lane >> 3) & 1)) * 8 + (lane & 7), fc, lane < 16);
        bar();
        if (w == 0) await(p.mbox, p.tmo, sb + 2, tb * 8 + et, 16, lane);
        bar(); STAMP(7);
      }
      {
        STAMP_PTR(4 * l + 3);
        STAMP(0);
        const int ln = fresh_v(lane);
        const unsigned a0 = tw * 8192 + ln * 16, f0 = kn_read_base(ln, fw);
        f32x16 acc = tile64_loop<GeoD>(lds, a0, [&](lds_t wb, int k) { return ld_tr(wb + f0 + k * 2048, wb + f0 + k * 2048 + 512); }, w, lane STAMP_PASS(4 * l + 3));
        add_res(acc, r0, r1);
        if (l == 1) {
          store_rowmajor(p.out, r0, r1, row0, col0, fresh_v(lane));
          if (w == 0) SPAN_END();
        } else {
          float v[16], m, m2;
          part_stats(r0, r1, v, m, m2);
          const int le = fresh_v(lane);
          store_stats((float*)p.gran, m, m2, row0, col0, le);
          STAMP(3);
          u32x2 st[8];
          for (unsigned spins = 0;; ++spins) {
            load_stats((const float*)p.gran, row0, le, st);
            bool ok = true;
#pragma unroll
            for (int i = 0; i < 8; ++i) ok = ok && st[i][1] != 0xffffffffu;
            if (__all(ok)) break;
            if (spins > SPIN_CAP) { if (lane == 0) __hip_atomic_store((gu32*)p.tmo, 2u, RLX_AGENT); break; }
            __builtin_amdgcn_s_sleep(1);
          }
          STAMP(4);
          float mean, rstd;
          combine_stats(st, mean, rstd);
          u32x4 n0, n1;
#pragma unroll
          for (int i = 0; i < 4; ++i) {
            n0[i] = pk2((v[2 * i] - mean) * rstd, (v[2 * i + 1] - mean) * rstd);
            n1[i] = pk2((v[8 + 2 * i] - mean) * rstd, (v[8 + 2 * i + 1] - mean) * rstd);
          }
          store_blocked(p.xn + XN_L, n0, n1, row0, col0, le);
          STAMP(5); WAIT_V(0); STAMP(6); bar();
          if (w == 0) signal(p.mbox, 3, (lane & 31) * 8 + et, tb, lane < 32);
          bar();
          if (w == 0) await(p.mbox, p.tmo, 3, tb * 8 + et, 32, lane);
          bar(); STAMP(7);
        }
      }
    }
  }
}

__device__ __forceinline__ float wave_sum(float v) {
#pragma unroll
  for (int o = 1; o < 64; o <<= 1) v += __shfl_xor(v, o);
  return v;
}
__global__ __launch_bounds__(512) void k_pre(Ptrs p) {
  const int lane = threadIdx.x & 63, wid = threadIdx.x >> 6;
  if (blockIdx.x < 256) {
    SPAN_BEGIN();
    const int row = blockIdx.x * 8 + wid;
    u32x4 t = *(const u32x4*)(p.x0 + (size_t)row * DM + lane * 8);
    const int gid = blockIdx.x * 512 + threadIdx.x;
    if (gid < 7 * 256 * 8) ((u32x4*)p.mbox)[gid] = u32x4{0u, 0u, 0u, 0u};
    if (gid < 16 * SEQ / 2) ((u32x4*)p.gran)[gid] = u32x4{~0u, ~0u, ~0u, ~0u};
    if (gid == 0) *p.tmo = 0u;
    float v[8], s = 0.f;
#pragma unroll
    for (int i = 0; i < 4; ++i) { v[2 * i] = bflo(t[i]); v[2 * i + 1] = bfhi(t[i]); s += v[2 * i] + v[2 * i + 1]; }
    const float m = wave_sum(s) * (1.f / 512.f);
    float m2 = 0.f;
#pragma unroll
    for (int i = 0; i < 8; ++i) { v[i] -= m; m2 += v[i] * v[i]; }
    const float rs = rsqrtf(wave_sum(m2) * (1.f / 512.f) + 1e-5f);
    u32x4 o = {pk2(v[0] * rs, v[1] * rs), pk2(v[2] * rs, v[3] * rs), pk2(v[4] * rs, v[5] * rs), pk2(v[6] * rs, v[7] * rs)};
    *(u32x4*)((char*)p.xn + ((size_t)(row >> 5) * 64 + lane) * 512 + (row & 31) * 16) = o;
  } else if (blockIdx.x >= 440) {
    const int li = (blockIdx.x - 440) * 512 + threadIdx.x;
    const char* a = li < 4096 ? (const char*)p.dre + (size_t)li * 128 : li < 8192 ? (const char*)p.dim + (size_t)(li - 4096) * 128 : (const char*)p.wd + (size_t)(li - 8192) * 128;
    unsigned d0;
    asm volatile("global_load_dword %0, %1, off\n\ts_waitcnt vmcnt(0)" : "=v"(d0) : "v"(a) : "memory");
  } else if (blockIdx.x >= 312) {
    const int q = blockIdx.x - 312, xcd = q & 7, i = q >> 3, tb = xcd * 4 + (i & 3);
    const int r = 32 * (i >> 2) + 4 * wid + (lane >> 4);
    const char* a = (const char*)(r < 64 ? p.sre : p.sim) + (size_t)(64 * tb + (r & 63)) * (SEQ * 2) + (lane & 15) * 128;
    unsigned d0, d1 = 0;
    asm volatile("global_load_dword %0, %1, off" : "=v"(d0) : "v"(a) : "memory");
    if (KT == 2) asm volatile("global_load_dword %0, %1, off offset:2048" : "=v"(d1) : "v"(a) : "memory");
    asm volatile("s_waitcnt vmcnt(0)" : "+v"(d0), "+v"(d1) :: "memory");
  } else {
    __shared__ float red[8][64];
    const int b = blockIdx.x - 256;
    const u16* w = (b < 28 ? p.wg : p.wu) + (b % 28) * 64 + (lane & 7) * 8;
    float a[8] = {};
#pragma unroll
    for (int i = 0; i < 8; ++i) {
      u32x4 t = *(const u32x4*)(w + (size_t)(i * 64 + wid * 8 + (lane >> 3)) * FF);
#pragma unroll
      for (int e = 0; e < 4; ++e) { a[2 * e] += bflo(t[e]); a[2 * e + 1] += bfhi(t[e]); }
    }
#pragma unroll
    for (int e = 0; e < 8; ++e) { a[e] += __shfl_xor(a[e], 8); a[e] += __shfl_xor(a[e], 16); a[e] += __shfl_xor(a[e], 32); }
    if (lane < 8) {
#pragma unroll
      for (int e = 0; e < 8; ++e) red[wid][lane * 8 + e] = a[e];
    }
    __syncthreads();
    if (threadIdx.x < 64) {
      float s = 0.f;
#pragma unroll
      for (int i = 0; i < 8; ++i) s += red[i][threadIdx.x];
      const int c = (b % 28) * 64 + threadIdx.x, fc = c / 112, rem = c - fc * 112, jj = rem >> 4, i = perm23(rem & 15) + (b < 28 ? 0 : 16);
      const unsigned hi = pk2(s, 0.f) & 0xffffu, lo = pk2(s - bflo(hi), 0.f) & 0xffffu;
      u32x2 o = {hi | (hi << 16), lo};
      *(u32x2*)(p.csf + ((size_t)fc * 32 + i) * 16 + 2 * jj) = o;
    }
  }
}
}

extern "C" void kernel_launch(void* const* d_in, const int* in_sizes, int n_in, void* d_out, int out_size, void* d_ws, size_t ws_size, hipStream_t stream) {
  Ptrs p;
  p.x0 = (const u16*)d_in[0]; p.sre = (const u16*)d_in[1]; p.sim = (const u16*)d_in[2]; p.dre = (const u16*)d_in[3]; p.dim = (const u16*)d_in[4];
  p.wg = (const u16*)d_in[6]; p.wu = (const u16*)d_in[7]; p.wd = (const u16*)d_in[8];
  char* ws = (char*)d_ws;
  const size_t MB = 1 << 20;
  p.out = (u16*)d_out;
  p.xn = (u16*)ws;
  p.pq = (u16*)(ws + 4 * MB);
  p.xa = (u16*)(ws + 12 * MB);
  p.hh = (u16*)(ws + 16 * MB);
  p.stats = (float*)(ws + 31 * MB);
  p.stamps = (unsigned long long*)(ws + 32 * MB);
  p.csf = (unsigned*)(ws + 33 * MB);
  p.gran = (unsigned*)(ws + 34 * MB);
  p.mbox = (unsigned*)(ws + 35 * MB);
  p.tmo = (unsigned*)(ws + 36 * MB);
  (void)hipFuncSetAttribute((const void*)k_main, hipFuncAttributeMaxDynamicSharedMemorySize, LDS_BYTES);
  k_pre<<<256 + 56 + (KT ? 128 : 0) + (KW ? 44 : 0), 512, 0, stream>>>(p);
  k_main<<<256, 512, LDS_BYTES, stream>>>(p);
}
```

```cpp
#include <hip/hip_runtime.h>
#include <stdint.h>

namespace {
typedef unsigned short u16;
typedef short s16x4 __attribute__((ext_vector_type(4)));
typedef short s16x8 __attribute__((ext_vector_type(8)));
typedef __bf16 bf16x8 __attribute__((ext_vector_type(8)));
typedef __bf16 bf16x2 __attribute__((ext_vector_type(2)));
typedef float f32x2 __attribute__((ext_vector_type(2)));
typedef float f32x4 __attribute__((ext_vector_type(4)));
typedef float f32x16 __attribute__((ext_vector_type(16)));
typedef unsigned u32x4 __attribute__((ext_vector_type(4)));
typedef unsigned u32x2 __attribute__((ext_vector_type(2)));
#define LAS __attribute__((address_space(3)))
typedef __amdgpu_buffer_rsrc_t rsrc_t;
typedef LAS char* lds_t;
typedef __attribute__((address_space(1))) unsigned gu32;
typedef __attribute__((address_space(1))) unsigned long long gu64;
#define RLX_AGENT __ATOMIC_RELAXED, __HIP_MEMORY_SCOPE_AGENT

constexpr int SEQ = 2048, DM = 512, FF = 1792;
constexpr int LDS_BYTES = 163840, XB = 98304;
constexpr int SC1 = 16;
constexpr unsigned SPIN_CAP = 1u << 18;

__device__ __forceinline__ rsrc_t mk_rsrc(const void* p, unsigned bytes) { return __builtin_amdgcn_make_buffer_rsrc((void*)p, 0, bytes, 0x00020000); }
template <int AUX> __device__ __forceinline__ void dma16(rsrc_t rs, lds_t dst, unsigned voff, unsigned soff) {
  __builtin_amdgcn_raw_ptr_buffer_load_lds(rs, (LAS void*)dst, 16, voff, soff, 0, AUX);
}
#define WAIT_V(n) asm volatile("s_waitcnt vmcnt(%0)" ::"n"(n) : "memory")
#define SCHED() __builtin_amdgcn_sched_barrier(0)
#define WAIT_L0() __builtin_amdgcn_s_waitcnt(0xC07F)
__device__ __forceinline__ void bar() { asm volatile("" ::: "memory"); __builtin_amdgcn_s_barrier(); asm volatile("" ::: "memory"); }
__device__ __forceinline__ void cbar() { WAIT_L0(); bar(); }
__device__ __forceinline__ int fresh_v(int x) { asm volatile("" : "+v"(x)); return x; }
__device__ __forceinline__ int fresh_s(int x) { asm volatile("" : "+s"(x)); return x; }

template <int I> struct IC { static constexpr int v = I; };
template <int A, int B, class F> __device__ __forceinline__ void static_for(F&& f) {
  if constexpr (A < B) { f(IC<A>{}); static_for<A + 1, B>(f); }
}

#define PXI 3
#define ROT 8
#define TS 17
#define KT 2
#define KW 0
#define TS4 0
#define ROT2 0
#define ROTD 1
struct GeoP { static constexpr int NST = 32, NX = 8, nX = 2, NW = 6, nW = 4, OW = 0; };
struct GeoY { static constexpr int NST = 8,  NX = 4, nX = 4, NW = 6, nW = 4, OW = 4; };
struct GeoC { static constexpr int NST = 8,  NX = 4, nX = 4, NW = 3, nW = 7, OW = 0; };
struct GeoD { static constexpr int NST = 14, NX = 4, nX = 4, NW = 6, nW = 4, OW = 4; };
constexpr int GAP = 39;
#define L6(a, b, c, d, e, f) {a, b, c, d, e, f}
#define L3(a, b, c) {a, b, c}
#define EY L6(29, 30, 31, GAP, 27, 28)
#define EC L3(4, 6, GAP)
#define ED L6(6, 7, 7, GAP, GAP, GAP)
#define EP L6(9, 10, 11, 12, 13, GAP)
struct EarlyNone { static constexpr int at(int) { return -1; } };
struct EarlyY { static constexpr int at(int j) { constexpr int t[6] = EY; return j < 6 ? t[j] : -1; } };
struct EarlyC { static constexpr int at(int j) { constexpr int t[3] = EC; return j < 3 ? t[j] : -1; } };
struct EarlyD { static constexpr int at(int j) { constexpr int t[6] = ED; return j < 6 ? t[j] : -1; } };
struct EarlyP { static constexpr int at(int j) { constexpr int t[6] = EP; return j < 6 ? t[j] : -1; } };

constexpr int PLAN_MAX = 40;
template <class G, class MINE, class GN, class NEXT> struct Plan {
  int xstep[PLAN_MAX], wstep[PLAN_MAX], waitn[PLAN_MAX];
  constexpr Plan() : xstep{}, wstep{}, waitn{} {
    int nx = 0;
    for (int j = 0; j < G::NST; ++j) { xstep[j] = -2; wstep[j] = MINE::at(j) >= 0 ? -3 : -2; }
    for (int s = -1; s < G::NST; ++s) {
      const int lx = s - 1 + G::NX < G::NX - 1 ? G::NX - 1 : s - 1 + G::NX, lw = s - 1 + G::NW < G::NW - 1 ? G::NW - 1 : s - 1 + G::NW;
      for (int c = 0; c < (s < 0 ? PXI : 2); ++c) if (nx < G::NST && nx <= lx) xstep[nx++] = s;
      int c = 0;
      for (int j = 0; j < G::NST; ++j)
        if (wstep[j] == -2 && j <= lw && (s < 0 ? (j < PXI) : c < 2)) { wstep[j] = s; ++c; }
    }
    for (int s = 0; s < G::NST; ++s) {
      int total = 0, last = 0;
      for (int b = 0; b < PLAN_MAX; ++b)
        for (int j = 0; j < G::NST; ++j) if (MINE::at(j) == b) { total += G::nW; if (j == s) last = total; }
      for (int t = -1; t < s; ++t) {
        for (int j = 0; j < G::NST; ++j) {
          if (xstep[j] == t) { total += G::nX; if (j == s) last = total; }
          if (t < 0 && wstep[j] == t) { total += G::nW; if (j == s) last = total; }
        }
        if (t >= 0) {
          for (int j = 0; j < G::NST; ++j) if (wstep[j] == t) { total += G::nW; if (j == s) last = total; }
          for (int j = 0; j < GN::NST; ++j) if (NEXT::at(j) == t) total += GN::nW;
        }
      }
      waitn[s] = total - last > 63 ? 63 : total - last;
    }
  }
  constexpr bool ok() const {
    for (int j = 0; j < G::NST; ++j) if (xstep[j] >= j || xstep[j] < -1 || wstep[j] >= j || wstep[j] == -2) return false;
    return true;
  }
};
template <class G, class MINE, class GN, class NEXT> struct PlanOf { static constexpr Plan<G, MINE, GN, NEXT> v{}; };
template <class G, class MINE, class GN, class NEXT, class FX, class FW, class FN>
__device__ __forceinline__ void l_run(FX issueX, FW issueW, FN issueNext) {
  using PL = PlanOf<G, MINE, GN, NEXT>;
  static_assert(PL::v.ok(), "a stage would be waited for before it is issued");
  static_for<0, G::NST>([&](auto j) {
    if constexpr (PL::v.xstep[j.v] == -1) issueX(j);
    if constexpr (PL::v.wstep[j.v] == -1) issueW(j);
  });
  static_for<0, G::NST>([&](auto s) {
    WAIT_V(PL::v.waitn[s.v]);
    bar();
    static_for<0, G::NST>([&](auto j) { if constexpr (PL::v.xstep[j.v] == s.v) issueX(j); });
    static_for<0, G::NST>([&](auto j) { if constexpr (PL::v.wstep[j.v] == s.v) issueW(j); });
    static_for<0, GN::NST>([&](auto j) { if constexpr (NEXT::at(j.v) == s.v) issueNext(j); });
  });
  bar();
  static_for<0, GN::NST>([&](auto j) { if constexpr (NEXT::at(j.v) == G::NST) issueNext(j); });
}
template <class G> constexpr int wslot(int j) { return (G::OW + j) % G::NW; }

__device__ __forceinline__ bf16x8 ld_b128(lds_t p) { return *(LAS bf16x8*)p; }
__device__ __forceinline__ bf16x8 ld_tr(lds_t p0, lds_t p1) {
  s16x4 a = __builtin_amdgcn_ds_read_tr16_b64_v4i16((LAS s16x4*)p0);
  s16x4 b = __builtin_amdgcn_ds_read_tr16_b64_v4i16((LAS s16x4*)p1);
  s16x8 f = __builtin_shufflevector(a, b, 0, 1, 2, 3, 4, 5, 6, 7);
  return __builtin_bit_cast(bf16x8, f);
}
__device__ __forceinline__ f32x16 mfma(bf16x8 a, bf16x8 b, f32x16 c) { return __builtin_amdgcn_mfma_f32_32x32x16_bf16(a, b, c, 0, 0, 0); }
__device__ __forceinline__ unsigned pk2(float a, float b) { f32x2 v = {a, b}; return __builtin_bit_cast(unsigned, __builtin_convertvector(v, bf16x2)); }
__device__ __forceinline__ float bflo(unsigned u) { return __uint_as_float(u << 16); }
__device__ __forceinline__ float bfhi(unsigned u) { return __uint_as_float(u & 0xffff0000u); }
__device__ __forceinline__ float xhalf_sum(float v) {
  auto r = __builtin_amdgcn_permlane32_swap(__float_as_uint(v), __float_as_uint(v), false, false);
  return __uint_as_float(r[0]) + __uint_as_float(r[1]);
}
__device__ __forceinline__ unsigned km_read_base(int row, int h) { return row * 128 + ((h ^ ((row >> 1) & 7)) << 4); }
__device__ __forceinline__ int perm23(int i) { return (i & ~12) | ((i & 4) << 1) | ((i & 8) >> 1); }
__device__ __forceinline__ unsigned km_src(int lw, int lane, int pitch) {
  return (8 * lw + (lane >> 3)) * pitch + (((lane & 7) ^ (((lw & 1) << 2) | (lane >> 4))) << 4);
}
__device__ __forceinline__ unsigned kn_src(int lw, int lane, int pitch) {
  return (8 * lw + (lane >> 3)) * pitch + (((lane & 7) ^ (((lane >> 4) & 1) << 2)) << 4);
}
__device__ __forceinline__ unsigned kn_read_base(int lane, int ft) {
  int h = lane >> 5, blk = (lane >> 4) & 1, q = (lane & 15) >> 2, p = lane & 3, pp = ((p & 1) << 1) | (p >> 1);
  int c = 4 * ft + 2 * blk + (pp >> 1);
  return (8 * h + q) * 128 + ((c ^ (((q >> 1) & 1) << 2)) << 4) + 8 * (pp & 1);
}

#define SPAN_BEGIN()
#define SPAN_MAIN()
#define SPAN_END()
#define STAMP(i)
#define STAMP_ARG
#define STAMP_PASS(ph)
#define STAMP_PTR(ph)

struct Ptrs {
  unsigned long long* stamps;
  const u16 *x0, *sre, *sim, *dre, *dim, *wg, *wu, *wd;
  u16 *out, *xn, *pq, *xa, *hh;
  float* stats;
  unsigned *csf, *gran, *mbox, *tmo;
};
constexpr size_t XN_L = (size_t)SEQ * DM, PQ_L = (size_t)SEQ * 1024, HH_L = (size_t)SEQ * FF, ST_L = (size_t)16 * SEQ * 2;

__device__ __forceinline__ void signal(unsigned* mbox, int seam, int consumer, int slot, bool active) {
  if (active) __hip_atomic_store((gu32*)mbox + ((size_t)seam * 256 + consumer) * 32 + slot, 1u, RLX_AGENT);
}
__device__ __forceinline__ void await(unsigned* mbox, unsigned* tmo, int seam, int me, int nprod, int lane) {
  gu32* q = (gu32*)mbox + ((size_t)seam * 256 + me) * 32 + (lane < nprod ? lane : 0);
  for (unsigned spins = 0;; ++spins) {
    unsigned x = __hip_atomic_load(q, RLX_AGENT);
    if (__all(x == 1u)) break;
    if (spins > SPIN_CAP) { if (lane == 0) __hip_atomic_store((gu32*)tmo, 1u, RLX_AGENT); break; }
    __builtin_amdgcn_s_sleep(1);
  }
}

struct PLoader {
  rsrc_t rre, rim; unsigned vw, vx; lds_t lds; int lw, k0;
  __device__ __forceinline__ PLoader(lds_t lds_, const Ptrs& p, int tb, int et, int lw_, int lane, int rot) : lds(lds_), lw(lw_), k0((rot * et) & 31) {
    rre = mk_rsrc(p.sre + (size_t)tb * 64 * SEQ, 64 * SEQ * 2); rim = mk_rsrc(p.sim + (size_t)tb * 64 * SEQ, 64 * SEQ * 2);
    vw = km_src(lw, lane, SEQ * 2);
    vx = (lane >> 5) * 512 + (((lane & 31) ^ (4 * ((2 * lw + (lane >> 5)) & 3))) << 4);
  }
  template <class S> __device__ __forceinline__ void W(S s) {
    const int ks = (s.v + k0) & 31;
    static_for<0, 4>([&](auto n) { dma16<0>(n.v < 2 ? rre : rim, lds + wslot<GeoP>(s.v) * 16384 + (lw + 4 * n.v) * 1024, vw, ks * 128 + (n.v & 1) * 32 * SEQ * 2); });
  }
  template <class S> __device__ __forceinline__ void X(S s, rsrc_t rx) {
    const int ks = (s.v + k0) & 31;
    static_for<0, 2>([&](auto n) { dma16<SC1>(rx, lds + XB + (s.v % 8) * 8192 + n.v * 4096 + lw * 1024, vx, (2 * ks + n.v) * 64 * 512); });
  }
  __device__ __forceinline__ rsrc_t xr(const u16* xn, int et) { return mk_rsrc((const char*)xn + (8 * et + 2 * lw) * 512, SEQ * DM * 2 - (8 * et + 2 * lw) * 512); }
};
struct YLoader {
  rsrc_t rre, rim; unsigned vw, vx; lds_t lds; int lw;
  __device__ __forceinline__ YLoader(lds_t lds_, const Ptrs& p, int et, int lw_, int lane) : lds(lds_), lw(lw_) {
    rre = mk_rsrc(p.dre + (size_t)et * 64 * DM, 64 * DM * 2); rim = mk_rsrc(p.dim + (size_t)et * 64 * DM, 64 * DM * 2);
    vw = km_src(lw, lane, DM * 2); vx = lane * 16;
  }
  template <class S> __device__ __forceinline__ void W(S s) {
    static_for<0, 4>([&](auto i) {
      constexpr int kb = i.v >> 1, n = i.v & 1;
      dma16<0>(s.v < 4 ? rre : rim, lds + wslot<GeoY>(s.v) * 16384 + kb * 8192 + (lw + 4 * n) * 1024, vw, (2 * (s.v & 3) + kb) * 128 + n * 32 * DM * 2);
    });
  }
  template <class S> __device__ __forceinline__ void X(S s, rsrc_t rx) {
    static_for<0, 4>([&](auto i) {
      constexpr int blk = i.v >> 1, n = i.v & 1;
      dma16<SC1>(rx, lds + XB + (s.v % 4) * 16384 + blk * 8192 + n * 1024 + lw * 2048, vx, blk * 128 * 512 + (16 * s.v + 2 * n) * 512);
    });
  }
  __device__ __forceinline__ rsrc_t xr(const u16* pq, int tb) { return mk_rsrc((const char*)pq + ((size_t)2 * tb * 128 + 4 * lw) * 512, 2 * 128 * 512 - 4 * lw * 512); }
};
constexpr int C_WS = 28672;
struct CLoader {
  const char* wsrc[7]; unsigned vx; lds_t lds; int lw;
  __device__ __forceinline__ CLoader(lds_t lds_, const Ptrs& p, int fc, int lw_, int lane) : lds(lds_), lw(lw_) {
    vx = lane * 16;
#pragma unroll
    for (int n = 0; n < 7; ++n) {
      int idx = 64 * n + lane, kr = idx / 28, c = idx - kr * 28;
      const u16* base = c < 14 ? p.wg : p.wu;
      int cc = c < 14 ? c : c - 14;
      wsrc[n] = (const char*)(base + (size_t)(16 * lw + kr) * FF + 112 * fc + 8 * cc);
    }
  }
  template <class S> __device__ __forceinline__ void W(S s) {
    static_for<0, 7>([&](auto n) {
      __builtin_amdgcn_global_load_lds((const unsigned*)(wsrc[n.v] + (size_t)s.v * 64 * FF * 2), (LAS unsigned*)(lds + wslot<GeoC>(s.v) * C_WS + lw * 7168 + n.v * 1024), 16, 0, 0);
    });
  }
  template <class S> __device__ __forceinline__ void X(S s, rsrc_t rx) {
    static_for<0, 4>([&](auto n) { dma16<SC1>(rx, lds + XB + (s.v % 4) * 16384 + lw * 4096 + n.v * 1024, vx, (8 * s.v + 2 * n.v) * 512); });
  }
  __device__ __forceinline__ rsrc_t xr(const u16* xa, int rb) { return mk_rsrc((const char*)xa + (size_t)(4 * rb + lw) * 64 * 512, 64 * 512); }
};
struct DLoader {
  rsrc_t rw; unsigned vw, vx; lds_t lds; int lw, k0;
  __device__ __forceinline__ DLoader(lds_t lds_, const Ptrs& p, int tb, int et, int lw_, int lane) : lds(lds_), lw(lw_), k0(ROTD ? (tb * 3) % 14 : 0) {
    rw = mk_rsrc(p.wd + et * 64, FF * DM * 2 - et * 128);
    vw = kn_src(lw, lane, DM * 2); vx = lane * 16;
  }
  template <class S> __device__ __forceinline__ void W(S s) {
    const int ks = s.v + k0 < 14 ? s.v + k0 : s.v + k0 - 14;
    static_for<0, 4>([&](auto i) {
      constexpr int kb = i.v >> 1, n = i.v & 1;
      dma16<0>(rw, lds + wslot<GeoD>(s.v) * 16384 + kb * 8192 + (lw + 4 * n) * 1024, vw, ((2 * ks + kb) * 64 + 32 * n) * DM * 2);
    });
  }
  template <class S> __device__ __forceinline__ void X(S s, rsrc_t rx) {
    const int ks = s.v + k0 < 14 ? s.v + k0 : s.v + k0 - 14;
    static_for<0, 4>([&](auto i) {
      constexpr int blk = i.v >> 1, n = i.v & 1;
      dma16<SC1>(rx, lds + XB + (s.v % 4) * 16384 + blk * 8192 + n * 1024 + lw * 2048, vx, blk * 224 * 512 + (16 * ks + 2 * n) * 512);
    });
  }
  __device__ __forceinline__ rsrc_t xr(const u16* hh, int tb) { return mk_rsrc((const char*)hh + ((size_t)2 * tb * 224 + 4 * lw) * 512, 2 * 224 * 512 - 4 * lw * 512); }
};
template <class G, class MINE, class LD> __device__ __forceinline__ void l_seam(LD& L) {
  bar(); bar();
  static_for<0, G::NST>([&](auto j) { if constexpr (MINE::at(j.v) == GAP) L.W(j); });
  bar();
}
template <class G, class MINE, class GN, class NEXT, class LD, class LN>
__device__ __forceinline__ void l_phase(LD& L, rsrc_t rx, LN& N) {
  l_run<G, MINE, GN, NEXT>([&](auto s) { L.X(s, rx); }, [&](auto s) { L.W(s); }, [&](auto s) { N.W(s); });
}

__device__ __forceinline__ void p_compute(lds_t lds, u16* pq, int tb, int et, int w, int lane, const char* t0, const char* t1, const char* t4, bool touch4, bool touch, unsigned& dummy STAMP_ARG) {
  const int r = lane & 31, h = lane >> 5;
  const unsigned a0 = km_read_base(32 * w + r, h);
  const int bl = (lane >> 4) & 1, q4 = (lane & 15) >> 2, pp = ((lane & 1) << 1) | ((lane >> 1) & 1), cc0 = 2 * bl + (pp >> 1);
  const unsigned f0 = cc0 * 512 + (((8 * h + q4) ^ (4 * cc0)) << 4) + 8 * (pp & 1), f1 = f0 ^ 64;
  f32x16 acc0 = {}, acc1 = {};
  unsigned wo = GeoP::OW * 16384, xo = XB;
  struct Fr { bf16x8 tok[4], fa[4], fb[4]; };
  Fr A, B;
  auto rdt = [&](Fr& f, int k) { f.tok[k] = ld_b128(lds + wo + (a0 ^ (k << 5))); };
  auto rda = [&](Fr& f, int k) { lds_t xb = lds + xo + (k >> 1) * 4096 + (k & 1) * 256; f.fa[k] = ld_tr(xb + f0, xb + f1); };
  auto rdb = [&](Fr& f, int k) { lds_t xb = lds + xo + (k >> 1) * 4096 + (k & 1) * 256 + 2048; f.fb[k] = ld_tr(xb + f0, xb + f1); };
  auto adv = [&]() {
    wo = wo + 16384 == 6 * 16384 ? 0 : wo + 16384;
    xo = xo + 8192 == LDS_BYTES ? XB : xo + 8192;
  };
  auto step = [&](Fr& nx, const Fr& cu) {
    cbar();
#pragma unroll
    for (int k = 0; k < 4; ++k) {
      rdt(nx, k); rda(nx, k); SCHED();
      acc0 = mfma(cu.fa[k], cu.tok[k], acc0); SCHED();
      rdb(nx, k); SCHED();
      acc1 = mfma(cu.fb[k], cu.tok[k], acc1); SCHED();
    }
    adv();
  };
  cbar();
  STAMP(1);
#pragma unroll
  for (int k = 0; k < 4; ++k) { rdt(A, k); rda(A, k); rdb(A, k); }
  adv();
#pragma unroll 1
  for (int s = 1; s < 31; s += 2) {
    step(B, A);
    if (touch4 && s == TS4) asm volatile("global_load_dword %0, %1, off" : "+v"(dummy) : "v"(t4) : "memory");
    if (touch && s == TS) {
      asm volatile("global_load_dword %0, %1, off" : "+v"(dummy) : "v"(t0) : "memory");
      asm volatile("global_load_dword %0, %1, off" : "+v"(dummy) : "v"(t1) : "memory");
    }
    step(A, B);
  }
  step(B, A);
  cbar();
  STAMP(2);
#pragma unroll
  for (int k = 0; k < 4; ++k) { acc0 = mfma(B.fa[k], B.tok[k], acc0); acc1 = mfma(B.fb[k], B.tok[k], acc1); }
  const float sg = w >= 2 ? -1.f : 1.f;
  const rsrc_t ro = mk_rsrc((char*)pq + ((size_t)(2 * tb + (w & 1)) * 128 + (w >> 1) * 64 + 8 * et) * 512, 4096);
#pragma unroll
  for (int ft = 0; ft < 2; ++ft)
#pragma unroll
    for (int q = 0; q < 2; ++q) {
      const f32x16& a = ft ? acc1 : acc0;
      u32x4 v = {pk2(sg * a[8 * q], sg * a[8 * q + 1]), pk2(sg * a[8 * q + 2], sg * a[8 * q + 3]), pk2(sg * a[8 * q + 4], sg * a[8 * q + 5]), pk2(sg * a[8 * q + 6], sg * a[8 * q + 7])};
      __builtin_amdgcn_raw_buffer_store_b128(v, ro, lane * 16 + ft * 2048 + q * 1024, 0, SC1);
    }
}

__device__ __forceinline__ void add_res(const f32x16& acc, u32x4& r0, u32x4& r1) {
#pragma unroll
  for (int i = 0; i < 4; ++i) {
    r0[i] = pk2(acc[2 * i] + bflo(r0[i]), acc[2 * i + 1] + bfhi(r0[i]));
    r1[i] = pk2(acc[8 + 2 * i] + bflo(r1[i]), acc[8 + 2 * i + 1] + bfhi(r1[i]));
  }
}
__device__ __forceinline__ void store_rowmajor(u16* xo, const u32x4& o0, const u32x4& o1, int row0, int col0, int lane) {
  const rsrc_t ro = mk_rsrc(xo + (size_t)row0 * DM + col0, 32 * DM * 2);
  const unsigned vo = (lane & 31) * DM * 2 + (lane >> 5) * 16;
  __builtin_amdgcn_raw_buffer_store_b128(o0, ro, vo, 0, 0);
  __builtin_amdgcn_raw_buffer_store_b128(o1, ro, vo + 32, 0, 0);
}
__device__ __forceinline__ void store_blocked(u16* xo, const u32x4& o0, const u32x4& o1, int row0, int col0, int lane) {
  const rsrc_t ro = mk_rsrc((char*)xo + ((size_t)(row0 >> 5) * 64 + (col0 >> 3)) * 512, 2048);
  __builtin_amdgcn_raw_buffer_store_b128(o0, ro, lane * 16, 0, SC1);
  __builtin_amdgcn_raw_buffer_store_b128(o1, ro, lane * 16 + 1024, 0, SC1);
}
__device__ __forceinline__ void part_stats(const u32x4& o0, const u32x4& o1, float (&v)[16], float& m, float& m2) {
  float s0 = 0.f, s1 = 0.f;
#pragma unroll
  for (int i = 0; i < 4; ++i) { v[2 * i] = bflo(o0[i]); v[2 * i + 1] = bfhi(o0[i]); v[8 + 2 * i] = bflo(o1[i]); v[8 + 2 * i + 1] = bfhi(o1[i]); }
#pragma unroll
  for (int i = 0; i < 8; ++i) { s0 += v[i]; s1 += v[8 + i]; }
  m = xhalf_sum(s0 + s1) * (1.f / 32.f);
  float q0 = 0.f, q1 = 0.f;
#pragma unroll
  for (int i = 0; i < 8; ++i) { float d = v[i] - m, e = v[8 + i] - m; q0 += d * d; q1 += e * e; }
  m2 = xhalf_sum(q0 + q1);
}
__device__ __forceinline__ void store_stats(float* stats, float m, float m2, int row0, int col0, int lane) {
  const rsrc_t rs = mk_rsrc(stats + ((size_t)(col0 >> 5) * SEQ + row0) * 2, 32 * 8);
  if (lane < 32) { u32x2 sv = {__float_as_uint(m), __float_as_uint(m2)}; __builtin_amdgcn_raw_buffer_store_b64(sv, rs, lane * 8, 0, SC1); }
}
__device__ __forceinline__ void load_stats(const float* stats, int row0, int lane, u32x2 (&t)[8]) {
  gu64* q = (gu64*)stats + row0 + (lane & 31) + (size_t)(8 * (lane >> 5)) * SEQ;
#pragma unroll
  for (int i = 0; i < 8; ++i) { unsigned long long x = __hip_atomic_load(q + i * SEQ, RLX_AGENT); t[i] = u32x2{(unsigned)x, (unsigned)(x >> 32)}; }
}
__device__ __forceinline__ void combine_stats(const u32x2 (&t)[8], float& mean, float& rstd) {
  float ms = 0.f, m2 = 0.f;
#pragma unroll
  for (int i = 0; i < 8; ++i) { ms += __uint_as_float(t[i][0]); m2 += __uint_as_float(t[i][1]); }
  mean = xhalf_sum(ms) * (1.f / 16.f);
#pragma unroll
  for (int i = 0; i < 8; ++i) { float d = __uint_as_float(t[i][0]) - mean; m2 += 32.f * d * d; }
  rstd = rsqrtf(xhalf_sum(m2) * (1.f / 512.f) + 1e-5f);
}

template <class G, class FF_> __device__ __forceinline__ f32x16 tile64_loop(lds_t lds, unsigned a0, FF_ feat, int w, int lane STAMP_ARG) {
  f32x16 acc = {};
  unsigned wo = G::OW * 16384, xo = XB;
  struct Fr { bf16x8 tok[8], f[8]; };
  Fr A, B;
  auto rd = [&](Fr& fr, int k) {
    fr.tok[k] = ld_b128(lds + xo + a0 + k * 1024);
    fr.f[k] = feat(lds + wo, k);
  };
  auto adv = [&]() {
    wo = wo + 16384 == 6 * 16384 ? 0 : wo + 16384;
    xo = xo + 16384 == LDS_BYTES ? XB : xo + 16384;
  };
  auto step = [&](Fr& nx, const Fr& cu) {
    cbar();
#pragma unroll
    for (int k = 0; k < 8; ++k) { rd(nx, k); SCHED(); acc = mfma(cu.f[k], cu.tok[k], acc); SCHED(); }
    adv();
  };
  cbar();
  STAMP(1);
#pragma unroll
  for (int k = 0; k < 8; ++k) rd(A, k);
  adv();
#pragma unroll 1
  for (int s = 1; s < G::NST - 1; s += 2) { step(B, A); step(A, B); }
  step(B, A);
  cbar();
  STAMP(2);
#pragma unroll
  for (int k = 0; k < 8; ++k) acc = mfma(B.f[k], B.tok[k], acc);
  return acc;
}

__device__ __forceinline__ void c_compute(lds_t lds, const float* stats, const unsigned* csf, u16* hh, int rb, int fc, int w, int lane STAMP_ARG) {
  const int r = lane & 31, h = lane >> 5;
  const unsigned a0 = w * 4096 + lane * 16;
  const int blk = (lane >> 4) & 1, q = (lane & 15) >> 2, pp = ((lane & 1) << 1) | ((lane >> 1) & 1);
  const unsigned f0 = (8 * h + q) * 448 + blk * 224 + 8 * pp;
  f32x16 acc[7];
#pragma unroll
  for (int j = 0; j < 7; ++j) acc[j] = f32x16{};
  unsigned wo = GeoC::OW * C_WS, xo = XB;
  struct Fr { bf16x8 tok, f[7]; };
  Fr A, B;
  auto rdt = [&](Fr& fr, int k) { fr.tok = ld_b128(lds + xo + a0 + k * 1024); };
  auto rdf = [&](Fr& fr, int k, int j) { lds_t wb = lds + wo + f0; fr.f[j] = ld_tr(wb + 16 * k * 448 + 32 * j, wb + (16 * k + 4) * 448 + 32 * j); };
  auto grp = [&](Fr& nx, const Fr& cu, int k, bool rdn) {
#pragma unroll
    for (int j = 0; j < 7; ++j) {
      if (rdn) { if (j == 0) rdt(nx, k); rdf(nx, k, j); }
      SCHED();
      acc[j] = mfma(cu.f[j], cu.tok, acc[j]);
      SCHED();
    }
  };
  auto adv = [&]() {
    wo = wo + C_WS == 3 * C_WS ? 0 : wo + C_WS;
    xo = xo + 16384 == LDS_BYTES ? XB : xo + 16384;
  };
  cbar();
  STAMP(1);
  u32x2 st[8];
  load_stats(stats, 128 * rb + 32 * w, lane, st);
  unsigned long long cs[7];
#pragma unroll
  for (int j = 0; j < 7; ++j) cs[j] = __hip_atomic_load((gu64*)csf + ((size_t)fc * 32 + r) * 8 + j, __ATOMIC_RELAXED, __HIP_MEMORY_SCOPE_WORKGROUP);
  rdt(A, 0);
#pragma unroll
  for (int j = 0; j < 7; ++j) rdf(A, 0, j);
  auto stage = [&]() {
    grp(B, A, 1, true);
    grp(A, B, 2, true);
    grp(B, A, 3, true);
    adv();
    cbar();
    grp(A, B, 0, true);
  };
  stage();
  float mean, rstd;
  combine_stats(st, mean, rstd);
  {
    const unsigned mh = pk2(-mean, 0.f) & 0xffffu;
    const unsigned ml = pk2(-mean - bflo(mh), 0.f) & 0xffffu;
    u32x4 bt = {h ? 0u : (mh | (ml << 16)), h ? 0u : mh, 0u, 0u};
#pragma unroll
    for (int j = 0; j < 7; ++j) {
      u32x4 at = {h ? 0u : (unsigned)cs[j], h ? 0u : (unsigned)(cs[j] >> 32), 0u, 0u};
      acc[j] = mfma(__builtin_bit_cast(bf16x8, at), __builtin_bit_cast(bf16x8, bt), acc[j]);
    }
  }
  SCHED();
#pragma unroll 1
  for (int s = 1; s < 7; ++s) stage();
  grp(B, A, 1, true);
  grp(A, B, 2, true);
  grp(B, A, 3, true);
  cbar();
  STAMP(2);
  grp(A, B, 0, false);
  const rsrc_t ro = mk_rsrc((char*)hh + ((size_t)(4 * rb + w) * 224 + 14 * fc) * 512, 7168);
  const float ng = -1.4426950408889634f * rstd;
#pragma unroll
  for (int j = 0; j < 7; ++j) {
    float o[8];
#pragma unroll
    for (int i = 0; i < 8; i += 2) {
      f32x2 g = {acc[j][i], acc[j][i + 1]}, u = {acc[j][8 + i], acc[j][9 + i]};
      g *= ng; u *= rstd;
      f32x2 e = {__builtin_amdgcn_exp2f(g[0]), __builtin_amdgcn_exp2f(g[1])};
      e += 1.f;
      f32x2 sg = {__builtin_amdgcn_rcpf(e[0]), __builtin_amdgcn_rcpf(e[1])};
      u *= sg;
      o[i] = u[0]; o[i + 1] = u[1];
    }
    u32x4 v = {pk2(o[0], o[1]), pk2(o[2], o[3]), pk2(o[4], o[5]), pk2(o[6], o[7])};
    __builtin_amdgcn_raw_buffer_store_b128(v, ro, lane * 16 + 1024 * j, 0, SC1);
    SCHED();
  }
}

__global__ __launch_bounds__(512, 2) __attribute__((amdgpu_waves_per_eu(2, 2))) void k_main(Ptrs p) {
  extern __shared__ __attribute__((aligned(16))) char smem[];
  lds_t lds = (lds_t)smem;
  const int lane = threadIdx.x & 63, wid = __builtin_amdgcn_readfirstlane(threadIdx.x >> 6);
  const int xcd = blockIdx.x & 7, j = blockIdx.x >> 3;
  const int tb = xcd * 4 + (j & 3), et = j >> 2;
  const int rb = (xcd & 1) * 8 + (j & 7), fc = (xcd >> 1) * 4 + (j >> 3);
  SPAN_MAIN();
  if (wid >= 4) {
    {
      PLoader L(lds, p, fresh_s(tb), fresh_s(et), fresh_s(wid - 4), fresh_v(lane), ROT);
      YLoader N(lds, p, fresh_s(et), fresh_s(wid - 4), fresh_v(lane));
      l_phase<GeoP, EarlyNone, GeoY, EarlyY>(L, L.xr(p.xn, et), N);
    }
#pragma unroll 1
    for (int l = 0; l < 2; ++l) {
      {
        YLoader L(lds, p, fresh_s(et), fresh_s(wid - 4), fresh_v(lane));
        CLoader N(lds, p, fresh_s(fc), fresh_s(wid - 4), fresh_v(lane));
        const rsrc_t rx = L.xr(p.pq + l * PQ_L, fresh_s(tb));
        l_seam<GeoY, EarlyY>(L);
        l_phase<GeoY, EarlyY, GeoC, EarlyC>(L, rx, N);
      }
      {
        CLoader L(lds, p, fresh_s(fc), fresh_s(wid - 4), fresh_v(lane));
        DLoader N(lds, p, fresh_s(tb), fresh_s(et), fresh_s(wid - 4), fresh_v(lane));
        const rsrc_t rx = L.xr(p.xa + l * XN_L, fresh_s(rb));
        l_seam<GeoC, EarlyC>(L);
        l_phase<GeoC, EarlyC, GeoD, EarlyD>(L, rx, N);
      }
      if (l == 0) {
        {
          DLoader L(lds, p, fresh_s(tb), fresh_s(et), fresh_s(wid - 4), fresh_v(lane));
          PLoader N(lds, p, fresh_s(tb), fresh_s(et), fresh_s(wid - 4), fresh_v(lane), ROT2);
          const rsrc_t rx = L.xr(p.hh, fresh_s(tb));
          l_seam<GeoD, EarlyD>(L);
          l_phase<GeoD, EarlyD, GeoP, EarlyP>(L, rx, N);
        }
        PLoader L(lds, p, fresh_s(tb), fresh_s(et), fresh_s(wid - 4), fresh_v(lane), ROT2);
        YLoader N(lds, p, fresh_s(et), fresh_s(wid - 4), fresh_v(lane));
        const rsrc_t rx = L.xr(p.xn + XN_L, fresh_s(et));
        l_seam<GeoP, EarlyP>(L);
        l_phase<GeoP, EarlyP, GeoY, EarlyY>(L, rx, N);
      } else {
        DLoader L(lds, p, fresh_s(tb), fresh_s(et), fresh_s(wid - 4), fresh_v(lane));
        const rsrc_t rx = L.xr(p.hh + HH_L, fresh_s(tb));
        l_seam<GeoD, EarlyD>(L);
        l_phase<GeoD, EarlyD, GeoP, EarlyNone>(L, rx, L);
      }
    }
  } else {
    const int w = wid, tw = w & 1, fw = w >> 1;
    const int row0 = 64 * tb + 32 * tw, col0 = 64 * et + 32 * fw;
    unsigned dummy = 0;
    u32x4 r0, r1;
    {
      const int r = lane & 31, h = lane >> 5;
      const rsrc_t rr = mk_rsrc(p.x0 + (size_t)row0 * DM + col0, 32 * DM * 2);
      r0 = __builtin_amdgcn_raw_buffer_load_b128(rr, r * DM * 2 + h * 16, 0, 0);
      r1 = __builtin_amdgcn_raw_buffer_load_b128(rr, r * DM * 2 + h * 16 + 32, 0, 0);
    }
#pragma unroll 1
    for (int l = 0; l < 2; ++l) {
      const int sb = 4 * l;
      {
        STAMP_PTR(4 * l + 0);
        STAMP(0);
        const int ln = fresh_v(lane), li = blockIdx.x * 64 + ln, lj = blockIdx.x * 32 + (ln & 31);
        const char* t0 = li < 14336 ? (const char*)p.wd + (size_t)li * 128 : (const char*)p.wd;
        const char* t1 = (ln < 32 ? (const char*)p.dre : (const char*)p.dim) + (size_t)(lj & 4095) * 128;
        const int trow = 16 * et + 4 * w + (ln >> 4);
        const char* t4 = (const char*)(trow < 64 ? p.sre : p.sim) + (size_t)(64 * tb + (trow & 63)) * (SEQ * 2) + 2048 + (ln & 15) * 128;
        p_compute(lds, p.pq + l * PQ_L, fresh_s(tb), fresh_s(et), fresh_s(w), fresh_v(lane), t0, t1, t4, TS4 > 0 && l == 0, !KW && l == 0 && w == 0 && blockIdx.x < 224, dummy STAMP_PASS(4 * l + 0));
        STAMP(5); WAIT_V(0); STAMP(6); bar();
        asm volatile("" :: "v"(dummy));
        if (w == 0) signal(p.mbox, sb + 0, tb * 8 + (lane & 7), et, lane < 8);
        bar();
        if (w == 0) await(p.mbox, p.tmo, sb + 0, tb * 8 + et, 8, lane);
        bar(); STAMP(7);
      }
      {
        STAMP_PTR(4 * l + 1);
        STAMP(0);
        const int ln = fresh_v(lane), r = ln & 31, h = ln >> 5;
        const unsigned a0 = tw * 8192 + ln * 16, b0 = km_read_base(32 * fw + perm23(r), h);
        f32x16 acc = tile64_loop<GeoY>(lds, a0, [&](lds_t wb, int k) { return ld_b128(wb + (k >> 2) * 8192 + (b0 ^ ((k & 3) << 5))); }, w, lane STAMP_PASS(4 * l + 1));
        add_res(acc, r0, r1);
        store_blocked(p.xa + l * XN_L, r0, r1, row0, col0, fresh_v(lane));
        float v[16], m, m2;
        part_stats(r0, r1, v, m, m2);
        store_stats(p.stats + l * ST_L, m, m2, row0, col0, fresh_v(lane));
        STAMP(5); WAIT_V(0); STAMP(6); bar();
        if (w == 0) signal(p.mbox, sb + 1, (tb >> 1) * 16 + (lane & 15), (tb & 1) * 8 + et, lane < 16);
        bar();
        if (w == 0) await(p.mbox, p.tmo, sb + 1, rb * 16 + fc, 16, lane);
        bar(); STAMP(7);
      }
      {
        STAMP_PTR(4 * l + 2);
        STAMP(0);
        c_compute(lds, p.stats + l * ST_L, p.csf, p.hh + l * HH_L, fresh_s(rb), fresh_s(fc), fresh_s(w), fresh_v(lane) STAMP_PASS(4 * l + 2));
        STAMP(5); WAIT_V(0); STAMP(6); bar();
        if (w == 0) signal(p.mbox, sb + 2, (2 * rb + ((lane >> 3) & 1)) * 8 + (lane & 7), fc, lane < 16);
        bar();
        if (w == 0) await(p.mbox, p.tmo, sb + 2, tb * 8 + et, 16, lane);
        bar(); STAMP(7);
      }
      {
        STAMP_PTR(4 * l + 3);
        STAMP(0);
        const int ln = fresh_v(lane);
        const unsigned a0 = tw * 8192 + ln * 16, f0 = kn_read_base(ln, fw);
        f32x16 acc = tile64_loop<GeoD>(lds, a0, [&](lds_t wb, int k) { return ld_tr(wb + f0 + k * 2048, wb + f0 + k * 2048 + 512); }, w, lane STAMP_PASS(4 * l + 3));
        add_res(acc, r0, r1);
        if (l == 1) {
          store_rowmajor(p.out, r0, r1, row0, col0, fresh_v(lane));
          if (w == 0) SPAN_END();
        } else {
          float v[16], m, m2;
          part_stats(r0, r1, v, m, m2);
          const int le = fresh_v(lane);
          store_stats((float*)p.gran, m, m2, row0, col0, le);
          STAMP(3);
          u32x2 st[8];
          for (unsigned spins = 0;; ++spins) {
            load_stats((const float*)p.gran, row0, le, st);
            bool ok = true;
#pragma unroll
            for (int i = 0; i < 8; ++i) ok = ok && st[i][1] != 0xffffffffu;
            if (__all(ok)) break;
            if (spins > SPIN_CAP) { if (lane == 0) __hip_atomic_store((gu32*)p.tmo, 2u, RLX_AGENT); break; }
            __builtin_amdgcn_s_sleep(1);
          }
          STAMP(4);
          float mean, rstd;
          combine_stats(st, mean, rstd);
          u32x4 n0, n1;
#pragma unroll
          for (int i = 0; i < 4; ++i) {
            n0[i] = pk2((v[2 * i] - mean) * rstd, (v[2 * i + 1] - mean) * rstd);
            n1[i] = pk2((v[8 + 2 * i] - mean) * rstd, (v[8 + 2 * i + 1] - mean) * rstd);
          }
          store_blocked(p.xn + XN_L, n0, n1, row0, col0, le);
          STAMP(5); WAIT_V(0); STAMP(6); bar();
          if (w == 0) signal(p.mbox, 3, (lane & 31) * 8 + et, tb, lane < 32);
          bar();
          if (w == 0) await(p.mbox, p.tmo, 3, tb * 8 + et, 32, lane);
          bar(); STAMP(7);
        }
      }
    }
  }
}

__device__ __forceinline__ float wave_sum(float v) {
#pragma unroll
  for (int o = 1; o < 64; o <<= 1) v += __shfl_xor(v, o);
  return v;
}
__global__ __launch_bounds__(512) void k_pre(Ptrs p) {
  const int lane = threadIdx.x & 63, wid = threadIdx.x >> 6;
  if (blockIdx.x < 256) {
    SPAN_BEGIN();
    const int row = blockIdx.x * 8 + wid;
    u32x4 t = *(const u32x4*)(p.x0 + (size_t)row * DM + lane * 8);
    const int gid = blockIdx.x * 512 + threadIdx.x;
    if (gid < 7 * 256 * 8) ((u32x4*)p.mbox)[gid] = u32x4{0u, 0u, 0u, 0u};
    if (gid < 16 * SEQ / 2) ((u32x4*)p.gran)[gid] = u32x4{~0u, ~0u, ~0u, ~0u};
    if (gid == 0) *p.tmo = 0u;
    float v[8], s = 0.f;
#pragma unroll
    for (int i = 0; i < 4; ++i) { v[2 * i] = bflo(t[i]); v[2 * i + 1] = bfhi(t[i]); s += v[2 * i] + v[2 * i + 1]; }
    const float m = wave_sum(s) * (1.f / 512.f);
    float m2 = 0.f;
#pragma unroll
    for (int i = 0; i < 8; ++i) { v[i] -= m; m2 += v[i] * v[i]; }
    const float rs = rsqrtf(wave_sum(m2) * (1.f / 512.f) + 1e-5f);
    u32x4 o = {pk2(v[0] * rs, v[1] * rs), pk2(v[2] * rs, v[3] * rs), pk2(v[4] * rs, v[5] * rs), pk2(v[6] * rs, v[7] * rs)};
    *(u32x4*)((char*)p.xn + ((size_t)(row >> 5) * 64 + lane) * 512 + (row & 31) * 16) = o;
  } else if (blockIdx.x >= 440) {
    const int li = (blockIdx.x - 440) * 512 + threadIdx.x;
    const char* a = li < 4096 ? (const char*)p.dre + (size_t)li * 128 : li < 8192 ? (const char*)p.dim + (size_t)(li - 4096) * 128 : (const char*)p.wd + (size_t)(li - 8192) * 128;
    unsigned d0;
    asm volatile("global_load_dword %0, %1, off\n\ts_waitcnt vmcnt(0)" : "=v"(d0) : "v"(a) : "memory");
  } else if (blockIdx.x >= 312) {
    const int q = blockIdx.x - 312, xcd = q & 7, i = q >> 3, tb = xcd * 4 + (i & 3);
    const int r = 32 * (i >> 2) + 4 * wid + (lane >> 4);
    const char* a = (const char*)(r < 64 ? p.sre : p.sim) + (size_t)(64 * tb + (r & 63)) * (SEQ * 2) + (lane & 15) * 128;
    unsigned d0, d1 = 0;
    asm volatile("global_load_dword %0, %1, off" : "=v"(d0) : "v"(a) : "memory");
    if (KT == 2) asm volatile("global_load_dword %0, %1, off offset:2048" : "=v"(d1) : "v"(a) : "memory");
    asm volatile("s_waitcnt vmcnt(0)" : "+v"(d0), "+v"(d1) :: "memory");
  } else {
    __shared__ float red[8][64];
    const int b = blockIdx.x - 256;
    const u16* w = (b < 28 ? p.wg : p.wu) + (b % 28) * 64 + (lane & 7) * 8;
    float a[8] = {};
#pragma unroll
    for (int i = 0; i < 8; ++i) {
      u32x4 t = *(const u32x4*)(w + (size_t)(i * 64 + wid * 8 + (lane >> 3)) * FF);
#pragma unroll
      for (int e = 0; e < 4; ++e) { a[2 * e] += bflo(t[e]); a[2 * e + 1] += bfhi(t[e]); }
    }
#pragma unroll
    for (int e = 0; e < 8; ++e) { a[e] += __shfl_xor(a[e], 8); a[e] += __shfl_xor(a[e], 16); a[e] += __shfl_xor(a[e], 32); }
    if (lane < 8) {
#pragma unroll
      for (int e = 0; e < 8; ++e) red[wid][lane * 8 + e] = a[e];
    }
    __syncthreads();
    if (threadIdx.x < 64) {
      float s = 0.f;
#pragma unroll
      for (int i = 0; i < 8; ++i) s += red[i][threadIdx.x];
      const int c = (b % 28) * 64 + threadIdx.x, fc = c / 112, rem = c - fc * 112, jj = rem >> 4, i = perm23(rem & 15) + (b < 28 ? 0 : 16);
      const unsigned hi = pk2(s, 0.f) & 0xffffu, lo = pk2(s - bflo(hi), 0.f) & 0xffffu;
      u32x2 o = {hi | (hi << 16), lo};
      *(u32x2*)(p.csf + ((size_t)fc * 32 + i) * 16 + 2 * jj) = o;
    }
  }
}
}

extern "C" void kernel_launch(void* const* d_in, const int* in_sizes, int n_in, void* d_out, int out_size, void* d_ws, size_t ws_size, hipStream_t stream) {
  Ptrs p;
  p.x0 = (const u16*)d_in[0]; p.sre = (const u16*)d_in[1]; p.sim = (const u16*)d_in[2]; p.dre = (const u16*)d_in[3]; p.dim = (const u16*)d_in[4];
  p.wg = (const u16*)d_in[6]; p.wu = (const u16*)d_in[7]; p.wd = (const u16*)d_in[8];
  char* ws = (char*)d_ws;
  const size_t MB = 1 << 20;
  p.out = (u16*)d_out;
  p.xn = (u16*)ws;
  p.pq = (u16*)(ws + 4 * MB);
  p.xa = (u16*)(ws + 12 * MB);
  p.hh = (u16*)(ws + 16 * MB);
  p.stats = (float*)(ws + 31 * MB);
  p.stamps = (unsigned long long*)(ws + 32 * MB);
  p.csf = (unsigned*)(ws + 33 * MB);
  p.gran = (unsigned*)(ws + 34 * MB);
  p.mbox = (unsigned*)(ws + 35 * MB);
  p.tmo = (unsigned*)(ws + 36 * MB);
  (void)hipFuncSetAttribute((const void*)k_main, hipFuncAttributeMaxDynamicSharedMemorySize, LDS_BYTES);
  k_pre<<<256 + 56 + (KT ? 128 : 0) + (KW ? 44 : 0), 512, 0, stream>>>(p);
  k_main<<<256, 512, LDS_BYTES, stream>>>(p);
}
```

```cpp
#include <hip/hip_runtime.h>
#include <stdint.h>

namespace {
typedef unsigned short u16;
typedef short s16x4 __attribute__((ext_vector_type(4)));
typedef short s16x8 __attribute__((ext_vector_type(8)));
typedef __bf16 bf16x8 __attribute__((ext_vector_type(8)));
typedef __bf16 bf16x2 __attribute__((ext_vector_type(2)));
typedef float f32x2 __attribute__((ext_vector_type(2)));
typedef float f32x4 __attribute__((ext_vector_type(4)));
typedef float f32x16 __attribute__((ext_vector_type(16)));
typedef unsigned u32x4 __attribute__((ext_vector_type(4)));
typedef unsigned u32x2 __attribute__((ext_vector_type(2)));
#define LAS __attribute__((address_space(3)))
typedef __amdgpu_buffer_rsrc_t rsrc_t;
typedef LAS char* lds_t;
typedef __attribute__((address_space(1))) unsigned gu32;
typedef __attribute__((address_space(1))) unsigned long long gu64;
#define RLX_AGENT __ATOMIC_RELAXED, __HIP_MEMORY_SCOPE_AGENT

constexpr int SEQ = 2048, DM = 512, FF = 1792;
constexpr int LDS_BYTES = 163840, XB = 98304;
constexpr int SC1 = 16;
constexpr unsigned SPIN_CAP = 1u << 18;

__device__ __forceinline__ rsrc_t mk_rsrc(const void* p, unsigned bytes) { return __builtin_amdgcn_make_buffer_rsrc((void*)p, 0, bytes, 0x00020000); }
template <int AUX> __device__ __forceinline__ void dma16(rsrc_t rs, lds_t dst, unsigned voff, unsigned soff) {
  __builtin_amdgcn_raw_ptr_buffer_load_lds(rs, (LAS void*)dst, 16, voff, soff, 0, AUX);
}
#define WAIT_V(n) asm volatile("s_waitcnt vmcnt(%0)" ::"n"(n) : "memory")
#define SCHED() __builtin_amdgcn_sched_barrier(0)
#define WAIT_L0() __builtin_amdgcn_s_waitcnt(0xC07F)
__device__ __forceinline__ void bar() { asm volatile("" ::: "memory"); __builtin_amdgcn_s_barrier(); asm volatile("" ::: "memory"); }
__device__ __forceinline__ void cbar() { WAIT_L0(); bar(); }
__device__ __forceinline__ int fresh_v(int x) { asm volatile("" : "+v"(x)); return x; }
__device__ __forceinline__ int fresh_s(int x) { asm volatile("" : "+s"(x)); return x; }

template <int I> struct IC { static constexpr int v = I; };
template <int A, int B, class F> __device__ __forceinline__ void static_for(F&& f) {
  if constexpr (A < B) { f(IC<A>{}); static_for<A + 1, B>(f); }
}

constexpr int PXI = 3;
constexpr int ROT1 = 8;
constexpr int TOUCH_AT = 17;

struct GeoP { static constexpr int NST = 32, NX = 8, nX = 2, NW = 6, nW = 4, OW = 0; };
struct GeoY { static constexpr int NST = 8,  NX = 4, nX = 4, NW = 6, nW = 4, OW = 4; };
struct GeoC { static constexpr int NST = 8,  NX = 4, nX = 4, NW = 3, nW = 7, OW = 0; };
struct GeoD { static constexpr int NST = 14, NX = 4, nX = 4, NW = 6, nW = 4, OW = 4; };
constexpr int GAP = 39;
struct EarlyNone { static constexpr int at(int) { return -1; } };
struct EarlyY { static constexpr int at(int j) { constexpr int t[6] = {29, 30, 31, GAP, 27, 28}; return j < 6 ? t[j] : -1; } };
struct EarlyC { static constexpr int at(int j) { constexpr int t[3] = {4, 6, GAP}; return j < 3 ? t[j] : -1; } };
struct EarlyD { static constexpr int at(int j) { constexpr int t[6] = {6, 7, 7, GAP, GAP, GAP}; return j < 6 ? t[j] : -1; } };
struct EarlyP { static constexpr int at(int j) { constexpr int t[6] = {9, 10, 11, 12, 13, GAP}; return j < 6 ? t[j] : -1; } };

constexpr int PLAN_MAX = 40;
template <class G, class MINE, class GN, class NEXT> struct Plan {
  int xstep[PLAN_MAX], wstep[PLAN_MAX], waitn[PLAN_MAX];
  constexpr Plan() : xstep{}, wstep{}, waitn{} {
    int nx = 0;
    for (int j = 0; j < G::NST; ++j) { xstep[j] = -2; wstep[j] = MINE::at(j) >= 0 ? -3 : -2; }
    for (int s = -1; s < G::NST; ++s) {
      const int lx = s - 1 + G::NX < G::NX - 1 ? G::NX - 1 : s - 1 + G::NX, lw = s - 1 + G::NW < G::NW - 1 ? G::NW - 1 : s - 1 + G::NW;
      for (int c = 0; c < (s < 0 ? PXI : 2); ++c) if (nx < G::NST && nx <= lx) xstep[nx++] = s;
      int c = 0;
      for (int j = 0; j < G::NST; ++j)
        if (wstep[j] == -2 && j <= lw && (s < 0 ? (j < PXI) : c < 2)) { wstep[j] = s; ++c; }
    }
    for (int s = 0; s < G::NST; ++s) {
      int total = 0, last = 0;
      for (int b = 0; b < PLAN_MAX; ++b)
        for (int j = 0; j < G::NST; ++j) if (MINE::at(j) == b) { total += G::nW; if (j == s) last = total; }
      for (int t = -1; t < s; ++t) {
        for (int j = 0; j < G::NST; ++j) {
          if (xstep[j] == t) { total += G::nX; if (j == s) last = total; }
          if (t < 0 && wstep[j] == t) { total += G::nW; if (j == s) last = total; }
        }
        if (t >= 0) {
          for (int j = 0; j < G::NST; ++j) if (wstep[j] == t) { total += G::nW; if (j == s) last = total; }
          for (int j = 0; j < GN::NST; ++j) if (NEXT::at(j) == t) total += GN::nW;
        }
      }
      waitn[s] = total - last > 63 ? 63 : total - last;
    }
  }
  constexpr bool ok() const {
    for (int j = 0; j < G::NST; ++j) if (xstep[j] >= j || xstep[j] < -1 || wstep[j] >= j || wstep[j] == -2) return false;
    return true;
  }
};
template <class G, class MINE, class GN, class NEXT> struct PlanOf { static constexpr Plan<G, MINE, GN, NEXT> v{}; };
template <class G, class MINE, class GN, class NEXT, class FX, class FW, class FN>
__device__ __forceinline__ void l_run(FX issueX, FW issueW, FN issueNext) {
  using PL = PlanOf<G, MINE, GN, NEXT>;
  static_assert(PL::v.ok(), "a stage would be waited for before it is issued");
  static_for<0, G::NST>([&](auto j) {
    if constexpr (PL::v.xstep[j.v] == -1) issueX(j);
    if constexpr (PL::v.wstep[j.v] == -1) issueW(j);
  });
  static_for<0, G::NST>([&](auto s) {
    WAIT_V(PL::v.waitn[s.v]);
    bar();
    static_for<0, G::NST>([&](auto j) { if constexpr (PL::v.xstep[j.v] == s.v) issueX(j); });
    static_for<0, G::NST>([&](auto j) { if constexpr (PL::v.wstep[j.v] == s.v) issueW(j); });
    static_for<0, GN::NST>([&](auto j) { if constexpr (NEXT::at(j.v) == s.v) issueNext(j); });
  });
  bar();
  static_for<0, GN::NST>([&](auto j) { if constexpr (NEXT::at(j.v) == G::NST) issueNext(j); });
}
template <class G> constexpr int wslot(int j) { return (G::OW + j) % G::NW; }

__device__ __forceinline__ bf16x8 ld_b128(lds_t p) { return *(LAS bf16x8*)p; }
__device__ __forceinline__ bf16x8 ld_tr(lds_t p0, lds_t p1) {
  s16x4 a = __builtin_amdgcn_ds_read_tr16_b64_v4i16((LAS s16x4*)p0);
  s16x4 b = __builtin_amdgcn_ds_read_tr16_b64_v4i16((LAS s16x4*)p1);
  s16x8 f = __builtin_shufflevector(a, b, 0, 1, 2, 3, 4, 5, 6, 7);
  return __builtin_bit_cast(bf16x8, f);
}
__device__ __forceinline__ f32x16 mfma(bf16x8 a, bf16x8 b, f32x16 c) { return __builtin_amdgcn_mfma_f32_32x32x16_bf16(a, b, c, 0, 0, 0); }
__device__ __forceinline__ unsigned pk2(float a, float b) { f32x2 v = {a, b}; return __builtin_bit_cast(unsigned, __builtin_convertvector(v, bf16x2)); }
__device__ __forceinline__ float bflo(unsigned u) { return __uint_as_float(u << 16); }
__device__ __forceinline__ float bfhi(unsigned u) { return __uint_as_float(u & 0xffff0000u); }
__device__ __forceinline__ float xhalf_sum(float v) {
  auto r = __builtin_amdgcn_permlane32_swap(__float_as_uint(v), __float_as_uint(v), false, false);
  return __uint_as_float(r[0]) + __uint_as_float(r[1]);
}
__device__ __forceinline__ unsigned km_read_base(int row, int h) { return row * 128 + ((h ^ ((row >> 1) & 7)) << 4); }
__device__ __forceinline__ int perm23(int i) { return (i & ~12) | ((i & 4) << 1) | ((i & 8) >> 1); }
__device__ __forceinline__ unsigned km_src(int lw, int lane, int pitch) {
  return (8 * lw + (lane >> 3)) * pitch + (((lane & 7) ^ (((lw & 1) << 2) | (lane >> 4))) << 4);
}
__device__ __forceinline__ unsigned kn_src(int lw, int lane, int pitch) {
  return (8 * lw + (lane >> 3)) * pitch + (((lane & 7) ^ (((lane >> 4) & 1) << 2)) << 4);
}
__device__ __forceinline__ unsigned kn_read_base(int lane, int ft) {
  int h = lane >> 5, blk = (lane >> 4) & 1, q = (lane & 15) >> 2, p = lane & 3, pp = ((p & 1) << 1) | (p >> 1);
  int c = 4 * ft + 2 * blk + (pp >> 1);
  return (8 * h + q) * 128 + ((c ^ (((q >> 1) & 1) << 2)) << 4) + 8 * (pp & 1);
}

struct Ptrs {
  const u16 *x0, *sre, *sim, *dre, *dim, *wg, *wu, *wd;
  u16 *out, *xn, *pq, *xa, *hh;
  float* stats;
  unsigned *csf, *gran, *mbox, *tmo;
};
constexpr size_t XN_L = (size_t)SEQ * DM, PQ_L = (size_t)SEQ * 1024, HH_L = (size_t)SEQ * FF, ST_L = (size_t)16 * SEQ * 2;

__device__ __forceinline__ void signal(unsigned* mbox, int seam, int consumer, int slot, bool active) {
  if (active) __hip_atomic_store((gu32*)mbox + ((size_t)seam * 256 + consumer) * 32 + slot, 1u, RLX_AGENT);
}
__device__ __forceinline__ void await(unsigned* mbox, unsigned* tmo, int seam, int me, int nprod, int lane) {
  gu32* q = (gu32*)mbox + ((size_t)seam * 256 + me) * 32 + (lane < nprod ? lane : 0);
  for (unsigned spins = 0;; ++spins) {
    unsigned x = __hip_atomic_load(q, RLX_AGENT);
    if (__all(x == 1u)) break;
    if (spins > SPIN_CAP) { if (lane == 0) __hip_atomic_store((gu32*)tmo, 1u, RLX_AGENT); break; }
    __builtin_amdgcn_s_sleep(1);
  }
}

struct PLoader {
  rsrc_t rre, rim; unsigned vw, vx; lds_t lds; int lw, k0;
  __device__ __forceinline__ PLoader(lds_t lds_, const Ptrs& p, int tb, int et, int lw_, int lane, int rot) : lds(lds_), lw(lw_), k0((rot * et) & 31) {
    rre = mk_rsrc(p.sre + (size_t)tb * 64 * SEQ, 64 * SEQ * 2); rim = mk_rsrc(p.sim + (size_t)tb * 64 * SEQ, 64 * SEQ * 2);
    vw = km_src(lw, lane, SEQ * 2);
    vx = (lane >> 5) * 512 + (((lane & 31) ^ (4 * ((2 * lw + (lane >> 5)) & 3))) << 4);
  }
  template <class S> __device__ __forceinline__ void W(S s) {
    const int ks = (s.v + k0) & 31;
    static_for<0, 4>([&](auto n) { dma16<0>(n.v < 2 ? rre : rim, lds + wslot<GeoP>(s.v) * 16384 + (lw + 4 * n.v) * 1024, vw, ks * 128 + (n.v & 1) * 32 * SEQ * 2); });
  }
  template <class S> __device__ __forceinline__ void X(S s, rsrc_t rx) {
    const int ks = (s.v + k0) & 31;
    static_for<0, 2>([&](auto n) { dma16<SC1>(rx, lds + XB + (s.v % 8) * 8192 + n.v * 4096 + lw * 1024, vx, (2 * ks + n.v) * 64 * 512); });
  }
  __device__ __forceinline__ rsrc_t xr(const u16* xn, int et) { return mk_rsrc((const char*)xn + (8 * et + 2 * lw) * 512, SEQ * DM * 2 - (8 * et + 2 * lw) * 512); }
};
struct YLoader {
  rsrc_t rre, rim; unsigned vw, vx; lds_t lds; int lw;
  __device__ __forceinline__ YLoader(lds_t lds_, const Ptrs& p, int et, int lw_, int lane) : lds(lds_), lw(lw_) {
    rre = mk_rsrc(p.dre + (size_t)et * 64 * DM, 64 * DM * 2); rim = mk_rsrc(p.dim + (size_t)et * 64 * DM, 64 * DM * 2);
    vw = km_src(lw, lane, DM * 2); vx = lane * 16;
  }
  template <class S> __device__ __forceinline__ void W(S s) {
    static_for<0, 4>([&](auto i) {
      constexpr int kb = i.v >> 1, n = i.v & 1;
      dma16<0>(s.v < 4 ? rre : rim, lds + wslot<GeoY>(s.v) * 16384 + kb * 8192 + (lw + 4 * n) * 1024, vw, (2 * (s.v & 3) + kb) * 128 + n * 32 * DM * 2);
    });
  }
  template <class S> __device__ __forceinline__ void X(S s, rsrc_t rx) {
    static_for<0, 4>([&](auto i) {
      constexpr int blk = i.v >> 1, n = i.v & 1;
      dma16<SC1>(rx, lds + XB + (s.v % 4) * 16384 + blk * 8192 + n * 1024 + lw * 2048, vx, blk * 128 * 512 + (16 * s.v + 2 * n) * 512);
    });
  }
  __device__ __forceinline__ rsrc_t xr(const u16* pq, int tb) { return mk_rsrc((const char*)pq + ((size_t)2 * tb * 128 + 4 * lw) * 512, 2 * 128 * 512 - 4 * lw * 512); }
};
constexpr int C_WS = 28672;
struct CLoader {
  const char* wsrc[7]; unsigned vx; lds_t lds; int lw;
  __device__ __forceinline__ CLoader(lds_t lds_, const Ptrs& p, int fc, int lw_, int lane) : lds(lds_), lw(lw_) {
    vx = lane * 16;
#pragma unroll
    for (int n = 0; n < 7; ++n) {
      int idx = 64 * n + lane, kr = idx / 28, c = idx - kr * 28;
      const u16* base = c < 14 ? p.wg : p.wu;
      int cc = c < 14 ? c : c - 14;
      wsrc[n] = (const char*)(base + (size_t)(16 * lw + kr) * FF + 112 * fc + 8 * cc);
    }
  }
  template <class S> __device__ __forceinline__ void W(S s) {
    static_for<0, 7>([&](auto n) {
      __builtin_amdgcn_global_load_lds((const unsigned*)(wsrc[n.v] + (size_t)s.v * 64 * FF * 2), (LAS unsigned*)(lds + wslot<GeoC>(s.v) * C_WS + lw * 7168 + n.v * 1024), 16, 0, 0);
    });
  }
  template <class S> __device__ __forceinline__ void X(S s, rsrc_t rx) {
    static_for<0, 4>([&](auto n) { dma16<SC1>(rx, lds + XB + (s.v % 4) * 16384 + lw * 4096 + n.v * 1024, vx, (8 * s.v + 2 * n.v) * 512); });
  }
  __device__ __forceinline__ rsrc_t xr(const u16* xa, int rb) { return mk_rsrc((const char*)xa + (size_t)(4 * rb + lw) * 64 * 512, 64 * 512); }
};
struct DLoader {
  rsrc_t rw; unsigned vw, vx; lds_t lds; int lw, k0;
  __device__ __forceinline__ DLoader(lds_t lds_, const Ptrs& p, int tb, int et, int lw_, int lane) : lds(lds_), lw(lw_), k0((tb * 3) % 14) {
    rw = mk_rsrc(p.wd + et * 64, FF * DM * 2 - et * 128);
    vw = kn_src(lw, lane, DM * 2); vx = lane * 16;
  }
  template <class S> __device__ __forceinline__ void W(S s) {
    const int ks = s.v + k0 < 14 ? s.v + k0 : s.v + k0 - 14;
    static_for<0, 4>([&](auto i) {
      constexpr int kb = i.v >> 1, n = i.v & 1;
      dma16<0>(rw, lds + wslot<GeoD>(s.v) * 16384 + kb * 8192 + (lw + 4 * n) * 1024, vw, ((2 * ks + kb) * 64 + 32 * n) * DM * 2);
    });
  }
  template <class S> __device__ __forceinline__ void X(S s, rsrc_t rx) {
    const int ks = s.v + k0 < 14 ? s.v + k0 : s.v + k0 - 14;
    static_for<0, 4>([&](auto i) {
      constexpr int blk = i.v >> 1, n = i.v & 1;
      dma16<SC1>(rx, lds + XB + (s.v % 4) * 16384 + blk * 8192 + n * 1024 + lw * 2048, vx, blk * 224 * 512 + (16 * ks + 2 * n) * 512);
    });
  }
  __device__ __forceinline__ rsrc_t xr(const u16* hh, int tb) { return mk_rsrc((const char*)hh + ((size_t)2 * tb * 224 + 4 * lw) * 512, 2 * 224 * 512 - 4 * lw * 512); }
};
template <class G, class MINE, class LD> __device__ __forceinline__ void l_seam(LD& L) {
  bar(); bar();
  static_for<0, G::NST>([&](auto j) { if constexpr (MINE::at(j.v) == GAP) L.W(j); });
  bar();
}
template <class G, class MINE, class GN, class NEXT, class LD, class LN>
__device__ __forceinline__ void l_phase(LD& L, rsrc_t rx, LN& N) {
  l_run<G, MINE, GN, NEXT>([&](auto s) { L.X(s, rx); }, [&](auto s) { L.W(s); }, [&](auto s) { N.W(s); });
}

__device__ __forceinline__ void p_compute(lds_t lds, u16* pq, int tb, int et, int w, int lane, const char* t0, const char* t1, bool touch, unsigned& dummy) {
  const int r = lane & 31, h = lane >> 5;
  const unsigned a0 = km_read_base(32 * w + r, h);
  const int bl = (lane >> 4) & 1, q4 = (lane & 15) >> 2, pp = ((lane & 1) << 1) | ((lane >> 1) & 1), cc0 = 2 * bl + (pp >> 1);
  const unsigned f0 = cc0 * 512 + (((8 * h + q4) ^ (4 * cc0)) << 4) + 8 * (pp & 1), f1 = f0 ^ 64;
  f32x16 acc0 = {}, acc1 = {};
  unsigned wo = GeoP::OW * 16384, xo = XB;
  struct Fr { bf16x8 tok[4], fa[4], fb[4]; };
  Fr A, B;
  auto rdt = [&](Fr& f, int k) { f.tok[k] = ld_b128(lds + wo + (a0 ^ (k << 5))); };
  auto rda = [&](Fr& f, int k) { lds_t xb = lds + xo + (k >> 1) * 4096 + (k & 1) * 256; f.fa[k] = ld_tr(xb + f0, xb + f1); };
  auto rdb = [&](Fr& f, int k) { lds_t xb = lds + xo + (k >> 1) * 4096 + (k & 1) * 256 + 2048; f.fb[k] = ld_tr(xb + f0, xb + f1); };
  auto adv = [&]() {
    wo = wo + 16384 == 6 * 16384 ? 0 : wo + 16384;
    xo = xo + 8192 == LDS_BYTES ? XB : xo + 8192;
  };
  auto step = [&](Fr& nx, const Fr& cu) {
    cbar();
#pragma unroll
    for (int k = 0; k < 4; ++k) {
      rdt(nx, k); rda(nx, k); SCHED();
      acc0 = mfma(cu.fa[k], cu.tok[k], acc0); SCHED();
      rdb(nx, k); SCHED();
      acc1 = mfma(cu.fb[k], cu.tok[k], acc1); SCHED();
    }
    adv();
  };
  cbar();
#pragma unroll
  for (int k = 0; k < 4; ++k) { rdt(A, k); rda(A, k); rdb(A, k); }
  adv();
#pragma unroll 1
  for (int s = 1; s < 31; s += 2) {
    step(B, A);
    if (touch && s == TOUCH_AT) {
      asm volatile("global_load_dword %0, %1, off" : "+v"(dummy) : "v"(t0) : "memory");
      asm volatile("global_load_dword %0, %1, off" : "+v"(dummy) : "v"(t1) : "memory");
    }
    step(A, B);
  }
  step(B, A);
  cbar();
#pragma unroll
  for (int k = 0; k < 4; ++k) { acc0 = mfma(B.fa[k], B.tok[k], acc0); acc1 = mfma(B.fb[k], B.tok[k], acc1); }
  const float sg = w >= 2 ? -1.f : 1.f;
  const rsrc_t ro = mk_rsrc((char*)pq + ((size_t)(2 * tb + (w & 1)) * 128 + (w >> 1) * 64 + 8 * et) * 512, 4096);
#pragma unroll
  for (int ft = 0; ft < 2; ++ft)
#pragma unroll
    for (int q = 0; q < 2; ++q) {
      const f32x16& a = ft ? acc1 : acc0;
      u32x4 v = {pk2(sg * a[8 * q], sg * a[8 * q + 1]), pk2(sg * a[8 * q + 2], sg * a[8 * q + 3]), pk2(sg * a[8 * q + 4], sg * a[8 * q + 5]), pk2(sg * a[8 * q + 6], sg * a[8 * q + 7])};
      __builtin_amdgcn_raw_buffer_store_b128(v, ro, lane * 16 + ft * 2048 + q * 1024, 0, SC1);
    }
}

__device__ __forceinline__ void add_res(const f32x16& acc, u32x4& r0, u32x4& r1) {
#pragma unroll
  for (int i = 0; i < 4; ++i) {
    r0[i] = pk2(acc[2 * i] + bflo(r0[i]), acc[2 * i + 1] + bfhi(r0[i]));
    r1[i] = pk2(acc[8 + 2 * i] + bflo(r1[i]), acc[8 + 2 * i + 1] + bfhi(r1[i]));
  }
}
__device__ __forceinline__ void store_rowmajor(u16* xo, const u32x4& o0, const u32x4& o1, int row0, int col0, int lane) {
  const rsrc_t ro = mk_rsrc(xo + (size_t)row0 * DM + col0, 32 * DM * 2);
  const unsigned vo = (lane & 31) * DM * 2 + (lane >> 5) * 16;
  __builtin_amdgcn_raw_buffer_store_b128(o0, ro, vo, 0, 0);
  __builtin_amdgcn_raw_buffer_store_b128(o1, ro, vo + 32, 0, 0);
}
__device__ __forceinline__ void store_blocked(u16* xo, const u32x4& o0, const u32x4& o1, int row0, int col0, int lane) {
  const rsrc_t ro = mk_rsrc((char*)xo + ((size_t)(row0 >> 5) * 64 + (col0 >> 3)) * 512, 2048);
  __builtin_amdgcn_raw_buffer_store_b128(o0, ro, lane * 16, 0, SC1);
  __builtin_amdgcn_raw_buffer_store_b128(o1, ro, lane * 16 + 1024, 0, SC1);
}
__device__ __forceinline__ void part_stats(const u32x4& o0, const u32x4& o1, float (&v)[16], float& m, float& m2) {
  float s0 = 0.f, s1 = 0.f;
#pragma unroll
  for (int i = 0; i < 4; ++i) { v[2 * i] = bflo(o0[i]); v[2 * i + 1] = bfhi(o0[i]); v[8 + 2 * i] = bflo(o1[i]); v[8 + 2 * i + 1] = bfhi(o1[i]); }
#pragma unroll
  for (int i = 0; i < 8; ++i) { s0 += v[i]; s1 += v[8 + i]; }
  m = xhalf_sum(s0 + s1) * (1.f / 32.f);
  float q0 = 0.f, q1 = 0.f;
#pragma unroll
  for (int i = 0; i < 8; ++i) { float d = v[i] - m, e = v[8 + i] - m; q0 += d * d; q1 += e * e; }
  m2 = xhalf_sum(q0 + q1);
}
__device__ __forceinline__ void store_stats(float* stats, float m, float m2, int row0, int col0, int lane) {
  const rsrc_t rs = mk_rsrc(stats + ((size_t)(col0 >> 5) * SEQ + row0) * 2, 32 * 8);
  if (lane < 32) { u32x2 sv = {__float_as_uint(m), __float_as_uint(m2)}; __builtin_amdgcn_raw_buffer_store_b64(sv, rs, lane * 8, 0, SC1); }
}
__device__ __forceinline__ void load_stats(const float* stats, int row0, int lane, u32x2 (&t)[8]) {
  gu64* q = (gu64*)stats + row0 + (lane & 31) + (size_t)(8 * (lane >> 5)) * SEQ;
#pragma unroll
  for (int i = 0; i < 8; ++i) { unsigned long long x = __hip_atomic_load(q + i * SEQ, RLX_AGENT); t[i] = u32x2{(unsigned)x, (unsigned)(x >> 32)}; }
}
__device__ __forceinline__ void combine_stats(const u32x2 (&t)[8], float& mean, float& rstd) {
  float ms = 0.f, m2 = 0.f;
#pragma unroll
  for (int i = 0; i < 8; ++i) { ms += __uint_as_float(t[i][0]); m2 += __uint_as_float(t[i][1]); }
  mean = xhalf_sum(ms) * (1.f / 16.f);
#pragma unroll
  for (int i = 0; i < 8; ++i) { float d = __uint_as_float(t[i][0]) - mean; m2 += 32.f * d * d; }
  rstd = rsqrtf(xhalf_sum(m2) * (1.f / 512.f) + 1e-5f);
}

template <class G, class FF_> __device__ __forceinline__ f32x16 tile64_loop(lds_t lds, unsigned a0, FF_ feat, int w, int lane) {
  f32x16 acc = {};
  unsigned wo = G::OW * 16384, xo = XB;
  struct Fr { bf16x8 tok[8], f[8]; };
  Fr A, B;
  auto rd = [&](Fr& fr, int k) {
    fr.tok[k] = ld_b128(lds + xo + a0 + k * 1024);
    fr.f[k] = feat(lds + wo, k);
  };
  auto adv = [&]() {
    wo = wo + 16384 == 6 * 16384 ? 0 : wo + 16384;
    xo = xo + 16384 == LDS_BYTES ? XB : xo + 16384;
  };
  auto step = [&](Fr& nx, const Fr& cu) {
    cbar();
#pragma unroll
    for (int k = 0; k < 8; ++k) { rd(nx, k); SCHED(); acc = mfma(cu.f[k], cu.tok[k], acc); SCHED(); }
    adv();
  };
  cbar();
#pragma unroll
  for (int k = 0; k < 8; ++k) rd(A, k);
  adv();
#pragma unroll 1
  for (int s = 1; s < G::NST - 1; s += 2) { step(B, A); step(A, B); }
  step(B, A);
  cbar();
#pragma unroll
  for (int k = 0; k < 8; ++k) acc = mfma(B.f[k], B.tok[k], acc);
  return acc;
}

__device__ __forceinline__ void c_compute(lds_t lds, const float* stats, const unsigned* csf, u16* hh, int rb, int fc, int w, int lane) {
  const int r = lane & 31, h = lane >> 5;
  const unsigned a0 = w * 4096 + lane * 16;
  const int blk = (lane >> 4) & 1, q = (lane & 15) >> 2, pp = ((lane & 1) << 1) | ((lane >> 1) & 1);
  const unsigned f0 = (8 * h + q) * 448 + blk * 224 + 8 * pp;
  f32x16 acc[7];
#pragma unroll
  for (int j = 0; j < 7; ++j) acc[j] = f32x16{};
  unsigned wo = GeoC::OW * C_WS, xo = XB;
  struct Fr { bf16x8 tok, f[7]; };
  Fr A, B;
  auto rdt = [&](Fr& fr, int k) { fr.tok = ld_b128(lds + xo + a0 + k * 1024); };
  auto rdf = [&](Fr& fr, int k, int j) { lds_t wb = lds + wo + f0; fr.f[j] = ld_tr(wb + 16 * k * 448 + 32 * j, wb + (16 * k + 4) * 448 + 32 * j); };
  auto grp = [&](Fr& nx, const Fr& cu, int k, bool rdn) {
#pragma unroll
    for (int j = 0; j < 7; ++j) {
      if (rdn) { if (j == 0) rdt(nx, k); rdf(nx, k, j); }
      SCHED();
      acc[j] = mfma(cu.f[j], cu.tok, acc[j]);
      SCHED();
    }
  };
  auto adv = [&]() {
    wo = wo + C_WS == 3 * C_WS ? 0 : wo + C_WS;
    xo = xo + 16384 == LDS_BYTES ? XB : xo + 16384;
  };
  cbar();
  u32x2 st[8];
  load_stats(stats, 128 * rb + 32 * w, lane, st);
  unsigned long long cs[7];
#pragma unroll
  for (int j = 0; j < 7; ++j) cs[j] = __hip_atomic_load((gu64*)csf + ((size_t)fc * 32 + r) * 8 + j, __ATOMIC_RELAXED, __HIP_MEMORY_SCOPE_WORKGROUP);
  rdt(A, 0);
#pragma unroll
  for (int j = 0; j < 7; ++j) rdf(A, 0, j);
  auto stage = [&]() {
    grp(B, A, 1, true);
    grp(A, B, 2, true);
    grp(B, A, 3, true);
    adv();
    cbar();
    grp(A, B, 0, true);
  };
  stage();
  float mean, rstd;
  combine_stats(st, mean, rstd);
  {
    const unsigned mh = pk2(-mean, 0.f) & 0xffffu;
    const unsigned ml = pk2(-mean - bflo(mh), 0.f) & 0xffffu;
    u32x4 bt = {h ? 0u : (mh | (ml << 16)), h ? 0u : mh, 0u, 0u};
#pragma unroll
    for (int j = 0; j < 7; ++j) {
      u32x4 at = {h ? 0u : (unsigned)cs[j], h ? 0u : (unsigned)(cs[j] >> 32), 0u, 0u};
      acc[j] = mfma(__builtin_bit_cast(bf16x8, at), __builtin_bit_cast(bf16x8, bt), acc[j]);
    }
  }
  SCHED();
#pragma unroll 1
  for (int s = 1; s < 7; ++s) stage();
  grp(B, A, 1, true);
  grp(A, B, 2, true);
  grp(B, A, 3, true);
  cbar();
  grp(A, B, 0, false);
  const rsrc_t ro = mk_rsrc((char*)hh + ((size_t)(4 * rb + w) * 224 + 14 * fc) * 512, 7168);
  const float ng = -1.4426950408889634f * rstd;
#pragma unroll
  for (int j = 0; j < 7; ++j) {
    float o[8];
#pragma unroll
    for (int i = 0; i < 8; i += 2) {
      f32x2 g = {acc[j][i], acc[j][i + 1]}, u = {acc[j][8 + i], acc[j][9 + i]};
      g *= ng; u *= rstd;
      f32x2 e = {__builtin_amdgcn_exp2f(g[0]), __builtin_amdgcn_exp2f(g[1])};
      e += 1.f;
      f32x2 sg = {__builtin_amdgcn_rcpf(e[0]), __builtin_amdgcn_rcpf(e[1])};
      u *= sg;
      o[i] = u[0]; o[i + 1] = u[1];
    }
    u32x4 v = {pk2(o[0], o[1]), pk2(o[2], o[3]), pk2(o[4], o[5]), pk2(o[6], o[7])};
    __builtin_amdgcn_raw_buffer_store_b128(v, ro, lane * 16 + 1024 * j, 0, SC1);
    SCHED();
  }
}

__global__ __launch_bounds__(512, 2) __attribute__((amdgpu_waves_per_eu(2, 2))) void k_main(Ptrs p) {
  extern __shared__ __attribute__((aligned(16))) char smem[];
  lds_t lds = (lds_t)smem;
  const int lane = threadIdx.x & 63, wid = __builtin_amdgcn_readfirstlane(threadIdx.x >> 6);
  const int xcd = blockIdx.x & 7, j = blockIdx.x >> 3;
  const int tb = xcd * 4 + (j & 3), et = j >> 2;
  const int rb = (xcd & 1) * 8 + (j & 7), fc = (xcd >> 1) * 4 + (j >> 3);
  if (wid >= 4) {
    {
      PLoader L(lds, p, fresh_s(tb), fresh_s(et), fresh_s(wid - 4), fresh_v(lane), ROT1);
      YLoader N(lds, p, fresh_s(et), fresh_s(wid - 4), fresh_v(lane));
      l_phase<GeoP, EarlyNone, GeoY, EarlyY>(L, L.xr(p.xn, et), N);
    }
#pragma unroll 1
    for (int l = 0; l < 2; ++l) {
      {
        YLoader L(lds, p, fresh_s(et), fresh_s(wid - 4), fresh_v(lane));
        CLoader N(lds, p, fresh_s(fc), fresh_s(wid - 4), fresh_v(lane));
        const rsrc_t rx = L.xr(p.pq + l * PQ_L, fresh_s(tb));
        l_seam<GeoY, EarlyY>(L);
        l_phase<GeoY, EarlyY, GeoC, EarlyC>(L, rx, N);
      }
      {
        CLoader L(lds, p, fresh_s(fc), fresh_s(wid - 4), fresh_v(lane));
        DLoader N(lds, p, fresh_s(tb), fresh_s(et), fresh_s(wid - 4), fresh_v(lane));
        const rsrc_t rx = L.xr(p.xa + l * XN_L, fresh_s(rb));
        l_seam<GeoC, EarlyC>(L);
        l_phase<GeoC, EarlyC, GeoD, EarlyD>(L, rx, N);
      }
      if (l == 0) {
        {
          DLoader L(lds, p, fresh_s(tb), fresh_s(et), fresh_s(wid - 4), fresh_v(lane));
          PLoader N(lds, p, fresh_s(tb), fresh_s(et), fresh_s(wid - 4), fresh_v(lane), 0);
          const rsrc_t rx = L.xr(p.hh, fresh_s(tb));
          l_seam<GeoD, EarlyD>(L);
          l_phase<GeoD, EarlyD, GeoP, EarlyP>(L, rx, N);
        }
        PLoader L(lds, p, fresh_s(tb), fresh_s(et), fresh_s(wid - 4), fresh_v(lane), 0);
        YLoader N(lds, p, fresh_s(et), fresh_s(wid - 4), fresh_v(lane));
        const rsrc_t rx = L.xr(p.xn + XN_L, fresh_s(et));
        l_seam<GeoP, EarlyP>(L);
        l_phase<GeoP, EarlyP, GeoY, EarlyY>(L, rx, N);
      } else {
        DLoader L(lds, p, fresh_s(tb), fresh_s(et), fresh_s(wid - 4), fresh_v(lane));
        const rsrc_t rx = L.xr(p.hh + HH_L, fresh_s(tb));
        l_seam<GeoD, EarlyD>(L);
        l_phase<GeoD, EarlyD, GeoP, EarlyNone>(L, rx, L);
      }
    }
  } else {
    const int w = wid, tw = w & 1, fw = w >> 1;
    const int row0 = 64 * tb + 32 * tw, col0 = 64 * et + 32 * fw;
    unsigned dummy = 0;
    u32x4 r0, r1;
    {
      const int r = lane & 31, h = lane >> 5;
      const rsrc_t rr = mk_rsrc(p.x0 + (size_t)row0 * DM + col0, 32 * DM * 2);
      r0 = __builtin_amdgcn_raw_buffer_load_b128(rr, r * DM * 2 + h * 16, 0, 0);
      r1 = __builtin_amdgcn_raw_buffer_load_b128(rr, r * DM * 2 + h * 16 + 32, 0, 0);
    }
#pragma unroll 1
    for (int l = 0; l < 2; ++l) {
      const int sb = 4 * l;
      {
        const int ln = fresh_v(lane), li = blockIdx.x * 64 + ln, lj = blockIdx.x * 32 + (ln & 31);
        const char* t0 = li < 14336 ? (const char*)p.wd + (size_t)li * 128 : (const char*)p.wd;
        const char* t1 = (ln < 32 ? (const char*)p.dre : (const char*)p.dim) + (size_t)(lj & 4095) * 128;
        p_compute(lds, p.pq + l * PQ_L, fresh_s(tb), fresh_s(et), fresh_s(w), fresh_v(lane), t0, t1, l == 0 && w == 0 && blockIdx.x < 224, dummy);
        WAIT_V(0); bar();
        asm volatile("" :: "v"(dummy));
        if (w == 0) signal(p.mbox, sb + 0, tb * 8 + (lane & 7), et, lane < 8);
        bar();
        if (w == 0) await(p.mbox, p.tmo, sb + 0, tb * 8 + et, 8, lane);
        bar();
      }
      {
        const int ln = fresh_v(lane), r = ln & 31, h = ln >> 5;
        const unsigned a0 = tw * 8192 + ln * 16, b0 = km_read_base(32 * fw + perm23(r), h);
        f32x16 acc = tile64_loop<GeoY>(lds, a0, [&](lds_t wb, int k) { return ld_b128(wb + (k >> 2) * 8192 + (b0 ^ ((k & 3) << 5))); }, w, lane);
        add_res(acc, r0, r1);
        store_blocked(p.xa + l * XN_L, r0, r1, row0, col0, fresh_v(lane));
        float v[16], m, m2;
        part_stats(r0, r1, v, m, m2);
        store_stats(p.stats + l * ST_L, m, m2, row0, col0, fresh_v(lane));
        WAIT_V(0); bar();
        if (w == 0) signal(p.mbox, sb + 1, (tb >> 1) * 16 + (lane & 15), (tb & 1) * 8 + et, lane < 16);
        bar();
        if (w == 0) await(p.mbox, p.tmo, sb + 1, rb * 16 + fc, 16, lane);
        bar();
      }
      {
        c_compute(lds, p.stats + l * ST_L, p.csf, p.hh + l * HH_L, fresh_s(rb), fresh_s(fc), fresh_s(w), fresh_v(lane));
        WAIT_V(0); bar();
        if (w == 0) signal(p.mbox, sb + 2, (2 * rb + ((lane >> 3) & 1)) * 8 + (lane & 7), fc, lane < 16);
        bar();
        if (w == 0) await(p.mbox, p.tmo, sb + 2, tb * 8 + et, 16, lane);
        bar();
      }
      {
        const int ln = fresh_v(lane);
        const unsigned a0 = tw * 8192 + ln * 16, f0 = kn_read_base(ln, fw);
        f32x16 acc = tile64_loop<GeoD>(lds, a0, [&](lds_t wb, int k) { return ld_tr(wb + f0 + k * 2048, wb + f0 + k * 2048 + 512); }, w, lane);
        add_res(acc, r0, r1);
        if (l == 1) {
          store_rowmajor(p.out, r0, r1, row0, col0, fresh_v(lane));
        } else {
          float v[16], m, m2;
          part_stats(r0, r1, v, m, m2);
          const int le = fresh_v(lane);
          store_stats((float*)p.gran, m, m2, row0, col0, le);
          u32x2 st[8];
          for (unsigned spins = 0;; ++spins) {
            load_stats((const float*)p.gran, row0, le, st);
            bool ok = true;
#pragma unroll
            for (int i = 0; i < 8; ++i) ok = ok && st[i][1] != 0xffffffffu;
            if (__all(ok)) break;
            if (spins > SPIN_CAP) { if (lane == 0) __hip_atomic_store((gu32*)p.tmo, 2u, RLX_AGENT); break; }
            __builtin_amdgcn_s_sleep(1);
          }
          float mean, rstd;
          combine_stats(st, mean, rstd);
          u32x4 n0, n1;
#pragma unroll
          for (int i = 0; i < 4; ++i) {
            n0[i] = pk2((v[2 * i] - mean) * rstd, (v[2 * i + 1] - mean) * rstd);
            n1[i] = pk2((v[8 + 2 * i] - mean) * rstd, (v[8 + 2 * i + 1] - mean) * rstd);
          }
          store_blocked(p.xn + XN_L, n0, n1, row0, col0, le);
          WAIT_V(0); bar();
          if (w == 0) signal(p.mbox, 3, (lane & 31) * 8 + et, tb, lane < 32);
          bar();
          if (w == 0) await(p.mbox, p.tmo, 3, tb * 8 + et, 32, lane);
          bar();
        }
      }
    }
  }
}

__device__ __forceinline__ float wave_sum(float v) {
#pragma unroll
  for (int o = 1; o < 64; o <<= 1) v += __shfl_xor(v, o);
  return v;
}
__global__ __launch_bounds__(512) void k_pre(Ptrs p) {
  const int lane = threadIdx.x & 63, wid = threadIdx.x >> 6;
  if (blockIdx.x < 256) {
    const int row = blockIdx.x * 8 + wid;
    u32x4 t = *(const u32x4*)(p.x0 + (size_t)row * DM + lane * 8);
    const int gid = blockIdx.x * 512 + threadIdx.x;
    if (gid < 7 * 256 * 8) ((u32x4*)p.mbox)[gid] = u32x4{0u, 0u, 0u, 0u};
    if (gid < 16 * SEQ / 2) ((u32x4*)p.gran)[gid] = u32x4{~0u, ~0u, ~0u, ~0u};
    if (gid == 0) *p.tmo = 0u;
    float v[8], s = 0.f;
#pragma unroll
    for (int i = 0; i < 4; ++i) { v[2 * i] = bflo(t[i]); v[2 * i + 1] = bfhi(t[i]); s += v[2 * i] + v[2 * i + 1]; }
    const float m = wave_sum(s) * (1.f / 512.f);
    float m2 = 0.f;
#pragma unroll
    for (int i = 0; i < 8; ++i) { v[i] -= m; m2 += v[i] * v[i]; }
    const float rs = rsqrtf(wave_sum(m2) * (1.f / 512.f) + 1e-5f);
    u32x4 o = {pk2(v[0] * rs, v[1] * rs), pk2(v[2] * rs, v[3] * rs), pk2(v[4] * rs, v[5] * rs), pk2(v[6] * rs, v[7] * rs)};
    *(u32x4*)((char*)p.xn + ((size_t)(row >> 5) * 64 + lane) * 512 + (row & 31) * 16) = o;
  } else if (blockIdx.x >= 312) {
    const int q = blockIdx.x - 312, xcd = q & 7, i = q >> 3, tb = xcd * 4 + (i & 3);
    const int r = 32 * (i >> 2) + 4 * wid + (lane >> 4);
    const char* a = (const char*)(r < 64 ? p.sre : p.sim) + (size_t)(64 * tb + (r & 63)) * (SEQ * 2) + (lane & 15) * 128;
    unsigned d0, d1;
    asm volatile("global_load_dword %0, %1, off" : "=v"(d0) : "v"(a) : "memory");
    asm volatile("global_load_dword %0, %1, off offset:2048" : "=v"(d1) : "v"(a) : "memory");
    asm volatile("s_waitcnt vmcnt(0)" : "+v"(d0), "+v"(d1) :: "memory");
  } else {
    __shared__ float red[8][64];
    const int b = blockIdx.x - 256;
    const u16* w = (b < 28 ? p.wg : p.wu) + (b % 28) * 64 + (lane & 7) * 8;
    float a[8] = {};
#pragma unroll
    for (int i = 0; i < 8; ++i) {
      u32x4 t = *(const u32x4*)(w + (size_t)(i * 64 + wid * 8 + (lane >> 3)) * FF);
#pragma unroll
      for (int e = 0; e < 4; ++e) { a[2 * e] += bflo(t[e]); a[2 * e + 1] += bfhi(t[e]); }
    }
#pragma unroll
    for (int e = 0; e < 8; ++e) { a[e] += __shfl_xor(a[e], 8); a[e] += __shfl_xor(a[e], 16); a[e] += __shfl_xor(a[e], 32); }
    if (lane < 8) {
#pragma unroll
      for (int e = 0; e < 8; ++e) red[wid][lane * 8 + e] = a[e];
    }
    __syncthreads();
    if (threadIdx.x < 64) {
      float s = 0.f;
#pragma unroll
      for (int i = 0; i < 8; ++i) s += red[i][threadIdx.x];
      const int c = (b % 28) * 64 + threadIdx.x, fc = c / 112, rem = c - fc * 112, jj = rem >> 4, i = perm23(rem & 15) + (b < 28 ? 0 : 16);
      const unsigned hi = pk2(s, 0.f) & 0xffffu, lo = pk2(s - bflo(hi), 0.f) & 0xffffu;
      u32x2 o = {hi | (hi << 16), lo};
      *(u32x2*)(p.csf + ((size_t)fc * 32 + i) * 16 + 2 * jj) = o;
    }
  }
}
}

extern "C" void kernel_launch(void* const* d_in, const int* in_sizes, int n_in, void* d_out, int out_size, void* d_ws, size_t ws_size, hipStream_t stream) {
  Ptrs p;
  p.x0 = (const u16*)d_in[0]; p.sre = (const u16*)d_in[1]; p.sim = (const u16*)d_in[2]; p.dre = (const u16*)d_in[3]; p.dim = (const u16*)d_in[4];
  p.wg = (const u16*)d_in[6]; p.wu = (const u16*)d_in[7]; p.wd = (const u16*)d_in[8];
  char* ws = (char*)d_ws;
  const size_t MB = 1 << 20;
  p.out = (u16*)d_out;
  p.xn = (u16*)ws;
  p.pq = (u16*)(ws + 4 * MB);
  p.xa = (u16*)(ws + 12 * MB);
  p.hh = (u16*)(ws + 16 * MB);
  p.stats = (float*)(ws + 31 * MB);
  p.csf = (unsigned*)(ws + 33 * MB);
  p.gran = (unsigned*)(ws + 34 * MB);
  p.mbox = (unsigned*)(ws + 35 * MB);
  p.tmo = (unsigned*)(ws + 36 * MB);
  (void)hipFuncSetAttribute((const void*)k_main, hipFuncAttributeMaxDynamicSharedMemorySize, LDS_BYTES);
  k_pre<<<256 + 56 + 128, 512, 0, stream>>>(p);
  k_main<<<256, 512, LDS_BYTES, stream>>>(p);
}
```
